```python
import torch

def _softmax(x, axis=-1):
    return torch.softmax(x, dim=axis)
def _gelu(x):
    return torch.nn.functional.gelu(x, approximate="tanh")
def _silu(x):
    return torch.nn.functional.silu(x)
def _sigmoid(x):
    return torch.sigmoid(x)
def _layernorm(x, axis=-1, eps=1e-5):
    m = x.mean(dim=axis, keepdim=True)
    v = x.var(dim=axis, keepdim=True, unbiased=False)
    return (x - m) / torch.sqrt(v + eps)
def _scatter_add(tgt, idx, src, axis=0):
    return tgt.clone().index_add_(axis, idx, src)
def _segment_sum(src, seg, n):
    out = torch.zeros((n,) + src.shape[1:], dtype=src.dtype)
    return out.index_add_(0, seg, src)
def _pad(x, pw):
    flat = tuple(v for pair in reversed(pw) for v in pair)
    return torch.nn.functional.pad(x, flat)

kernel_name = 'parallel_sum__sliding_block__glu_gated_reglu__prompt'

def setup_inputs():

    _w = (lambda a: (lambda t: t.to(torch.bfloat16) if t.is_floating_point() else t)(torch.as_tensor(a)))
    def _A_setup():
        B, S, D = 1, 2048, 512
        return {
            "x": _w((lambda *s: torch.randn(*s, dtype=torch.bfloat16) * 0.05)(B, S, D)),
            "wqkv": _w((lambda *s: torch.randn(*s, dtype=torch.bfloat16) * 0.05)(D, 3 * D)),
            "wo": _w((lambda *s: torch.randn(*s, dtype=torch.bfloat16) * 0.05)(D, D)),
        }
    _A_in = _A_setup()

    def _B_setup():
        B, S, D, F = 1, 2048, 512, 1376
        return {
            "x": _w((lambda *s: torch.randn(*s, dtype=torch.bfloat16) * 0.05)(B, S, D)),
            "W_gate": _w((lambda *s: torch.randn(*s, dtype=torch.bfloat16) * 0.8)(D, F) * (2.0 / D) ** 0.5),
            "W_up": _w((lambda *s: torch.randn(*s, dtype=torch.bfloat16) * 0.8)(D, F) * (2.0 / D) ** 0.5),
            "W_down": _w((lambda *s: torch.randn(*s, dtype=torch.bfloat16) * 0.8)(F, D) * (1.0 / F) ** 0.5),
        }
    _B_in = _B_setup()

    return {
        "A_x": _A_in["x"],
        "A_wqkv": _A_in["wqkv"],
        "A_wo": _A_in["wo"],
        "B_x": _B_in["x"],
        "B_W_gate": _B_in["W_gate"],
        "B_W_up": _B_in["W_up"],
        "B_W_down": _B_in["W_down"],
    }

def reference(A_x, A_wqkv, A_wo, B_x, B_W_gate, B_W_up, B_W_down):
    def _A_ref(x, wqkv, wo):
        B, S, D = x.shape
        H = 32; Dh = D // H; W = 256
        qkv = (lambda x, *d: x.permute(*d))((x @ wqkv).reshape(B, S, 3, H, Dh), 2, 0, 3, 1, 4)
        q, k, v = qkv[0], qkv[1], qkv[2]
        scores = torch.einsum("bhqd,bhkd->bhqk", q, k) * (Dh ** -0.5)
        idx = torch.arange(S)
        mask = (idx[:, None] - idx[None, :] >= 0) & (idx[:, None] - idx[None, :] < W)
        scores = torch.where(mask[None, None], scores, -1e9)
        p = _softmax(scores, axis=-1)
        o = torch.einsum("bhqk,bhkd->bhqd", p, v)
        return (lambda x, *d: x.permute(*d))(o, 0, 2, 1, 3).reshape(B, S, D) @ wo

    def _B_ref(x, W_gate, W_up, W_down):
        gate = torch.einsum("bsd,df->bsf", x, W_gate)
        up = torch.einsum("bsd,df->bsf", x, W_up)
        gate = gate * (lambda x, dt: x.to(dt))(gate > 0, gate.dtype)
        h = gate * up
        return torch.einsum("bsf,fd->bsd", h, W_down)

    x = A_x
    xn = _layernorm(x, axis=-1)
    return x + _A_ref(xn, A_wqkv, A_wo) + _B_ref(xn, B_W_gate, B_W_up, B_W_down)
```

```cpp
#include <hip/hip_runtime.h>
#include <hip/hip_bf16.h>
#include <stdint.h>

#define MK_SPLIT 0

namespace {

typedef short bf16x8 __attribute__((ext_vector_type(8)));
typedef short s16x4 __attribute__((ext_vector_type(4)));
typedef float f32x16 __attribute__((ext_vector_type(16)));
typedef float f32x4 __attribute__((ext_vector_type(4)));
typedef float f32x2 __attribute__((ext_vector_type(2)));
typedef unsigned u32x4 __attribute__((ext_vector_type(4)));
typedef unsigned u32x2 __attribute__((ext_vector_type(2)));
typedef __bf16 bf16x2_t __attribute__((ext_vector_type(2)));
typedef __amdgpu_buffer_rsrc_t rsrc_t;
#define LAS __attribute__((address_space(3)))
#define GAS __attribute__((address_space(1)))
#define DI __device__ __forceinline__
typedef unsigned long long u64;

constexpr int SEQ = 2048, DM = 512, FF = 1376, NU = 86;
constexpr unsigned WS_EPOCH = 0;
constexpr unsigned WS_FQKV = 4096, WS_FH = 8192, WS_FO = 12288;
constexpr unsigned WS_QKV = 1u << 20;
constexpr unsigned WS_H = 7u << 20;
constexpr unsigned WS_O = 13u << 20;
constexpr unsigned WS_BYTES = 16u << 20;
constexpr unsigned SLOT1 = 28672;
constexpr unsigned L_MISC = 5 * SLOT1;
constexpr unsigned L_STATS = L_MISC;
constexpr unsigned L_CSP = L_MISC + 1024;
constexpr unsigned L_CSW = L_MISC + 4096;
constexpr unsigned L_FLAG = L_MISC + 8192;
constexpr unsigned LDS_BYTES = 163840;
constexpr unsigned LA_Q = 0, LA_K = 8192, LA_V = 8192 + 24576, LA_ONES = 8192 + 49152;
constexpr unsigned SLOT3 = 32768;
constexpr unsigned L3_RED = 4 * SLOT3;

__shared__ __attribute__((aligned(1024))) char lds[LDS_BYTES];

DI unsigned pk(float a, float b) { f32x2 v = {a, b}; bf16x2_t r = __builtin_convertvector(v, bf16x2_t); return __builtin_bit_cast(unsigned, r); }
DI float dot2(unsigned a, unsigned b, float c) { return __builtin_amdgcn_fdot2_f32_bf16(__builtin_bit_cast(bf16x2_t, a), __builtin_bit_cast(bf16x2_t, b), c, false); }
DI rsrc_t mk_rsrc(const void* p, unsigned bytes) { return __builtin_amdgcn_make_buffer_rsrc((void*)p, 0, bytes, 0x00020000); }
DI void dma16(unsigned ldsaddr, unsigned voff, rsrc_t rs, unsigned soff) {
  asm volatile("s_mov_b32 m0, %0\n\ts_nop 0\n\tbuffer_load_dwordx4 %1, %2, %3 offen lds" ::"s"(ldsaddr), "v"(voff), "s"(rs), "s"(soff) : "memory");
}
DI void dma16c(unsigned ldsaddr, unsigned voff, rsrc_t rs, unsigned soff) {
  asm volatile("s_mov_b32 m0, %0\n\ts_nop 0\n\tbuffer_load_dwordx4 %1, %2, %3 offen sc1 lds" ::"s"(ldsaddr), "v"(voff), "s"(rs), "s"(soff) : "memory");
}
template <int N> DI void wait_vm() { asm volatile("s_waitcnt vmcnt(%0)" ::"n"(N) : "memory"); }
DI void wait_lgkm0() { asm volatile("s_waitcnt lgkmcnt(0)" ::: "memory"); }
DI void bar() { asm volatile("s_waitcnt lgkmcnt(0)\n\ts_barrier" ::: "memory"); }
DI f32x16 mfma(bf16x8 a, bf16x8 b, f32x16 c) { return __builtin_amdgcn_mfma_f32_32x32x16_bf16(a, b, c, 0, 0, 0); }
DI bf16x8 lds_b128(unsigned a) { return *(const LAS bf16x8*)(lds + a); }
DI f32x4 lds_f4(unsigned a) { return *(const LAS f32x4*)(lds + a); }
typedef short v4i16_t __attribute__((ext_vector_type(4)));
DI s16x4 lds_tr(unsigned a) { return __builtin_amdgcn_ds_read_tr16_b64_v4i16((LAS v4i16_t*)(lds + a)); }
DI bf16x8 cat(s16x4 lo, s16x4 hi) { return __builtin_shufflevector(lo, hi, 0, 1, 2, 3, 4, 5, 6, 7); }
DI float xhalf_sum(float v) { auto r = __builtin_amdgcn_permlane32_swap(__float_as_uint(v), __float_as_uint(v), false, false); return __uint_as_float(r[0]) + __uint_as_float(r[1]); }
DI float xhalf_max(float v) { auto r = __builtin_amdgcn_permlane32_swap(__float_as_uint(v), __float_as_uint(v), false, false); return fmaxf(__uint_as_float(r[0]), __uint_as_float(r[1])); }
DI void st16c(u32x4 v, rsrc_t rs, unsigned voff, unsigned soff) { __builtin_amdgcn_raw_buffer_store_b128(v, rs, voff, soff, 16); }
constexpr unsigned ONES2 = 0x3F803F80u;

struct Ptrs { const void *x, *wqkv, *wo, *wg, *wu, *wd; void* out; char* ws; };

DI void flag_publish(char* ws, unsigned off, u64 tag) { __hip_atomic_store((GAS u64*)(ws + off), tag, __ATOMIC_RELAXED, __HIP_MEMORY_SCOPE_AGENT); }
DI void flag_wait(char* ws, unsigned off, bool active, u64 tag) {
  for (unsigned spins = 0; spins < (1u << 20); ++spins) {
    u64 v = tag;
    if (active) v = __hip_atomic_load((GAS u64*)(ws + off), __ATOMIC_RELAXED, __HIP_MEMORY_SCOPE_AGENT);
    if (__all(v == tag)) break;
    __builtin_amdgcn_s_sleep(1);
  }
}

template <int NJ>
DI void phase1(const Ptrs& P, int rb, int g, int mi, int lane, bool fused, u64 tag) {
  const int w = NJ * 4 + mi;
  const int r = lane & 31, h = lane >> 5, nsub = (lane >> 4) & 1, q4 = (lane & 15) >> 2, p4 = lane & 3;
  const rsrc_t rs_x = mk_rsrc(P.x, SEQ * DM * 2), rs_q = mk_rsrc(P.wqkv, DM * 1536 * 2);
  const rsrc_t rs_f = mk_rsrc(NJ ? P.wu : P.wg, DM * FF * 2), rs_ws = mk_rsrc(P.ws, WS_BYTES);
  const int u0 = (g * NU) >> 4, nu = (((g + 1) * NU) >> 4) - u0;
  unsigned vx[2], vq[2], vf[3];
#pragma unroll
  for (int j = 0; j < 2; ++j) vx[j] = (unsigned)(16 * w + 8 * j + (lane >> 3)) * 1024u + (unsigned)(((lane & 7) ^ (4 * j + (lane >> 4))) * 16);
  constexpr int NQ = NJ ? 2 : 1;
#pragma unroll
  for (int j = 0; j < NQ; ++j) { const int ins = NJ ? 2 * mi + j : 8 + mi; const int G = ins * 64 + lane, kg = G / 48, rr = G % 48;
    vq[j] = (unsigned)(4 * kg + ((rr & 7) >> 1)) * 3072u + (unsigned)((rr >> 3) * 32 + (rr & 1) * 16); }
#pragma unroll
  for (int j = 0; j < 3; ++j) { const int G = (3 * mi + j) * 64 + lane, kg = G / 48, rr = G % 48, unit = (rr >> 3) ^ NJ;
    vf[j] = unit < nu ? (unsigned)(4 * kg + ((rr & 7) >> 1)) * 2752u + (unsigned)(unit * 32 + (rr & 1) * 16) : 0x40000000u; }
  const unsigned sx0 = (unsigned)rb * 131072u, sq0 = (unsigned)g * 192u, sf0 = (unsigned)u0 * 32u;
  auto issue = [&](int gs) {
    const unsigned slot = (unsigned)(gs % 5) * SLOT1;
    if (gs < 8) {
#pragma unroll
      for (int j = 0; j < 2; ++j) dma16(slot + (unsigned)(2 * w + j) * 1024u, vx[j], rs_x, sx0 + gs * 128u);
#pragma unroll
      for (int j = 0; j < NQ; ++j) dma16(slot + 16384u + (unsigned)(NJ ? 2 * mi + j : 8 + mi) * 1024u, vq[j], rs_q, sq0 + gs * 64u * 3072u);
    } else {
#pragma unroll
      for (int j = 0; j < 3; ++j) dma16(slot + NJ * 12288u + (unsigned)(3 * mi + j) * 1024u, vf[j], rs_f, sf0 + (gs - 8) * 64u * 2752u);
    }
  };
  constexpr int NA = 2 + NQ;
  unsigned xa[4];
#pragma unroll
  for (int ks = 0; ks < 4; ++ks) xa[ks] = (unsigned)(32 * mi + r) * 128u + (unsigned)(((2 * ks + h) ^ ((r >> 1) & 7)) * 16);
  const unsigned wa = 16384u + (unsigned)(2 * h) * 768u + nsub * 128u + q4 * 32u + p4 * 8u;
  const unsigned wbE = nsub * 12288u + (unsigned)(2 * h) * 768u + q4 * 32u + p4 * 8u + nsub * 128u;
  const unsigned wbO = nsub * 12288u + (unsigned)(2 * h) * 768u + q4 * 32u + p4 * 8u - nsub * 128u;

#pragma unroll
  for (int s = 0; s < 5; ++s) issue(s);

  bf16x8 xf[32];
  constexpr int NB1 = NJ ? 1 : 2;
  f32x16 acc[3];
  float cs[3] = {0.f, 0.f, 0.f}, sx = 0.f, sxx = 0.f;
#pragma unroll
  for (int b = 0; b < 3; ++b)
#pragma unroll
    for (int i = 0; i < 16; ++i) acc[b][i] = 0.f;
#pragma unroll
  for (int s = 0; s < 8; ++s) {
    if (s == 0) wait_vm<4 * NA>();
    else if (s <= 4) wait_vm<3 * NA>();
    else if (s == 5) wait_vm<2 * NA + 3>();
    else if (s == 6) wait_vm<NA + 6>();
    else wait_vm<9>();
    bar();
    if (s >= 1) issue(s + 4);
    const unsigned slot = (unsigned)(s % 5) * SLOT1;
#pragma unroll
    for (int ks = 0; ks < 4; ++ks) {
      const bf16x8 xv = lds_b128(slot + xa[ks]);
      xf[s * 4 + ks] = xv;
      const u32x4 xu = __builtin_bit_cast(u32x4, xv);
#pragma unroll
      for (int b = 0; b < NB1; ++b) {
        const unsigned a = slot + wa + ks * 3072u + (NJ ? 2 : b) * 256u;
        const bf16x8 wf = cat(lds_tr(a), lds_tr(a + 768u));
        acc[b] = mfma(wf, xv, acc[b]);
        if (ks == mi) { const u32x4 wu = __builtin_bit_cast(u32x4, wf);
          cs[b] = dot2(wu[0], ONES2, cs[b]); cs[b] = dot2(wu[1], ONES2, cs[b]); cs[b] = dot2(wu[2], ONES2, cs[b]); cs[b] = dot2(wu[3], ONES2, cs[b]); }
      }
      if (NJ) {
#pragma unroll
        for (int i = 0; i < 4; ++i) { sx = dot2(xu[i], ONES2, sx); sxx = dot2(xu[i], xu[i], sxx); }
      }
    }
  }
  if (NJ) {
    sx = xhalf_sum(sx); sxx = xhalf_sum(sxx);
    const float mean = sx * (1.f / 512.f), var = fmaxf(sxx * (1.f / 512.f) - mean * mean, 0.f);
    if (lane < 32) *(LAS f32x2*)(lds + L_STATS + (32 * mi + r) * 8) = f32x2{mean, __builtin_amdgcn_rsqf(var + 1e-5f)};
  }
#pragma unroll
  for (int b = 0; b < NB1; ++b) cs[b] = xhalf_sum(cs[b]);
#pragma unroll
  for (int b = 0; b < NB1; ++b) if (lane < 32) *(LAS float*)(lds + L_CSP + (((NJ ? 2 : b) * 32 + r) * 4 + mi) * 4) = cs[b];
  bar();
  const f32x2 st = *(const LAS f32x2*)(lds + L_STATS + (32 * mi + r) * 8);
  const float mean = st[0], rstd = st[1];
  const int sb = rb * 4 + mi;
  {
#pragma unroll
    for (int b = 0; b < NB1; ++b) { const f32x4 c4 = lds_f4(L_CSP + ((NJ ? 2 : b) * 32 + r) * 16);
      if (lane < 32) *(LAS float*)(lds + L_CSW + w * 384 + b * 128 + r * 4) = (c4[0] + c4[1]) + (c4[2] + c4[3]); }
    wait_lgkm0();
#pragma unroll
    for (int b = 0; b < NB1; ++b) {
      const int blk = 3 * g + (NJ ? 2 : b), which = blk >> 4, hp = blk & 15;
      const float sc = which == 0 ? rstd * (0.25f * 1.4426950408889634f) : rstd;
      float v[16];
#pragma unroll
      for (int i = 0; i < 4; ++i) { const f32x4 c4 = lds_f4(L_CSW + w * 384 + b * 128 + (8 * i + 4 * h) * 4);
#pragma unroll
        for (int e = 0; e < 4; ++e) v[4 * i + e] = (acc[b][4 * i + e] - mean * c4[e]) * sc; }
      const unsigned vo = which == 2 ? (unsigned)(h * 512 + ((r ^ (4 * h)) * 16)) : (unsigned)(lane * 16);
#pragma unroll
      for (int hd = 0; hd < 2; ++hd) {
        const u32x4 o = {pk(v[8 * hd], v[8 * hd + 1]), pk(v[8 * hd + 2], v[8 * hd + 3]), pk(v[8 * hd + 4], v[8 * hd + 5]), pk(v[8 * hd + 6], v[8 * hd + 7])};
        st16c(o, rs_ws, vo, WS_QKV + (unsigned)((which * 32 + 2 * hp + hd) * 64 + sb) * 1024u);
      }
    }
  }
  constexpr int NST = 2 * NB1;
#pragma unroll
  for (int b = 0; b < 3; ++b) { cs[b] = 0.f;
#pragma unroll
    for (int i = 0; i < 16; ++i) acc[b][i] = 0.f; }
  const int nb3 = NJ ? nu - 3 : 3;
#pragma unroll
  for (int s = 0; s < 8; ++s) {
    const int gs = 8 + s;
    if (s == 0) wait_vm<9 + NST>();
    else if (s == 1) wait_vm<6 + NST + 3>();
    else if (s == 2) wait_vm<3 + NST + 6>();
    else if (s == 3) wait_vm<NST + 9>();
    else if (s == 4) wait_vm<9>();
    else if (s == 5) wait_vm<6>();
    else if (s == 6) wait_vm<3>();
    else wait_vm<0>();
    bar();
    if (gs + 4 <= 15) issue(gs + 4);
    if (s == 4 && fused && threadIdx.x == 0) flag_publish(P.ws, WS_FQKV + (unsigned)(rb * 16 + g) * 8u, tag);
    const unsigned slot = (unsigned)(gs % 5) * SLOT1;
#pragma unroll
    for (int ks = 0; ks < 4; ++ks) {
#pragma unroll
      for (int b = 0; b < 3; ++b) {
        if (b < 2 || nb3 == 3) {
          const int u = NJ * 3 + b;
          const unsigned a = slot + ((u & 1) ? wbO : wbE) + ks * 3072u + u * 128u;
          const bf16x8 wf = cat(lds_tr(a), lds_tr(a + 768u));
          acc[b] = mfma(wf, xf[s * 4 + ks], acc[b]);
          if (ks == mi) { const u32x4 wu = __builtin_bit_cast(u32x4, wf);
            cs[b] = dot2(wu[0], ONES2, cs[b]); cs[b] = dot2(wu[1], ONES2, cs[b]); cs[b] = dot2(wu[2], ONES2, cs[b]); cs[b] = dot2(wu[3], ONES2, cs[b]); }
        }
      }
    }
  }
#pragma unroll
  for (int b = 0; b < 3; ++b) { cs[b] = xhalf_sum(cs[b]); if (lane < 32) *(LAS float*)(lds + L_CSP + (((NJ * 3 + b) * 32 + r) * 4 + mi) * 4) = cs[b]; }
  bar();
#pragma unroll
  for (int b = 0; b < 3; ++b) { const f32x4 c4 = lds_f4(L_CSP + ((NJ * 3 + b) * 32 + r) * 16);
    if (lane < 32) *(LAS float*)(lds + L_CSW + w * 384 + b * 128 + r * 4) = (c4[0] + c4[1]) + (c4[2] + c4[3]); }
  wait_lgkm0();
  const float r2 = rstd * rstd;
#pragma unroll
  for (int b = 0; b < 3; ++b) {
    if (b < 2 || nb3 == 3) {
      float v[16];
#pragma unroll
      for (int i = 0; i < 4; ++i) { const f32x4 c4 = lds_f4(L_CSW + w * 384 + b * 128 + (8 * i + 4 * h) * 4);
#pragma unroll
        for (int e = 0; e < 4; ++e) v[4 * i + e] = acc[b][4 * i + e] - mean * c4[e]; }
      float hv[8];
#pragma unroll
      for (int i = 0; i < 8; ++i) hv[i] = fmaxf(v[i], 0.f) * v[8 + i] * r2;
      const u32x4 o = {pk(hv[0], hv[1]), pk(hv[2], hv[3]), pk(hv[4], hv[5]), pk(hv[6], hv[7])};
      st16c(o, rs_ws, (unsigned)lane * 16u, WS_H + (unsigned)(sb * NU + u0 + NJ * 3 + b) * 1024u);
    }
  }
  wait_vm<0>();
  bar();
  if (fused && threadIdx.x == 0) flag_publish(P.ws, WS_FH + (unsigned)(rb * 16 + g) * 8u, tag);
}

DI void phase2(const Ptrs& P, int rb, int hp, int w, int lane, bool fused, u64 tag) {
  const rsrc_t rs_ws = mk_rsrc(P.ws, WS_BYTES);
  if (fused) {
    if (w == 0) {
      const int kind = lane == 0 ? 0 : (lane <= 3 ? 1 : 2), d = lane == 0 ? 0 : (lane - 1) % 3;
      const int rr = rb - d;
      flag_wait(P.ws, WS_FQKV + (unsigned)((rr < 0 ? 0 : rr) * 16 + (kind * 16 + hp) / 3) * 8u, lane < 7 && rr >= 0, tag);
    }
    bar();
  }
#pragma unroll
  for (int j = 0; j < 7; ++j) {
    const int t = w * 7 + j;
    int which, hd, sbk;
    if (t < 8) { which = 0; hd = t >> 2; sbk = 4 * rb + (t & 3); }
    else { const int tt = t - 8; which = 1 + tt / 24; hd = (tt % 24) / 12; sbk = 4 * rb - 8 + tt % 12; }
    if (sbk < 0) sbk = 0;
    dma16c((unsigned)t * 1024u, (unsigned)lane * 16u, rs_ws, WS_QKV + (unsigned)((which * 32 + 2 * hp + hd) * 64 + sbk) * 1024u);
  }
  if (threadIdx.x < 64) *(LAS unsigned*)(lds + LA_ONES + threadIdx.x * 4) = ONES2;
  wait_vm<0>();
  bar();
  const int qb = w & 3, hd = w >> 2, sb = 4 * rb + qb;
  const int q = lane & 31, hh = lane >> 5;
  const int j0 = sb >= 8 ? 0 : 8 - sb;
  const bf16x8 qf = lds_b128(LA_Q + (unsigned)(hd * 4 + qb) * 1024u + lane * 16u);
  const unsigned kbase = LA_K + (unsigned)(hd * 12 + qb) * 1024u + lane * 16u;
  f32x16 S[9];
  const float NEG = -__builtin_inff();
  f32x16 zero;
#pragma unroll
  for (int i = 0; i < 16; ++i) zero[i] = 0.f;
  float m = NEG;
#pragma unroll
  for (int j = 0; j < 9; ++j) {
    if (j >= j0) {
      S[j] = mfma(lds_b128(kbase + j * 1024u), qf, zero);
      if (j == 0) {
#pragma unroll
        for (int i = 0; i < 16; ++i) { const int kl = (i & 3) + 8 * (i >> 2) + 4 * hh; if (kl <= q) S[j][i] = NEG; }
      }
      if (j == 8) {
#pragma unroll
        for (int i = 0; i < 16; ++i) { const int kl = (i & 3) + 8 * (i >> 2) + 4 * hh; if (kl > q) S[j][i] = NEG; }
      }
#pragma unroll
      for (int i = 0; i < 16; ++i) m = fmaxf(m, S[j][i]);
    }
  }
  m = xhalf_max(m);
  const int grp = (lane >> 4) & 1, q4 = (lane & 15) >> 2, p4 = lane & 3;
  const unsigned vlane = (unsigned)((p4 & 1) * 512 + (((4 * hh + q4) ^ (4 * (p4 & 1))) * 16) + (p4 >> 1) * 8);
  const unsigned vb0 = grp ? LA_ONES + 128u : LA_V + (unsigned)(hd * 12 + qb) * 1024u + vlane;
  const unsigned vb1 = grp ? LA_ONES : LA_V + (unsigned)(hd * 12 + qb) * 1024u + vlane + 128u;
  const unsigned vstr = grp ? 0u : 1024u, vs1 = grp ? 0u : 256u;
  f32x16 acc = zero;
#pragma unroll
  for (int j = 0; j < 9; ++j) {
    if (j >= j0) {
      unsigned pp[8];
#pragma unroll
      for (int i = 0; i < 8; ++i) pp[i] = pk(__builtin_amdgcn_exp2f(S[j][2 * i] - m), __builtin_amdgcn_exp2f(S[j][2 * i + 1] - m));
#pragma unroll
      for (int s = 0; s < 2; ++s) {
        const bf16x8 vf = cat(lds_tr(vb0 + j * vstr + s * vs1), lds_tr(vb1 + j * vstr + s * vs1));
        const u32x4 pu = {pp[4 * s], pp[4 * s + 1], pp[4 * s + 2], pp[4 * s + 3]};
        acc = mfma(vf, __builtin_bit_cast(bf16x8, pu), acc);
      }
    }
  }
  const float inv = __builtin_amdgcn_rcpf(acc[8]);
  const u32x4 o = {pk(acc[0] * inv, acc[1] * inv), pk(acc[2] * inv, acc[3] * inv), pk(acc[4] * inv, acc[5] * inv), pk(acc[6] * inv, acc[7] * inv)};
  st16c(o, rs_ws, (unsigned)lane * 16u, WS_O + (unsigned)(sb * 32 + 2 * hp + hd) * 1024u);
  wait_vm<0>();
  bar();
  if (fused && threadIdx.x == 0) flag_publish(P.ws, WS_FO + (unsigned)(rb * 16 + hp) * 8u, tag);
}

DI void phase3(const Ptrs& P, int rb64, int cb, int w, int lane, bool fused, u64 tag) {
  const rsrc_t rs_ws = mk_rsrc(P.ws, WS_BYTES), rs_d = mk_rsrc(P.wd, FF * DM * 2), rs_o = mk_rsrc(P.wo, DM * DM * 2);
  if (fused) {
    if (w == 0) flag_wait(P.ws, (lane < 16 ? WS_FH : WS_FO) + (unsigned)((rb64 >> 1) * 16 + (lane & 15)) * 8u, lane < 32, tag);
    bar();
  }
  unsigned vw[2];
#pragma unroll
  for (int j = 0; j < 2; ++j) { const int G = (2 * w + j) * 64 + lane, kg = G >> 5, rr = G & 31;
    vw[j] = (unsigned)(4 * kg + ((rr & 7) >> 1)) * 1024u + (unsigned)((rr >> 3) * 32 + (rr & 1) * 16); }
  const int sbl = w >> 2, sbg = 2 * rb64 + sbl;
  auto issue = [&](int st) {
    const unsigned slot = (unsigned)(st & 3) * SLOT3;
#pragma unroll
    for (int j = 0; j < 2; ++j) { const int ks = 2 * (w & 3) + j;
      const unsigned src = st < 11 ? WS_H + (unsigned)(sbg * NU + st * 8 + ks) * 1024u : WS_O + (unsigned)(sbg * 32 + (st - 11) * 8 + ks) * 1024u;
      dma16c(slot + (unsigned)(sbl * 8 + ks) * 1024u, (unsigned)lane * 16u, rs_ws, src); }
#pragma unroll
    for (int j = 0; j < 2; ++j)
      dma16(slot + 16384u + (unsigned)(2 * w + j) * 1024u, vw[j], st < 11 ? rs_d : rs_o, (unsigned)cb * 128u + (unsigned)(st < 11 ? st : st - 11) * 131072u);
  };
  const int mb = w & 1, nb2 = (w >> 1) & 1, kh = w >> 2;
  const int h = lane >> 5, nsub = (lane >> 4) & 1, q4 = (lane & 15) >> 2, p4 = lane & 3;
  const unsigned aa = (unsigned)(mb * 8 + kh * 4) * 1024u + lane * 16u;
  const unsigned wa = 16384u + (unsigned)(16 * kh + h) * 512u + (unsigned)(2 * nb2 + nsub) * 128u + q4 * 32u + p4 * 8u;
  const int row = 64 * rb64 + 32 * mb + (lane & 31);
#pragma unroll
  for (int s = 0; s < 3; ++s) issue(s);
  f32x16 acc;
#pragma unroll
  for (int i = 0; i < 16; ++i) acc[i] = 0.f;
#pragma unroll 1
  for (int st = 0; st < 15; ++st) {
    if (st <= 12) wait_vm<8>(); else if (st == 13) wait_vm<4>(); else wait_vm<0>();
    bar();
    if (st + 3 < 15) issue(st + 3);
    const unsigned slot = (unsigned)(st & 3) * SLOT3;
#pragma unroll
    for (int ks = 0; ks < 4; ++ks) {
      if (st != 10 || kh == 0 || ks < 2) {
        const bf16x8 af = lds_b128(slot + aa + ks * 1024u);
        const unsigned a = slot + wa + ks * 2048u;
        const bf16x8 wf = cat(lds_tr(a), lds_tr(a + 1024u));
        acc = mfma(wf, af, acc);
      }
    }
  }
  bar();
  {
    const unsigned base = L3_RED + (unsigned)((w & 3) * 2 + kh) * 2048u + lane * 16u;
    const int so = kh ? 0 : 8;
    *(LAS f32x4*)(lds + base) = f32x4{acc[so], acc[so + 1], acc[so + 2], acc[so + 3]};
    *(LAS f32x4*)(lds + base + 1024u) = f32x4{acc[so + 4], acc[so + 5], acc[so + 6], acc[so + 7]};
  }
  bar();
  {
    const unsigned base = L3_RED + (unsigned)((w & 3) * 2 + (kh ^ 1)) * 2048u + lane * 16u;
    const f32x4 a0 = lds_f4(base), a1 = lds_f4(base + 1024u);
    const int ko = kh ? 8 : 0;
    const int col0 = 64 * cb + 32 * nb2 + 16 * kh + 4 * h;
    const unsigned short* xr = (const unsigned short*)P.x + (size_t)row * DM + col0;
    unsigned short* orow = (unsigned short*)P.out + (size_t)row * DM + col0;
    const u32x2 x0 = *(const GAS u32x2*)xr, x1 = *(const GAS u32x2*)(xr + 8);
    float v[8];
#pragma unroll
    for (int e = 0; e < 4; ++e) { v[e] = acc[ko + e] + a0[e]; v[4 + e] = acc[ko + 4 + e] + a1[e]; }
    v[0] += __uint_as_float(x0[0] << 16); v[1] += __uint_as_float(x0[0] & 0xffff0000u); v[2] += __uint_as_float(x0[1] << 16); v[3] += __uint_as_float(x0[1] & 0xffff0000u);
    v[4] += __uint_as_float(x1[0] << 16); v[5] += __uint_as_float(x1[0] & 0xffff0000u); v[6] += __uint_as_float(x1[1] << 16); v[7] += __uint_as_float(x1[1] & 0xffff0000u);
    *(GAS u32x2*)orow = u32x2{pk(v[0], v[1]), pk(v[2], v[3])};
    *(GAS u32x2*)(orow + 8) = u32x2{pk(v[4], v[5]), pk(v[6], v[7])};
  }
}

template <int PH>
__global__ __launch_bounds__(512, 2) void mk_kernel(const void* x, const void* wqkv, const void* wo, const void* wg, const void* wu, const void* wd, void* out, char* ws) {
  const Ptrs P{x, wqkv, wo, wg, wu, wd, out, ws};
  constexpr bool fused = PH == 7;
  const int b = blockIdx.x, xcd = b & 7, idx = b >> 3;
  const int w = __builtin_amdgcn_readfirstlane(threadIdx.x >> 6), lane = threadIdx.x & 63;
  u64 tag = 0;
  if (fused) tag = *(const u64*)(ws + WS_EPOCH) + 1ull;
  if (PH & 1) {
    const int rb = (xcd >> 2) * 8 + (idx >> 2), g = (xcd & 3) * 4 + (idx & 3);
    if (w < 4) phase1<0>(P, rb, g, w, lane, fused, tag); else phase1<1>(P, rb, g, w - 4, lane, fused, tag);
  }
  if (PH & 2) { if (fused) bar(); phase2(P, idx & 15, xcd * 2 + (idx >> 4), w, lane, fused, tag); }
  if (PH & 4) { if (fused) bar(); phase3(P, xcd * 4 + (idx >> 3), idx & 7, w, lane, fused, tag); }
  if (fused && b == 0 && w == 0) {
#pragma unroll 1
    for (int i = 0; i < 4; ++i) flag_wait(ws, WS_FQKV + (unsigned)(i * 64 + lane) * 8u, true, tag);
    if (lane == 0) __hip_atomic_store((GAS u64*)(ws + WS_EPOCH), tag, __ATOMIC_RELAXED, __HIP_MEMORY_SCOPE_AGENT);
  }
}

}

extern "C" void kernel_launch(void* const* d_in, const int* in_sizes, int n_in, void* d_out, int out_size, void* d_ws, size_t ws_size, hipStream_t stream) {
  const void *x = d_in[0], *wqkv = d_in[1], *wo = d_in[2], *wg = d_in[4], *wu = d_in[5], *wd = d_in[6];
  mk_kernel<7><<<256, 512, 0, stream>>>(x, wqkv, wo, wg, wu, wd, d_out, (char*)d_ws);
}
```

```cpp
#include <hip/hip_runtime.h>
#include <hip/hip_bf16.h>
#include <stdint.h>

namespace {

typedef short bf16x8 __attribute__((ext_vector_type(8)));
typedef short s16x4 __attribute__((ext_vector_type(4)));
typedef float f32x16 __attribute__((ext_vector_type(16)));
typedef float f32x4 __attribute__((ext_vector_type(4)));
typedef float f32x2 __attribute__((ext_vector_type(2)));
typedef unsigned u32x4 __attribute__((ext_vector_type(4)));
typedef unsigned u32x2 __attribute__((ext_vector_type(2)));
typedef __bf16 bf16x2_t __attribute__((ext_vector_type(2)));
typedef short v4i16_t __attribute__((ext_vector_type(4)));
typedef __amdgpu_buffer_rsrc_t rsrc_t;
#define LAS __attribute__((address_space(3)))
#define GAS __attribute__((address_space(1)))
#define DI __device__ __forceinline__
typedef unsigned long long u64;

constexpr int SEQ = 2048, DM = 512, FF = 1376, NU = 86;
constexpr unsigned WS_EPOCH = 0;
constexpr unsigned WS_FQKV = 16384, WS_FH = 32768, WS_FO = 49152;
constexpr unsigned WS_RS = 65536;
constexpr unsigned WS_QKV = 1u << 20;
constexpr unsigned WS_H = 7u << 20;
constexpr unsigned WS_O = 13u << 20;
constexpr unsigned WS_BYTES = 16u << 20;
constexpr unsigned SLOT1 = 28672;
constexpr unsigned L_CSP = 143360;
constexpr unsigned L_CSW = 146432;
constexpr unsigned L_ONES = 163328;
constexpr unsigned L_WORD = 163584;
constexpr unsigned LDS_BYTES = 163840;
constexpr unsigned LA_Q = 28672, LA_K = LA_Q + 8192, LA_V = LA_K + 24576;
DI constexpr unsigned l3w(int i) { return i == 0 ? 0u : 86016u + (unsigned)(i - 1) * 16384u; }
DI constexpr unsigned l3a(int i) { return i == 0 ? 135168u : 28672u + (unsigned)(i - 1) * 16384u; }
constexpr unsigned L3_RED = 0;

__shared__ __attribute__((aligned(1024))) char lds[LDS_BYTES];

DI unsigned pk(float a, float b) { f32x2 v = {a, b}; bf16x2_t r = __builtin_convertvector(v, bf16x2_t); return __builtin_bit_cast(unsigned, r); }
DI float dot2(unsigned a, unsigned b, float c) { return __builtin_amdgcn_fdot2_f32_bf16(__builtin_bit_cast(bf16x2_t, a), __builtin_bit_cast(bf16x2_t, b), c, false); }
DI rsrc_t mk_rsrc(const void* p, unsigned bytes) { return __builtin_amdgcn_make_buffer_rsrc((void*)p, 0, bytes, 0x00020000); }
DI void dma16(unsigned ldsaddr, unsigned voff, rsrc_t rs, unsigned soff) {
  asm volatile("s_mov_b32 m0, %0\n\ts_nop 0\n\tbuffer_load_dwordx4 %1, %2, %3 offen lds" ::"s"(ldsaddr), "v"(voff), "s"(rs), "s"(soff) : "memory");
}
DI void dma16c(unsigned ldsaddr, unsigned voff, rsrc_t rs, unsigned soff) {
  asm volatile("s_mov_b32 m0, %0\n\ts_nop 0\n\tbuffer_load_dwordx4 %1, %2, %3 offen sc1 lds" ::"s"(ldsaddr), "v"(voff), "s"(rs), "s"(soff) : "memory");
}
template <int N> DI void wait_vm() { asm volatile("s_waitcnt vmcnt(%0)" ::"n"(N) : "memory"); }
template <int N> DI void wait_vm_tie(u32x4& v) { asm volatile("s_waitcnt vmcnt(%1)" : "+v"(v) : "n"(N) : "memory"); }
DI void tie(u32x4& v) { asm volatile("" : "+v"(v) :: "memory"); }
DI void bar() { asm volatile("s_waitcnt lgkmcnt(0)\n\ts_barrier" ::: "memory"); }
DI f32x16 mfma(bf16x8 a, bf16x8 b, f32x16 c) { return __builtin_amdgcn_mfma_f32_32x32x16_bf16(a, b, c, 0, 0, 0); }
DI bf16x8 lds_b128(unsigned a) { return *(const LAS bf16x8*)(lds + a); }
DI s16x4 lds_tr(unsigned a) { return __builtin_amdgcn_ds_read_tr16_b64_v4i16((LAS v4i16_t*)(lds + a)); }
DI f32x4 lds_f4(unsigned a) { return *(const LAS f32x4*)(lds + a); }
DI bf16x8 cat(s16x4 lo, s16x4 hi) { return __builtin_shufflevector(lo, hi, 0, 1, 2, 3, 4, 5, 6, 7); }
DI float xhalf_sum(float v) { auto r = __builtin_amdgcn_permlane32_swap(__float_as_uint(v), __float_as_uint(v), false, false); return __uint_as_float(r[0]) + __uint_as_float(r[1]); }
DI float xhalf_max(float v) { auto r = __builtin_amdgcn_permlane32_swap(__float_as_uint(v), __float_as_uint(v), false, false); return fmaxf(__uint_as_float(r[0]), __uint_as_float(r[1])); }
DI void st16c(u32x4 v, rsrc_t rs, unsigned voff, unsigned soff) { __builtin_amdgcn_raw_buffer_store_b128(v, rs, voff, soff, 16); }
DI void st8c(u32x2 v, rsrc_t rs, unsigned voff, unsigned soff) { __builtin_amdgcn_raw_buffer_store_b64(v, rs, voff, soff, 16); }
constexpr unsigned ONES2 = 0x3F803F80u;
template <int CTRL> DI float dpp_add(float v) { return v + __int_as_float(__builtin_amdgcn_update_dpp(0, __float_as_int(v), CTRL, 0xf, 0xf, true)); }
DI float wave_sum(float v) {
  v = dpp_add<0xB1>(v); v = dpp_add<0x4E>(v); v = dpp_add<0x141>(v); v = dpp_add<0x140>(v);
  auto a = __builtin_amdgcn_permlane16_swap(__float_as_uint(v), __float_as_uint(v), false, false);
  v = __uint_as_float(a[0]) + __uint_as_float(a[1]);
  return xhalf_sum(v);
}
DI float bf_lo(unsigned u) { return __uint_as_float(u << 16); }
DI float bf_hi(unsigned u) { return __uint_as_float(u & 0xffff0000u); }
DI u32x4 ld16_asm(rsrc_t rs, unsigned voff, unsigned soff) { u32x4 v; asm volatile("buffer_load_dwordx4 %0, %1, %2, %3 offen" : "=v"(v) : "v"(voff), "s"(rs), "s"(soff) : "memory"); return v; }
DI u32x4 ld16c_asm(rsrc_t rs, unsigned voff, unsigned soff) { u32x4 v; asm volatile("buffer_load_dwordx4 %0, %1, %2, %3 offen sc1" : "=v"(v) : "v"(voff), "s"(rs), "s"(soff) : "memory"); return v; }
#define SB0() __builtin_amdgcn_sched_barrier(0)
#define STAMP2(i) do {} while (0)
struct Stamps {};
#define STAMP(i) do {} while (0)
#define CSTAMP(i) do {} while (0)

struct Ptrs { const void *x, *wqkv, *wo, *wg, *wu, *wd; void* out; char* ws; };
struct Tag { unsigned a, b; };

DI void flag_store(rsrc_t rs_ws, unsigned off, Tag tg, int lane) { if (lane == 0) st8c(u32x2{tg.a, tg.b}, rs_ws, 0u, off); }
DI bool flags_ok(u32x4 v, bool act, Tag tg) { return __all(!act || (v[0] == tg.a && v[1] == tg.b && v[2] == tg.a && v[3] == tg.b)); }
DI void flags_spin(rsrc_t rs_ws, unsigned voff, bool act, Tag tg) {
  for (unsigned spins = 0; spins < (1u << 20); ++spins) {
    u32x4 v = ld16c_asm(rs_ws, voff, 0u); wait_vm_tie<0>(v);
    if (flags_ok(v, act, tg)) break;
    __builtin_amdgcn_s_sleep(1);
  }
}
DI void word_set(int i, unsigned v) { *(volatile LAS unsigned*)(lds + L_WORD + i * 4) = v; }
DI unsigned word_get(int i) { return __builtin_amdgcn_readfirstlane(*(volatile LAS unsigned*)(lds + L_WORD + i * 4)); }

DI void csum(float& c, bf16x8 f) {
  const u32x4 u = __builtin_bit_cast(u32x4, f);
  c += ((bf_lo(u[0]) + bf_hi(u[0])) + (bf_lo(u[1]) + bf_hi(u[1]))) + ((bf_lo(u[2]) + bf_hi(u[2])) + (bf_lo(u[3]) + bf_hi(u[3])));
}

DI void attn_issue(rsrc_t rs_ws, int rb, int hp, int w, int lane) {
#pragma unroll
  for (int j = 0; j < 7; ++j) {
    const int t = w * 7 + j;
    int which, hd, sbk;
    if (t < 8) { which = 0; hd = t >> 2; sbk = 4 * rb + (t & 3); }
    else { const int tt = t - 8; which = 1 + tt / 24; hd = (tt % 24) / 12; sbk = 4 * rb - 8 + tt % 12; }
    if (sbk < 0) sbk = 0;
    dma16c(LA_Q + (unsigned)t * 1024u, (unsigned)lane * 16u, rs_ws, WS_QKV + (unsigned)((which * 32 + 2 * hp + hd) * 64 + sbk) * 1024u);
  }
}
DI unsigned qkv_flag_voff(int rb, int hp, int lane, bool& act) {
  const int c = lane >> 2, kind = c == 0 ? 0 : (c <= 3 ? 1 : 2), d = c == 0 ? 0 : (c - 1) % 3, rr = rb - d;
  act = lane < 28 && rr >= 0;
  return act ? WS_FQKV + (unsigned)((rr * 16 + (kind * 16 + hp) / 3) * 64 + (lane & 3) * 16) : 0u;
}

template <int NJ>
DI bool phase1(const Ptrs& P, int rb, int g, int rot, int mi, int lane, int rb2, int hp2, Tag& tg, u64& epoch, Stamps& T) {
  const int w = NJ * 4 + mi;
  u64 e0; asm volatile("s_load_dwordx2 %0, %1, 0x0" : "=s"(e0) : "s"(P.ws) : "memory");
  const rsrc_t rs_x = mk_rsrc(P.x, SEQ * DM * 2), rs_q = mk_rsrc(P.wqkv, DM * 1536 * 2);
  const rsrc_t rs_f = mk_rsrc(NJ ? P.wu : P.wg, DM * FF * 2), rs_ws = mk_rsrc(P.ws, WS_BYTES);
  const unsigned sx0 = (unsigned)rb * 131072u, sq0 = (unsigned)g * 192u;
  constexpr int NQ = NJ ? 2 : 1;
  constexpr int NA = 2 + NQ;
  unsigned vx[2], vq[2], vf[3];
#pragma unroll
  for (int j = 0; j < 2; ++j) vx[j] = (unsigned)(16 * w + 8 * j + (lane >> 3)) * 1024u + (unsigned)(((lane & 7) ^ (4 * j + (lane >> 4))) * 16);
#pragma unroll
  for (int j = 0; j < NQ; ++j) { const int ins = NJ ? 2 * mi + j : 8 + mi; const int G = ins * 64 + lane, kg = G / 48, rr = G % 48;
    vq[j] = (unsigned)(4 * kg + ((rr & 7) >> 1)) * 3072u + (unsigned)((rr >> 3) * 32 + (rr & 1) * 16); }
  const int u0 = (g * NU) >> 4, nu = (((g + 1) * NU) >> 4) - u0;
  const unsigned sf0 = (unsigned)u0 * 32u;
  auto issue = [&](int gs) {
    const unsigned slot = (unsigned)(gs % 5) * SLOT1;
    const unsigned c = (unsigned)(gs + rot) & 7u;
    if (gs < 8) {
#pragma unroll
      for (int j = 0; j < 2; ++j) dma16(slot + (unsigned)(2 * w + j) * 1024u, vx[j], rs_x, sx0 + c * 128u);
#pragma unroll
      for (int j = 0; j < NQ; ++j) dma16(slot + 16384u + (unsigned)(NJ ? 2 * mi + j : 8 + mi) * 1024u, vq[j], rs_q, sq0 + c * (64u * 3072u));
    } else {
#pragma unroll
      for (int j = 0; j < 3; ++j) dma16(slot + NJ * 12288u + (unsigned)(3 * mi + j) * 1024u, vf[j], rs_f, sf0 + c * (64u * 2752u));
    }
  };
  issue(0);
  const unsigned myrow = blockIdx.x * 8u + (unsigned)w;
  u32x4 xrow = ld16_asm(rs_x, (unsigned)lane * 16u, myrow * 1024u);
  SB0();
#pragma unroll
  for (int j = 0; j < 3; ++j) { const int G = (3 * mi + j) * 64 + lane, kg = G / 48, rr = G % 48, unit = (rr >> 3) ^ NJ;
    vf[j] = unit < nu ? (unsigned)(4 * kg + ((rr & 7) >> 1)) * 2752u + (unsigned)(unit * 32 + (rr & 1) * 16) : 0x40000000u; }
  const int r = lane & 31, h = lane >> 5, nsub = (lane >> 4) & 1, q4 = (lane & 15) >> 2, p4 = lane & 3;
  unsigned xa[4], ro[4];
#pragma unroll
  for (int p = 0; p < 4; ++p) { const int ks = (p + mi) & 3;
    xa[p] = (unsigned)(32 * mi + r) * 128u + (unsigned)(((2 * ks + h) ^ ((r >> 1) & 7)) * 16);
    ro[p] = (unsigned)ks * 3072u; }
  const unsigned wl = (unsigned)(2 * h) * 768u + q4 * 32u + p4 * 8u;
  const unsigned wa = 16384u + wl + nsub * 128u;
  const unsigned wbE = nsub * 12288u + wl + nsub * 128u;
  const unsigned wbO = nsub * 12288u + wl - nsub * 128u;
  SB0();
#pragma unroll
  for (int s = 1; s < 5; ++s) issue(s);
  SB0();
  STAMP(1);
  asm volatile("s_waitcnt lgkmcnt(0)" : "+s"(e0) :: "memory");
  epoch = e0 + 1ull;
  tg.a = (unsigned)epoch; tg.b = ((unsigned)(e0 >> 32) ^ 0x5bd1e995u) + tg.a;

  bf16x8 xf[32];
  constexpr int NB1 = NJ ? 1 : 2;
  constexpr int CSP1A = NJ ? 1 : 0, CSP1B = NJ ? 2 : 0;
  f32x16 acc[3];
  float cs[3] = {0.f, 0.f, 0.f};
#pragma unroll
  for (int b = 0; b < 3; ++b)
#pragma unroll
    for (int i = 0; i < 16; ++i) acc[b][i] = 0.f;
  bf16x8 wf[3][NB1], wg[2][3];
  auto ld1a = [&](int t) {
    const unsigned slot = (unsigned)((t >> 2) % 5) * SLOT1;
    xf[t] = lds_b128(slot + xa[t & 3]);
#pragma unroll
    for (int b = 0; b < NB1; ++b) { const unsigned a = slot + wa + ro[t & 3] + (NJ ? 2 : b) * 256u; wf[t % 3][b] = cat(lds_tr(a), lds_tr(a + 768u)); }
  };
  auto ld1b = [&](int t) {
    const unsigned slot = (unsigned)((8 + (t >> 2)) % 5) * SLOT1;
#pragma unroll
    for (int b = 0; b < 3; ++b) { const int u = NJ * 3 + b; const unsigned a = slot + ((u & 1) ? wbO : wbE) + ro[t & 3] + u * 128u; wg[t & 1][b] = cat(lds_tr(a), lds_tr(a + 768u)); }
  };
  auto cs_put = [&](int nb, int b0) {
#pragma unroll
    for (int b = 0; b < 3; ++b) if (b < nb) { const float v = xhalf_sum(cs[b]); if (lane < 32) *(LAS float*)(lds + L_CSP + (((b0 + b) * 32 + r) * 4 + mi) * 4) = v; }
  };
  auto cs_get = [&](int nb, int b0) {
#pragma unroll
    for (int b = 0; b < 3; ++b) if (b < nb) { const f32x4 c4 = lds_f4(L_CSP + ((b0 + b) * 32 + r) * 16);
      *(LAS float*)(lds + L_CSW + w * 384 + b * 128 + r * 4) = (c4[0] + c4[1]) + (c4[2] + c4[3]); }
  };
  wait_vm<4 * NA>();
  bar();
  STAMP(2); CSTAMP(23);
  ld1a(0); ld1a(1);
  u32x4 sv;
  const unsigned sv_off = WS_RS + (unsigned)(128 * rb + 32 * mi) * 16u;
#pragma unroll
  for (int t = 0; t < 32; ++t) {
    const int s = t >> 2, p = t & 3;
    if (p == 2) {
      if (s == 7) cs_put(NB1, NJ ? 2 : 0);
      if (s <= 3) wait_vm<3 * NA + 1>();
      else if (s == 4) wait_vm<2 * NA + 3>();
      else if (s == 5) wait_vm<NA + 6>();
      else wait_vm<10>();
      bar();
      STAMP2(24 + s);
      issue(s + 5);
      if (s == 5) sv = ld16c_asm(rs_ws, (unsigned)r * 16u, sv_off);
      if (s == 7) cs_get(NB1, NJ ? 2 : 0);
    }
    if (t < 30) ld1a(t + 2); else if (t == 31) ld1b(0);
    SB0();
#pragma unroll
    for (int b = 0; b < NB1; ++b) { acc[b] = mfma(wf[t % 3][b], xf[t], acc[b]); if (p == CSP1A) csum(cs[b], wf[t % 3][b]); }
    if (t == 1) {
      tie(xrow);
      float e[8];
#pragma unroll
      for (int i = 0; i < 4; ++i) { e[2 * i] = bf_lo(xrow[i]); e[2 * i + 1] = bf_hi(xrow[i]); }
      const float mean = wave_sum(((e[0] + e[1]) + (e[2] + e[3])) + ((e[4] + e[5]) + (e[6] + e[7]))) * (1.f / 512.f);
      float q = 0.f;
#pragma unroll
      for (int i = 0; i < 8; ++i) { const float d = e[i] - mean; q = fmaf(d, d, q); }
      const float rstd = __builtin_amdgcn_rsqf(wave_sum(q) * (1.f / 512.f) + 1e-5f);
      if (lane == 0) st16c(u32x4{__float_as_uint(mean), tg.a, __float_as_uint(rstd), tg.b}, rs_ws, 0u, WS_RS + myrow * 16u);
    }
    SB0();
  }
  STAMP(3); CSTAMP(22);
  wait_vm_tie<6>(sv);
  for (unsigned spins = 0; !__all(sv[1] == tg.a && sv[3] == tg.b) && spins < (1u << 20); ++spins) {
    __builtin_amdgcn_s_sleep(1);
    sv = ld16c_asm(rs_ws, (unsigned)r * 16u, sv_off); wait_vm_tie<0>(sv);
  }
  const float mean = __uint_as_float(sv[0]), rstd = __uint_as_float(sv[2]);
  const int sb = rb * 4 + mi;
#pragma unroll
  for (int b = 0; b < NB1; ++b) {
    const int blk = 3 * g + (NJ ? 2 : b), which = blk >> 4, hp = blk & 15;
    const float sc = which == 0 ? rstd * (0.25f * 1.4426950408889634f) : rstd;
    float v[16];
#pragma unroll
    for (int i = 0; i < 4; ++i) { const f32x4 c4 = lds_f4(L_CSW + w * 384 + b * 128 + (8 * i + 4 * h) * 4);
#pragma unroll
      for (int e = 0; e < 4; ++e) v[4 * i + e] = (acc[b][4 * i + e] - mean * c4[e]) * sc; }
    const unsigned vo = which == 2 ? (unsigned)(h * 512 + ((r ^ (4 * h)) * 16)) : (unsigned)(lane * 16);
#pragma unroll
    for (int hd = 0; hd < 2; ++hd) {
      const u32x4 o = {pk(v[8 * hd], v[8 * hd + 1]), pk(v[8 * hd + 2], v[8 * hd + 3]), pk(v[8 * hd + 4], v[8 * hd + 5]), pk(v[8 * hd + 6], v[8 * hd + 7])};
      st16c(o, rs_ws, vo, WS_QKV + (unsigned)((which * 32 + 2 * hp + hd) * 64 + sb) * 1024u);
    }
  }
  STAMP(4); CSTAMP(18);
  constexpr int NST = 2 * NB1;
#pragma unroll
  for (int b = 0; b < 3; ++b) { cs[b] = 0.f;
#pragma unroll
    for (int i = 0; i < 16; ++i) acc[b][i] = 0.f; }
  bool early = false, act;
  const unsigned qv = qkv_flag_voff(rb2, hp2, lane, act);
  u32x4 fq;
  SB0();
#pragma unroll
  for (int t = 0; t < 32; ++t) {
    const int s = 8 + (t >> 2), p = t & 3;
    if (p == 3) {
      if (s == 15) cs_put(3, NJ * 3);
      if (s <= 9) wait_vm<9 + NST>();
      else if (s == 10) { wait_vm<6>(); }
      else if (s == 11) wait_vm<10>();
      else if (s == 12) wait_vm<7>();
      else if (s == 13) { wait_vm<0>(); if (w == 0) { tie(fq); word_set(0, flags_ok(fq, act, tg) ? 1u : 0u); } }
      else if (s == 14) { if (early) wait_vm<7>(); else wait_vm<0>(); }
      bar();
      STAMP2(24 + s);
      if (s + 5 <= 15) issue(s + 5);
      if (s == 10) flag_store(rs_ws, WS_FQKV + (unsigned)((rb * 16 + g) * 64 + w * 8), tg, lane);
      if (s == 12 && w == 0) fq = ld16c_asm(rs_ws, qv, 0u);
      if (s == 13) { early = word_get(0) != 0u; if (early) attn_issue(rs_ws, rb2, hp2, w, lane); }
      if (s == 15) cs_get(3, NJ * 3);
    }
    if (t < 31) ld1b(t + 1);
    SB0();
#pragma unroll
    for (int b = 0; b < 3; ++b) { acc[b] = mfma(wg[t & 1][b], xf[t], acc[b]); if (p == CSP1B) csum(cs[b], wg[t & 1][b]); }
    SB0();
  }
  STAMP(5); CSTAMP(19);
  const float r2 = rstd * rstd;
#pragma unroll
  for (int b = 0; b < 3; ++b) {
    float v[16];
#pragma unroll
    for (int i = 0; i < 4; ++i) { const f32x4 c4 = lds_f4(L_CSW + w * 384 + b * 128 + (8 * i + 4 * h) * 4);
#pragma unroll
      for (int e = 0; e < 4; ++e) v[4 * i + e] = acc[b][4 * i + e] - mean * c4[e]; }
    float hv[8];
#pragma unroll
    for (int i = 0; i < 8; ++i) hv[i] = fmaxf(v[i], 0.f) * v[8 + i] * r2;
    const u32x4 o = {pk(hv[0], hv[1]), pk(hv[2], hv[3]), pk(hv[4], hv[5]), pk(hv[6], hv[7])};
    if (NJ * 3 + b < nu) st16c(o, rs_ws, (unsigned)lane * 16u, WS_H + (unsigned)(sb * NU + u0 + NJ * 3 + b) * 1024u);
  }
  STAMP(6);
  if (early) { if (NJ == 0 || nu == 6) wait_vm<3>(); else wait_vm<2>(); }
  return early;
}

template <bool FULL>
DI float attn_scores(int j0, int qb, int hd, int lane, f32x16 (&S)[9]) {
  const int q = lane & 31, hh = lane >> 5;
  const bf16x8 qf = lds_b128(LA_Q + (unsigned)(hd * 4 + qb) * 1024u + lane * 16u);
  const unsigned kbase = LA_K + (unsigned)(hd * 12 + qb) * 1024u + lane * 16u;
  const float NEG = -__builtin_inff();
  f32x16 zero;
#pragma unroll
  for (int i = 0; i < 16; ++i) zero[i] = 0.f;
  float m = NEG;
#pragma unroll
  for (int j = 0; j < 9; ++j) {
    if (FULL || j >= j0) {
      S[j] = mfma(lds_b128(kbase + j * 1024u), qf, zero);
      if (j == 0) {
#pragma unroll
        for (int i = 0; i < 16; ++i) { const int kl = (i & 3) + 8 * (i >> 2) + 4 * hh; if (kl <= q) S[j][i] = NEG; }
      }
      if (j == 8) {
#pragma unroll
        for (int i = 0; i < 16; ++i) { const int kl = (i & 3) + 8 * (i >> 2) + 4 * hh; if (kl > q) S[j][i] = NEG; }
      }
#pragma unroll
      for (int i = 0; i < 16; i += 2) m = fmaxf(fmaxf(m, S[j][i]), S[j][i + 1]);
    }
  }
  return xhalf_max(m);
}
template <bool FULL>
DI u32x4 attn_pv(int j0, int qb, int hd, int lane, float m, f32x16 (&S)[9]) {
  const int hh = lane >> 5;
  const int grp = (lane >> 4) & 1, q4 = (lane & 15) >> 2, p4 = lane & 3;
  const unsigned vlane = (unsigned)((p4 & 1) * 512 + (((4 * hh + q4) ^ (4 * (p4 & 1))) * 16) + (p4 >> 1) * 8);
  const unsigned vb0 = grp ? L_ONES + 128u : LA_V + (unsigned)(hd * 12 + qb) * 1024u + vlane;
  const unsigned vb1 = grp ? L_ONES : LA_V + (unsigned)(hd * 12 + qb) * 1024u + vlane + 128u;
  const unsigned vstr = grp ? 0u : 1024u, vs1 = grp ? 0u : 256u;
  f32x16 acc;
#pragma unroll
  for (int i = 0; i < 16; ++i) acc[i] = 0.f;
#pragma unroll
  for (int j = 0; j < 9; ++j) {
    if (FULL || j >= j0) {
      unsigned pp[8];
#pragma unroll
      for (int i = 0; i < 8; ++i) pp[i] = pk(__builtin_amdgcn_exp2f(S[j][2 * i] - m), __builtin_amdgcn_exp2f(S[j][2 * i + 1] - m));
#pragma unroll
      for (int s = 0; s < 2; ++s) {
        const bf16x8 vf = cat(lds_tr(vb0 + j * vstr + s * vs1), lds_tr(vb1 + j * vstr + s * vs1));
        const u32x4 pu = {pp[4 * s], pp[4 * s + 1], pp[4 * s + 2], pp[4 * s + 3]};
        acc = mfma(vf, __builtin_bit_cast(bf16x8, pu), acc);
      }
    }
  }
  const float inv = __builtin_amdgcn_rcpf(acc[8]);
  return u32x4{pk(acc[0] * inv, acc[1] * inv), pk(acc[2] * inv, acc[3] * inv), pk(acc[4] * inv, acc[5] * inv), pk(acc[6] * inv, acc[7] * inv)};
}

DI void phase23(const Ptrs& P, bool early, int rb1, int g1, int nH, int rb, int hp, int rb64, int cb, int rot, int w, int lane, Tag tg, Stamps& T) {
  const rsrc_t rs_ws = mk_rsrc(P.ws, WS_BYTES), rs_d = mk_rsrc(P.wd, FF * DM * 2), rs_o = mk_rsrc(P.wo, DM * DM * 2);
  if (threadIdx.x < 64) *(LAS unsigned*)(lds + L_ONES + threadIdx.x * 4) = ONES2;
  if (!early) {
    bool act; const unsigned qv = qkv_flag_voff(rb, hp, lane, act);
    if (w == 0) flags_spin(rs_ws, qv, act, tg);
    bar();
    attn_issue(rs_ws, rb, hp, w, lane);
    wait_vm<0>();
  }
  bar();
  STAMP(9);
  unsigned vw[2];
#pragma unroll
  for (int j = 0; j < 2; ++j) { const int G = (2 * w + j) * 64 + lane, kg = G >> 5, rr = G & 31;
    vw[j] = (unsigned)(4 * kg + ((rr & 7) >> 1)) * 1024u + (unsigned)((rr >> 3) * 32 + (rr & 1) * 16); }
  const int sbl = w >> 2, sbg = 2 * rb64 + sbl;
  auto chunk = [&](int st) { return st < 10 ? (st + rot >= 10 ? st + rot - 10 : st + rot) : (st == 10 ? 10 : ((st - 11 + rot) & 3)); };
  auto issueA = [&](int st) {
    const int c = chunk(st);
#pragma unroll
    for (int j = 0; j < 2; ++j) { const int ks = 2 * (w & 3) + j;
      const unsigned src = st < 11 ? WS_H + (unsigned)(sbg * NU + c * 8 + ks) * 1024u : WS_O + (unsigned)(sbg * 32 + c * 8 + ks) * 1024u;
      dma16c(l3a(st & 3) + (unsigned)(sbl * 8 + ks) * 1024u, (unsigned)lane * 16u, rs_ws, src); }
  };
  auto issueW = [&](int st) {
    const int c = chunk(st);
#pragma unroll
    for (int j = 0; j < 2; ++j)
      dma16(l3w(st & 3) + (unsigned)(2 * w + j) * 1024u, vw[j], st < 11 ? rs_d : rs_o, (unsigned)cb * 128u + (unsigned)c * 131072u);
  };
#pragma unroll
  for (int s = 0; s < 4; ++s) issueW(s);
  const int qb = w & 3, hd = w >> 2, sb = 4 * rb + qb;
  const unsigned hv = WS_FH + (unsigned)((rb64 >> 1) * 1024 + lane * 16), ov = WS_FO + (unsigned)((rb64 >> 1) * 1024 + lane * 16);
  u32x4 o, fh;
  {
    f32x16 S[9];
    if (sb >= 8) {
      const float m = attn_scores<true>(0, qb, hd, lane, S);
      wait_vm<8>(); flag_store(rs_ws, WS_FH + (unsigned)((rb1 * 16 + g1) * 64 + w * 8), tg, lane);
      o = attn_pv<true>(0, qb, hd, lane, m, S);
    } else {
      const float m = attn_scores<false>(8 - sb, qb, hd, lane, S);
      wait_vm<8>(); flag_store(rs_ws, WS_FH + (unsigned)((rb1 * 16 + g1) * 64 + w * 8), tg, lane);
      o = attn_pv<false>(8 - sb, qb, hd, lane, m, S);
    }
  }
  if (w == 0) fh = ld16c_asm(rs_ws, hv, 0u);
  st16c(o, rs_ws, (unsigned)lane * 16u, WS_O + (unsigned)(sb * 32 + 2 * hp + hd) * 1024u);
  STAMP(11);
  if (w == 0) { wait_vm_tie<1>(fh); if (!flags_ok(fh, true, tg)) flags_spin(rs_ws, hv, true, tg); }
  bar();
  STAMP(13);
#pragma unroll
  for (int s = 0; s < 4; ++s) issueA(s);
  const int mb = w & 1, nb2 = (w >> 1) & 1, kh = w >> 2;
  const int h = lane >> 5, nsub = (lane >> 4) & 1, q4 = (lane & 15) >> 2, p4 = lane & 3;
  const unsigned aa = (unsigned)(mb * 8 + kh * 4) * 1024u + lane * 16u;
  const unsigned wa = (unsigned)(16 * kh + h) * 512u + (unsigned)(2 * nb2 + nsub) * 128u + q4 * 32u + p4 * 8u;
  const int row = 64 * rb64 + 32 * mb + (lane & 31);
  f32x16 acc;
#pragma unroll
  for (int i = 0; i < 16; ++i) acc[i] = 0.f;
  bf16x8 af[2][4], wfr[2][4];
  auto ld3 = [&](int st) {
#pragma unroll
    for (int ks = 0; ks < 4; ++ks) { af[st & 1][ks] = lds_b128(l3a(st & 3) + aa + ks * 1024u); const unsigned a = l3w(st & 3) + wa + ks * 2048u; wfr[st & 1][ks] = cat(lds_tr(a), lds_tr(a + 1024u)); }
  };
  wait_vm<6>();
  flag_store(rs_ws, WS_FO + (unsigned)((rb * 16 + hp) * 64 + w * 8), tg, lane);
  bar();
  STAMP(14);
  ld3(0);
  u32x4 fo;
#pragma unroll
  for (int st = 0; st < 15; ++st) {
    if (st < 14) {
      if (st == 0) wait_vm<5>(); else if (st == 1) wait_vm<7>(); else if (st == 2) wait_vm<9>(); else if (st <= 11) wait_vm<8>(); else if (st == 12) wait_vm<4>(); else wait_vm<0>();
      if (st == 7 && w == 0) { tie(fo); if (!flags_ok(fo, true, tg)) flags_spin(rs_ws, ov, true, tg); }
      bar();
      STAMP2(40 + st);
      if (st + 4 < 15) { issueA(st + 4); issueW(st + 4); }
      if (st == 4 && w == 0) fo = ld16c_asm(rs_ws, ov, 0u);
      ld3(st + 1);
    }
    SB0();
    acc = mfma(wfr[st & 1][0], af[st & 1][0], acc); acc = mfma(wfr[st & 1][1], af[st & 1][1], acc);
    if (st != 10 || kh == 0) { acc = mfma(wfr[st & 1][2], af[st & 1][2], acc); acc = mfma(wfr[st & 1][3], af[st & 1][3], acc); }
    SB0();
  }
  STAMP(15);
  const int col0 = 64 * cb + 32 * nb2 + 16 * kh + 4 * h;
  const unsigned short* xr = (const unsigned short*)P.x + (size_t)row * DM + col0;
  const u32x2 x0 = *(const GAS u32x2*)xr, x1 = *(const GAS u32x2*)(xr + 8);
  bar();
  {
    const unsigned base = L3_RED + (unsigned)((w & 3) * 2 + kh) * 2048u + lane * 16u;
    if (kh) { *(LAS f32x4*)(lds + base) = f32x4{acc[0], acc[1], acc[2], acc[3]}; *(LAS f32x4*)(lds + base + 1024u) = f32x4{acc[4], acc[5], acc[6], acc[7]}; }
    else { *(LAS f32x4*)(lds + base) = f32x4{acc[8], acc[9], acc[10], acc[11]}; *(LAS f32x4*)(lds + base + 1024u) = f32x4{acc[12], acc[13], acc[14], acc[15]}; }
  }
  bar();
  {
    const unsigned base = L3_RED + (unsigned)((w & 3) * 2 + (kh ^ 1)) * 2048u + lane * 16u;
    const f32x4 a0 = lds_f4(base), a1 = lds_f4(base + 1024u);
    unsigned short* orow = (unsigned short*)P.out + (size_t)row * DM + col0;
    float v[8];
#pragma unroll
    for (int e = 0; e < 4; ++e) { v[e] = (kh ? acc[8 + e] : acc[e]) + a0[e]; v[4 + e] = (kh ? acc[12 + e] : acc[4 + e]) + a1[e]; }
    v[0] += bf_lo(x0[0]); v[1] += bf_hi(x0[0]); v[2] += bf_lo(x0[1]); v[3] += bf_hi(x0[1]);
    v[4] += bf_lo(x1[0]); v[5] += bf_hi(x1[0]); v[6] += bf_lo(x1[1]); v[7] += bf_hi(x1[1]);
    *(GAS u32x2*)orow = u32x2{pk(v[0], v[1]), pk(v[2], v[3])};
    *(GAS u32x2*)(orow + 8) = u32x2{pk(v[4], v[5]), pk(v[6], v[7])};
  }
}

__global__ __launch_bounds__(512, 2) void mk_kernel(const void* x, const void* wqkv, const void* wo, const void* wg, const void* wu, const void* wd, void* out, char* ws) {
  const Ptrs P{x, wqkv, wo, wg, wu, wd, out, ws};
  Stamps T;
  STAMP(0); CSTAMP(20);
  const int b = blockIdx.x, xcd = b & 7, idx = b >> 3;
  const int w = __builtin_amdgcn_readfirstlane(threadIdx.x >> 6), lane = threadIdx.x & 63;
  Tag tg; u64 epoch;
  const int rb1 = (xcd >> 2) * 8 + (idx >> 2), g1 = (xcd & 3) * 4 + (idx & 3), rot1 = ((idx >> 2) + 2 * (idx & 3)) & 7;
  const int rb2 = idx & 15, hp2 = xcd * 2 + (idx >> 4);
  bool early;
  if (w < 4) early = phase1<0>(P, rb1, g1, rot1, w, lane, rb2, hp2, tg, epoch, T); else early = phase1<1>(P, rb1, g1, rot1, w - 4, lane, rb2, hp2, tg, epoch, T);
  const int rot3 = ((idx & 7) + 3 * (idx >> 3)) % 10;
  phase23(P, early, rb1, g1, 0, rb2, hp2, xcd * 4 + (idx >> 3), idx & 7, rot3, w, lane, tg, T);
  if (b == 0 && w == 0) {
    const rsrc_t rs_ws = mk_rsrc(ws, WS_BYTES);
#pragma unroll 1
    for (int i = 0; i < 16; ++i) flags_spin(rs_ws, WS_FQKV + (unsigned)(i * 1024 + lane * 16), true, tg);
    if (lane == 0) __hip_atomic_store((GAS u64*)(ws + WS_EPOCH), epoch, __ATOMIC_RELAXED, __HIP_MEMORY_SCOPE_AGENT);
  }
}

}

extern "C" void kernel_launch(void* const* d_in, const int* in_sizes, int n_in, void* d_out, int out_size, void* d_ws, size_t ws_size, hipStream_t stream) {
  const void *x = d_in[0], *wqkv = d_in[1], *wo = d_in[2], *wg = d_in[4], *wu = d_in[5], *wd = d_in[6];
  mk_kernel<<<256, 512, 0, stream>>>(x, wqkv, wo, wg, wu, wd, d_out, (char*)d_ws);
}
```

```cpp
#include <hip/hip_runtime.h>
#include <hip/hip_bf16.h>
#include <stdint.h>

namespace {

typedef short bf16x8 __attribute__((ext_vector_type(8)));
typedef short s16x4 __attribute__((ext_vector_type(4)));
typedef float f32x16 __attribute__((ext_vector_type(16)));
typedef float f32x4 __attribute__((ext_vector_type(4)));
typedef float f32x2 __attribute__((ext_vector_type(2)));
typedef unsigned u32x4 __attribute__((ext_vector_type(4)));
typedef unsigned u32x2 __attribute__((ext_vector_type(2)));
typedef __bf16 bf16x2_t __attribute__((ext_vector_type(2)));
typedef short v4i16_t __attribute__((ext_vector_type(4)));
typedef __amdgpu_buffer_rsrc_t rsrc_t;
#define LAS __attribute__((address_space(3)))
#define GAS __attribute__((address_space(1)))
#define DI __device__ __forceinline__
typedef unsigned long long u64;

constexpr int SEQ = 2048, DM = 512, FF = 1376, NU = 86;
constexpr unsigned WS_EPOCH = 0;
constexpr unsigned WS_FQKV = 16384, WS_FH = 32768, WS_FO = 49152;
constexpr unsigned WS_RS = 65536;
constexpr unsigned WS_QKV = 1u << 20;
constexpr unsigned WS_H = 7u << 20;
constexpr unsigned WS_O = 13u << 20;
constexpr unsigned WS_BYTES = 16u << 20;
constexpr unsigned SLOT1 = 28672;
constexpr unsigned L_CSP = 143360;
constexpr unsigned L_CSW = 146432;
constexpr unsigned L_ONES = 163328;
constexpr unsigned L_WORD = 163584;
constexpr unsigned LDS_BYTES = 163840;
constexpr unsigned LA_Q = 28672, LA_K = LA_Q + 8192, LA_V = LA_K + 24576;
DI constexpr unsigned l3w(int i) { return i == 0 ? 0u : 86016u + (unsigned)(i - 1) * 16384u; }
DI constexpr unsigned l3a(int i) { return i == 0 ? 135168u : 28672u + (unsigned)(i - 1) * 16384u; }
constexpr unsigned L3_RED = 0;

__shared__ __attribute__((aligned(1024))) char lds[LDS_BYTES];

DI unsigned pk(float a, float b) { f32x2 v = {a, b}; bf16x2_t r = __builtin_convertvector(v, bf16x2_t); return __builtin_bit_cast(unsigned, r); }
DI float dot2(unsigned a, unsigned b, float c) { return __builtin_amdgcn_fdot2_f32_bf16(__builtin_bit_cast(bf16x2_t, a), __builtin_bit_cast(bf16x2_t, b), c, false); }
DI rsrc_t mk_rsrc(const void* p, unsigned bytes) { return __builtin_amdgcn_make_buffer_rsrc((void*)p, 0, bytes, 0x00020000); }
DI void dma16(unsigned ldsaddr, unsigned voff, rsrc_t rs, unsigned soff) {
  asm volatile("s_mov_b32 m0, %0\n\ts_nop 0\n\tbuffer_load_dwordx4 %1, %2, %3 offen lds" ::"s"(ldsaddr), "v"(voff), "s"(rs), "s"(soff) : "memory");
}
DI void dma16c(unsigned ldsaddr, unsigned voff, rsrc_t rs, unsigned soff) {
  asm volatile("s_mov_b32 m0, %0\n\ts_nop 0\n\tbuffer_load_dwordx4 %1, %2, %3 offen sc1 lds" ::"s"(ldsaddr), "v"(voff), "s"(rs), "s"(soff) : "memory");
}
template <int N> DI void wait_vm() { asm volatile("s_waitcnt vmcnt(%0)" ::"n"(N) : "memory"); }
template <int N> DI void wait_vm_tie(u32x4& v) { asm volatile("s_waitcnt vmcnt(%1)" : "+v"(v) : "n"(N) : "memory"); }
template <int V> struct IC { static constexpr int value = V; };
template <int I, int N, class F> DI void static_for(F&& f) { if constexpr (I < N) { f(IC<I>{}); static_for<I + 1, N>(f); } }
DI void tie(u32x4& v) { asm volatile("" : "+v"(v) :: "memory"); }
DI void bar() { asm volatile("s_waitcnt lgkmcnt(0)\n\ts_barrier" ::: "memory"); }
DI f32x16 mfma(bf16x8 a, bf16x8 b, f32x16 c) { return __builtin_amdgcn_mfma_f32_32x32x16_bf16(a, b, c, 0, 0, 0); }
DI bf16x8 lds_b128(unsigned a) { return *(const LAS bf16x8*)(lds + a); }
DI s16x4 lds_tr(unsigned a) { return __builtin_amdgcn_ds_read_tr16_b64_v4i16((LAS v4i16_t*)(lds + a)); }
DI f32x4 lds_f4(unsigned a) { return *(const LAS f32x4*)(lds + a); }
DI bf16x8 cat(s16x4 lo, s16x4 hi) { return __builtin_shufflevector(lo, hi, 0, 1, 2, 3, 4, 5, 6, 7); }
DI float xhalf_sum(float v) { auto r = __builtin_amdgcn_permlane32_swap(__float_as_uint(v), __float_as_uint(v), false, false); return __uint_as_float(r[0]) + __uint_as_float(r[1]); }
DI float xhalf_max(float v) { auto r = __builtin_amdgcn_permlane32_swap(__float_as_uint(v), __float_as_uint(v), false, false); return fmaxf(__uint_as_float(r[0]), __uint_as_float(r[1])); }
DI void st16c(u32x4 v, rsrc_t rs, unsigned voff, unsigned soff) { __builtin_amdgcn_raw_buffer_store_b128(v, rs, voff, soff, 16); }
DI void st8c(u32x2 v, rsrc_t rs, unsigned voff, unsigned soff) { __builtin_amdgcn_raw_buffer_store_b64(v, rs, voff, soff, 16); }
constexpr unsigned ONES2 = 0x3F803F80u;
template <int CTRL> DI float dpp_add(float v) { return v + __int_as_float(__builtin_amdgcn_update_dpp(0, __float_as_int(v), CTRL, 0xf, 0xf, true)); }
DI float wave_sum(float v) {
  v = dpp_add<0xB1>(v); v = dpp_add<0x4E>(v); v = dpp_add<0x141>(v); v = dpp_add<0x140>(v);
  auto a = __builtin_amdgcn_permlane16_swap(__float_as_uint(v), __float_as_uint(v), false, false);
  v = __uint_as_float(a[0]) + __uint_as_float(a[1]);
  return xhalf_sum(v);
}
DI float bf_lo(unsigned u) { return __uint_as_float(u << 16); }
DI float bf_hi(unsigned u) { return __uint_as_float(u & 0xffff0000u); }
DI u32x4 ld16_asm(rsrc_t rs, unsigned voff, unsigned soff) { u32x4 v; asm volatile("buffer_load_dwordx4 %0, %1, %2, %3 offen" : "=v"(v) : "v"(voff), "s"(rs), "s"(soff) : "memory"); return v; }
DI u32x4 ld16c_asm(rsrc_t rs, unsigned voff, unsigned soff) { u32x4 v; asm volatile("buffer_load_dwordx4 %0, %1, %2, %3 offen sc1" : "=v"(v) : "v"(voff), "s"(rs), "s"(soff) : "memory"); return v; }
#define SB0() __builtin_amdgcn_sched_barrier(0)
#define STAMP2(i) do {} while (0)
struct Stamps {};
#define STAMP(i) do {} while (0)
#define CSTAMP(i) do {} while (0)

struct Ptrs { const void *x, *wqkv, *wo, *wg, *wu, *wd; void* out; char* ws; };
struct Tag { unsigned a, b; };

DI void flag_store(rsrc_t rs_ws, unsigned off, Tag tg, int lane) { if (lane == 0) st8c(u32x2{tg.a, tg.b}, rs_ws, 0u, off); }
DI bool flags_ok(u32x4 v, bool act, Tag tg) { return __all(!act || (v[0] == tg.a && v[1] == tg.b && v[2] == tg.a && v[3] == tg.b)); }
DI void flags_spin(rsrc_t rs_ws, unsigned voff, bool act, Tag tg) {
  for (unsigned spins = 0; spins < (1u << 20); ++spins) {
    u32x4 v = ld16c_asm(rs_ws, voff, 0u); wait_vm_tie<0>(v);
    if (flags_ok(v, act, tg)) break;
    __builtin_amdgcn_s_sleep(1);
  }
}
DI void word_set(int i, unsigned v) { *(volatile LAS unsigned*)(lds + L_WORD + i * 4) = v; }
DI unsigned word_get(int i) { return __builtin_amdgcn_readfirstlane(*(volatile LAS unsigned*)(lds + L_WORD + i * 4)); }

DI void csum(float& c, bf16x8 f) {
  const u32x4 u = __builtin_bit_cast(u32x4, f);
  c += ((bf_lo(u[0]) + bf_hi(u[0])) + (bf_lo(u[1]) + bf_hi(u[1]))) + ((bf_lo(u[2]) + bf_hi(u[2])) + (bf_lo(u[3]) + bf_hi(u[3])));
  asm volatile("" : "+v"(c));
}

DI void attn_issue(rsrc_t rs_ws, int rb, int hp, int w, int lane, int piece) {
  const unsigned vo = (unsigned)lane * 16u;
  if (piece == 0) { dma16c(LA_Q + (unsigned)w * 1024u, vo, rs_ws, WS_QKV + (unsigned)((2 * hp + (w >> 2)) * 64 + 4 * rb + (w & 3)) * 1024u); return; }
  const int which = 1 + (w >> 2), hd = (w >> 1) & 1, j = 6 * (w & 1) + piece - 1;
  const int sbk = 4 * rb - 8 + j < 0 ? 0 : 4 * rb - 8 + j;
  dma16c(LA_K + (unsigned)((w >> 1) * 12 + j) * 1024u, vo, rs_ws, WS_QKV + (unsigned)((which * 32 + 2 * hp + hd) * 64 + sbk) * 1024u);
}
DI unsigned qkv_flag_voff(int rb, int hp, int lane, bool& act) {
  const int c = lane >> 2, kind = c == 0 ? 0 : (c <= 3 ? 1 : 2), d = c == 0 ? 0 : (c - 1) % 3, rr = rb - d;
  act = lane < 28 && rr >= 0;
  return act ? WS_FQKV + (unsigned)((rr * 16 + (kind * 16 + hp) / 3) * 64 + (lane & 3) * 16) : 0u;
}

template <int NJ> struct Led1 {
  static constexpr int NA = NJ ? 4 : 3, NST = NJ ? 2 : 4;
  static constexpr int n(int sg) { return sg < 8 ? NA : 3; }
  static constexpr int TB(int s) { return s < 8 ? 4 * s + 2 : 4 * s + 3; }
  static constexpr int piece_stage(int T) { for (int sg = 5; sg <= 15; ++sg) { const int j = T - TB(sg - 5); if (j >= 0 && j < n(sg)) return sg; } return -1; }
  static constexpr int extras(int T) { return (T == 5 ? 1 : 0)   + (T == 22 ? 1 : 0)   + (T == 43 ? 1 : 0)  ; }
  static constexpr int PRO = 5 * NA + 1;
  static constexpr int before(int T) {
    int c = PRO;
    for (int t = 0; t < T; ++t) { c += (piece_stage(t) >= 0 ? 1 : 0) + extras(t); if (t == 31) c += NST; }
    return c;
  }
  static constexpr int last_of(int sg) {
    if (sg == 0) return NA;
    if (sg < 5) return NA + 1 + sg * NA;
    const int t = TB(sg - 5) + n(sg) - 1;
    return before(t) + 1;
  }
  static constexpr int wait_stage(int T, int sg) { return before(T) - last_of(sg); }
};

template <int NJ>
DI bool phase1(const Ptrs& P, int rb, int g, int rot, int mi, int lane, int rb2, int hp2, Tag& tg, u64& epoch, Stamps& T_) {
  Stamps& T = T_;
  typedef Led1<NJ> L;
  const int w = NJ * 4 + mi;
  u64 e0 = 0;
  if (w == 0) asm volatile("s_load_dwordx2 %0, %1, 0x0" : "=s"(e0) : "s"(P.ws) : "memory");
  const rsrc_t rs_x = mk_rsrc(P.x, SEQ * DM * 2), rs_q = mk_rsrc(P.wqkv, DM * 1536 * 2);
  const rsrc_t rs_f = mk_rsrc(NJ ? P.wu : P.wg, DM * FF * 2), rs_ws = mk_rsrc(P.ws, WS_BYTES);
  const unsigned sx0 = (unsigned)rb * 131072u, sq0 = (unsigned)g * 192u;
  constexpr int NQ = NJ ? 2 : 1;
  constexpr int NA = 2 + NQ;
  unsigned vx[2], vq[2], vf[3];
#pragma unroll
  for (int j = 0; j < 2; ++j) vx[j] = (unsigned)(16 * w + 8 * j + (lane >> 3)) * 1024u + (unsigned)(((lane & 7) ^ (4 * j + (lane >> 4))) * 16);
#pragma unroll
  for (int j = 0; j < NQ; ++j) { const int ins = NJ ? 2 * mi + j : 8 + mi; const int G = ins * 64 + lane, kg = G / 48, rr = G % 48;
    vq[j] = (unsigned)(4 * kg + ((rr & 7) >> 1)) * 3072u + (unsigned)((rr >> 3) * 32 + (rr & 1) * 16); }
  const int u0 = (g * NU) >> 4, nu = (((g + 1) * NU) >> 4) - u0;
  const unsigned sf0 = (unsigned)u0 * 32u;
  auto piece = [&](int gs, int j) {
    const unsigned slot = (unsigned)(gs % 5) * SLOT1;
    const unsigned c = (unsigned)(gs + rot) & 7u;
    if (gs < 8) {
      if (j < 2) dma16(slot + (unsigned)(2 * w + j) * 1024u, vx[j], rs_x, sx0 + c * 128u);
      else dma16(slot + 16384u + (unsigned)(NJ ? 2 * mi + j - 2 : 8 + mi) * 1024u, vq[j - 2], rs_q, sq0 + c * (64u * 3072u));
    } else dma16(slot + NJ * 12288u + (unsigned)(3 * mi + j) * 1024u, vf[j], rs_f, sf0 + c * (64u * 2752u));
  };
  auto issue = [&](int gs) {
#pragma unroll
    for (int j = 0; j < NA; ++j) piece(gs, j);
  };
  issue(0);
  const unsigned myrow = blockIdx.x * 8u + (unsigned)w;
  u32x4 xrow = ld16_asm(rs_x, (unsigned)lane * 16u, myrow * 1024u);
  SB0();
#pragma unroll
  for (int j = 0; j < 3; ++j) { const int G = (3 * mi + j) * 64 + lane, kg = G / 48, rr = G % 48, unit = (rr >> 3) ^ NJ;
    vf[j] = unit < nu ? (unsigned)(4 * kg + ((rr & 7) >> 1)) * 2752u + (unsigned)(unit * 32 + (rr & 1) * 16) : 0x40000000u; }
  const int r = lane & 31, h = lane >> 5, nsub = (lane >> 4) & 1, q4 = (lane & 15) >> 2, p4 = lane & 3;
  unsigned xa[4], ro[4];
#pragma unroll
  for (int p = 0; p < 4; ++p) { const int ks = (p + mi) & 3;
    xa[p] = (unsigned)(32 * mi + r) * 128u + (unsigned)(((2 * ks + h) ^ ((r >> 1) & 7)) * 16);
    ro[p] = (unsigned)ks * 3072u; }
  const unsigned wl = (unsigned)(2 * h) * 768u + q4 * 32u + p4 * 8u;
  const unsigned wa = 16384u + wl + nsub * 128u;
  const unsigned wbE = nsub * 12288u + wl + nsub * 128u;
  const unsigned wbO = nsub * 12288u + wl - nsub * 128u;
  SB0();
#pragma unroll
  for (int s = 1; s < 5; ++s) issue(s);
  SB0();
  STAMP(1);
  if (w == 0) {
    asm volatile("s_waitcnt lgkmcnt(0)" : "+s"(e0) :: "memory");
    word_set(2, (unsigned)e0); word_set(3, (unsigned)(e0 >> 32));
  }

  bf16x8 xf[32];
  constexpr int NB1 = NJ ? 1 : 2;
  f32x16 acc[3];
  float cs[3] = {0.f, 0.f, 0.f};
#pragma unroll
  for (int b = 0; b < 3; ++b)
#pragma unroll
    for (int i = 0; i < 16; ++i) acc[b][i] = 0.f;
  bf16x8 wf[3][NB1], wg[2][3];
  auto ld1a = [&](int t) {
    const unsigned slot = (unsigned)((t >> 2) % 5) * SLOT1;
    xf[t] = lds_b128(slot + xa[t & 3]);
#pragma unroll
    for (int b = 0; b < NB1; ++b) { const unsigned a = slot + wa + ro[t & 3] + (NJ ? 2 : b) * 256u; wf[t % 3][b] = cat(lds_tr(a), lds_tr(a + 768u)); }
  };
  auto ld1b = [&](int t) {
    const unsigned slot = (unsigned)((8 + (t >> 2)) % 5) * SLOT1;
#pragma unroll
    for (int b = 0; b < 3; ++b) { const int u = NJ * 3 + b; const unsigned a = slot + ((u & 1) ? wbO : wbE) + ro[t & 3] + u * 128u; wg[t & 1][b] = cat(lds_tr(a), lds_tr(a + 768u)); }
  };
  auto cs_put = [&](int nb, int b0) {
#pragma unroll
    for (int b = 0; b < 3; ++b) if (b < nb) { const float v = xhalf_sum(cs[b]); if (lane < 32) *(LAS float*)(lds + L_CSP + (((b0 + b) * 32 + r) * 4 + mi) * 4) = v; }
  };
  auto cs_get = [&](int nb, int b0) {
#pragma unroll
    for (int b = 0; b < 3; ++b) if (b < nb) { const f32x4 c4 = lds_f4(L_CSP + ((b0 + b) * 32 + r) * 16);
      *(LAS float*)(lds + L_CSW + w * 384 + b * 128 + r * 4) = (c4[0] + c4[1]) + (c4[2] + c4[3]); }
  };
  auto piece_at = [&](auto tc) { constexpr int Tg = decltype(tc)::value; constexpr int sg = L::piece_stage(Tg); if constexpr (sg >= 0) piece(sg, Tg - L::TB(sg - 5)); };
  wait_vm<4 * NA>();
  bar();
  e0 = (u64)word_get(2) | ((u64)word_get(3) << 32);
  epoch = e0 + 1ull;
  tg.a = (unsigned)epoch; tg.b = ((unsigned)(e0 >> 32) ^ 0x5bd1e995u) + tg.a;
  STAMP(2); CSTAMP(23);
  ld1a(0); ld1a(1);
  u32x4 sv;
  float rmean = 0.f, rrstd = 0.f;
  const unsigned sv_off = WS_RS + (unsigned)(128 * rb + 32 * mi) * 16u;
  static_for<0, 32>([&](auto tc) {
    constexpr int t = decltype(tc)::value, s = t >> 2, p = t & 3;
    if constexpr (p == 2) {
      if (s == 7) cs_put(NB1, NJ ? 2 : 0);
      wait_vm<L::wait_stage(t, s + 1)>();
      bar();
      STAMP2(24 + s);
      if (s == 7) cs_get(NB1, NJ ? 2 : 0);
      if (s == 1) CSTAMP(60); if (s == 4) CSTAMP(61); if (s == 7) CSTAMP(62);
    }
    if (NJ) piece_at(tc);
    if (t < 30) ld1a(t + 2); else if (t == 31) ld1b(0);
    SB0();
    constexpr int csp = s < 7 ? (NJ ? 3 : 2) : (NJ ? 1 : 0);
#pragma unroll
    for (int b = 0; b < NB1; ++b) { acc[b] = mfma(wf[t % 3][b], xf[t], acc[b]); if (p == csp) csum(cs[b], wf[t % 3][b]); }
    if (t == 2 + NJ) {
      tie(xrow);
      float e[8];
#pragma unroll
      for (int i = 0; i < 4; ++i) { e[2 * i] = bf_lo(xrow[i]); e[2 * i + 1] = bf_hi(xrow[i]); }
      rmean = wave_sum(((e[0] + e[1]) + (e[2] + e[3])) + ((e[4] + e[5]) + (e[6] + e[7]))) * (1.f / 512.f);
      asm volatile("" : "+v"(rmean));
    }
    if (t == 4) {
      float q = 0.f;
#pragma unroll
      for (int i = 0; i < 4; ++i) { const float d0 = bf_lo(xrow[i]) - rmean, d1 = bf_hi(xrow[i]) - rmean; q = fmaf(d0, d0, q); q = fmaf(d1, d1, q); }
      rrstd = __builtin_amdgcn_rsqf(wave_sum(q) * (1.f / 512.f) + 1e-5f);
      asm volatile("" : "+v"(rrstd));
    }
    SB0();
    if (!NJ) piece_at(tc);
    if (t == 22) sv = ld16c_asm(rs_ws, (unsigned)r * 16u, sv_off);
    if (t == 5 && lane == 0) st16c(u32x4{__float_as_uint(rmean), tg.a, __float_as_uint(rrstd), tg.b}, rs_ws, 0u, WS_RS + myrow * 16u);
    SB0();
  });
  STAMP(3); CSTAMP(22);
  { constexpr int after = L::before(32) - L::NST - L::before(23); wait_vm_tie<after>(sv); }
  for (unsigned spins = 0; !__all(sv[1] == tg.a && sv[3] == tg.b) && spins < (1u << 20); ++spins) {
    __builtin_amdgcn_s_sleep(1);
    sv = ld16c_asm(rs_ws, (unsigned)r * 16u, sv_off); wait_vm_tie<0>(sv);
  }
  const float mean = __uint_as_float(sv[0]), rstd = __uint_as_float(sv[2]);
  const int sb = rb * 4 + mi;
#pragma unroll
  for (int b = 0; b < NB1; ++b) {
    const int blk = 3 * g + (NJ ? 2 : b), which = blk >> 4, hp = blk & 15;
    const float sc = which == 0 ? rstd * (0.25f * 1.4426950408889634f) : rstd;
    float v[16];
#pragma unroll
    for (int i = 0; i < 4; ++i) { const f32x4 c4 = lds_f4(L_CSW + w * 384 + b * 128 + (8 * i + 4 * h) * 4);
#pragma unroll
      for (int e = 0; e < 4; ++e) v[4 * i + e] = (acc[b][4 * i + e] - mean * c4[e]) * sc; }
    const unsigned vo = which == 2 ? (unsigned)(h * 512 + ((r ^ (4 * h)) * 16)) : (unsigned)(lane * 16);
#pragma unroll
    for (int hd = 0; hd < 2; ++hd) {
      const u32x4 o = {pk(v[8 * hd], v[8 * hd + 1]), pk(v[8 * hd + 2], v[8 * hd + 3]), pk(v[8 * hd + 4], v[8 * hd + 5]), pk(v[8 * hd + 6], v[8 * hd + 7])};
      st16c(o, rs_ws, vo, WS_QKV + (unsigned)((which * 32 + 2 * hp + hd) * 64 + sb) * 1024u);
    }
  }
  STAMP(4); CSTAMP(18);
#pragma unroll
  for (int b = 0; b < 3; ++b) { cs[b] = 0.f;
#pragma unroll
    for (int i = 0; i < 16; ++i) acc[b][i] = 0.f; }
  bool early = false, act;
  const unsigned qv = qkv_flag_voff(rb2, hp2, lane, act);
  u32x4 fq;
  SB0();
  static_for<32, 64>([&](auto tc) {
    constexpr int Tg = decltype(tc)::value, t = Tg - 32, s = Tg >> 2, p = t & 3;
    if constexpr (p == 3) {
      if (s == 15) cs_put(3, NJ * 3);
      if constexpr (s == 10) wait_vm<L::before(Tg) - L::before(32)>();
      else if constexpr (s == 13) { wait_vm<0>(); if (w == 0) { tie(fq); word_set(0, flags_ok(fq, act, tg) ? 1u : 0u); } }
      else if constexpr (s <= 12) wait_vm<L::wait_stage(Tg, s + 1)>();
      bar();
      STAMP2(24 + s);
      if (s == 8) CSTAMP(56); if (s == 11) CSTAMP(57); if (s == 13) CSTAMP(58); if (s == 15) CSTAMP(59);
      if (s == 13) early = word_get(0) != 0u;
      if (s == 15) cs_get(3, NJ * 3);
    }
    if (NJ) { piece_at(tc); if (Tg >= 55 && Tg < 62 && early) attn_issue(rs_ws, rb2, hp2, w, lane, Tg - 55); }
    if (t < 31) ld1b(t + 1);
    SB0();
    constexpr int csp = s < 15 ? (NJ ? 0 : 3) : (NJ ? 1 : 0);
#pragma unroll
    for (int b = 0; b < 3; ++b) { acc[b] = mfma(wg[t & 1][b], xf[t], acc[b]); if (p == csp) csum(cs[b], wg[t & 1][b]); }
    SB0();
    if (!NJ) { piece_at(tc); if (Tg >= 55 && Tg < 62 && early) attn_issue(rs_ws, rb2, hp2, w, lane, Tg - 55); }
    if (Tg == 43) flag_store(rs_ws, WS_FQKV + (unsigned)((rb * 16 + g) * 64 + w * 8), tg, lane);
    if (Tg == 51 && w == 0) fq = ld16c_asm(rs_ws, qv, 0u);
    SB0();
  });
  STAMP(5); CSTAMP(19);
  const float r2 = rstd * rstd;
#pragma unroll
  for (int b = 0; b < 3; ++b) {
    float v[16];
#pragma unroll
    for (int i = 0; i < 4; ++i) { const f32x4 c4 = lds_f4(L_CSW + w * 384 + b * 128 + (8 * i + 4 * h) * 4);
#pragma unroll
      for (int e = 0; e < 4; ++e) v[4 * i + e] = acc[b][4 * i + e] - mean * c4[e]; }
    float hv[8];
#pragma unroll
    for (int i = 0; i < 8; ++i) hv[i] = fmaxf(v[i], 0.f) * v[8 + i] * r2;
    const u32x4 o = {pk(hv[0], hv[1]), pk(hv[2], hv[3]), pk(hv[4], hv[5]), pk(hv[6], hv[7])};
    if (NJ * 3 + b < nu) st16c(o, rs_ws, (unsigned)lane * 16u, WS_H + (unsigned)(sb * NU + u0 + NJ * 3 + b) * 1024u);
  }
  STAMP(6);
  if (early) { if (NJ == 0 || nu == 6) wait_vm<3>(); else wait_vm<2>(); }
  return early;
}

template <bool FULL, class F>
DI float attn_scores(int j0, int qb, int hd, int lane, f32x16 (&S)[9], F side) {
  const int q = lane & 31, hh = lane >> 5;
  const bf16x8 qf = lds_b128(LA_Q + (unsigned)(hd * 4 + qb) * 1024u + lane * 16u);
  const unsigned kbase = LA_K + (unsigned)(hd * 12 + qb) * 1024u + lane * 16u;
  const float NEG = -__builtin_inff();
  f32x16 zero;
#pragma unroll
  for (int i = 0; i < 16; ++i) zero[i] = 0.f;
  float m = NEG;
#pragma unroll
  for (int j = 0; j < 9; ++j) {
    if (FULL || j >= j0) {
      S[j] = mfma(lds_b128(kbase + j * 1024u), qf, zero);
      if (j == 0) {
#pragma unroll
        for (int i = 0; i < 16; ++i) { const int kl = (i & 3) + 8 * (i >> 2) + 4 * hh; if (kl <= q) S[j][i] = NEG; }
      }
      if (j == 8) {
#pragma unroll
        for (int i = 0; i < 16; ++i) { const int kl = (i & 3) + 8 * (i >> 2) + 4 * hh; if (kl > q) S[j][i] = NEG; }
      }
#pragma unroll
      for (int i = 0; i < 16; i += 2) m = fmaxf(fmaxf(m, S[j][i]), S[j][i + 1]);
    }
    if (j < 8) side(j);
  }
  return xhalf_max(m);
}
template <bool FULL>
DI u32x4 attn_pv(int j0, int qb, int hd, int lane, float m, f32x16 (&S)[9]) {
  const int hh = lane >> 5;
  const int grp = (lane >> 4) & 1, q4 = (lane & 15) >> 2, p4 = lane & 3;
  const unsigned vlane = (unsigned)((p4 & 1) * 512 + (((4 * hh + q4) ^ (4 * (p4 & 1))) * 16) + (p4 >> 1) * 8);
  const unsigned vb0 = grp ? L_ONES + 128u : LA_V + (unsigned)(hd * 12 + qb) * 1024u + vlane;
  const unsigned vb1 = grp ? L_ONES : LA_V + (unsigned)(hd * 12 + qb) * 1024u + vlane + 128u;
  const unsigned vstr = grp ? 0u : 1024u, vs1 = grp ? 0u : 256u;
  f32x16 acc;
#pragma unroll
  for (int i = 0; i < 16; ++i) acc[i] = 0.f;
#pragma unroll
  for (int j = 0; j < 9; ++j) {
    if (FULL || j >= j0) {
      unsigned pp[8];
#pragma unroll
      for (int i = 0; i < 8; ++i) pp[i] = pk(__builtin_amdgcn_exp2f(S[j][2 * i] - m), __builtin_amdgcn_exp2f(S[j][2 * i + 1] - m));
#pragma unroll
      for (int s = 0; s < 2; ++s) {
        const bf16x8 vf = cat(lds_tr(vb0 + j * vstr + s * vs1), lds_tr(vb1 + j * vstr + s * vs1));
        const u32x4 pu = {pp[4 * s], pp[4 * s + 1], pp[4 * s + 2], pp[4 * s + 3]};
        acc = mfma(vf, __builtin_bit_cast(bf16x8, pu), acc);
      }
    }
  }
  const float inv = __builtin_amdgcn_rcpf(acc[8]);
  return u32x4{pk(acc[0] * inv, acc[1] * inv), pk(acc[2] * inv, acc[3] * inv), pk(acc[4] * inv, acc[5] * inv), pk(acc[6] * inv, acc[7] * inv)};
}

DI void phase23(const Ptrs& P, bool early, int rb1, int g1, u64 epoch, int rb, int hp, int rb64, int cb, int rot, int w, int lane, Tag tg, Stamps& T) {
  const rsrc_t rs_ws = mk_rsrc(P.ws, WS_BYTES), rs_d = mk_rsrc(P.wd, FF * DM * 2), rs_o = mk_rsrc(P.wo, DM * DM * 2);
  if (threadIdx.x < 64) *(LAS unsigned*)(lds + L_ONES + threadIdx.x * 4) = ONES2;
  if (!early) {
    bool act; const unsigned qv = qkv_flag_voff(rb, hp, lane, act);
    if (w == 0) flags_spin(rs_ws, qv, act, tg);
    bar();
#pragma unroll
    for (int i = 0; i < 7; ++i) attn_issue(rs_ws, rb, hp, w, lane, i);
    wait_vm<0>();
  }
  bar();
  STAMP(9);
  unsigned vw[2];
#pragma unroll
  for (int j = 0; j < 2; ++j) { const int G = (2 * w + j) * 64 + lane, kg = G >> 5, rr = G & 31;
    vw[j] = (unsigned)(4 * kg + ((rr & 7) >> 1)) * 1024u + (unsigned)((rr >> 3) * 32 + (rr & 1) * 16); }
  const int sbl = w >> 2, sbg = 2 * rb64 + sbl;
  auto chunk = [&](int st) { return st < 10 ? (st + rot >= 10 ? st + rot - 10 : st + rot) : (st == 10 ? 10 : ((st - 11 + rot) & 3)); };
  auto pieceA = [&](int st, int j) {
    const int c = chunk(st), ks = 2 * (w & 3) + j;
    const unsigned src = st < 11 ? WS_H + (unsigned)(sbg * NU + c * 8 + ks) * 1024u : WS_O + (unsigned)(sbg * 32 + c * 8 + ks) * 1024u;
    dma16c(l3a(st & 3) + (unsigned)(sbl * 8 + ks) * 1024u, (unsigned)lane * 16u, rs_ws, src);
  };
  auto pieceW = [&](int st, int j) {
    dma16(l3w(st & 3) + (unsigned)(2 * w + j) * 1024u, vw[j], st < 11 ? rs_d : rs_o, (unsigned)cb * 128u + (unsigned)chunk(st) * 131072u);
  };
  auto side = [&](int j) { pieceW(j >> 1, j & 1); };
  const int qb = w & 3, hd = w >> 2, sb = 4 * rb + qb;
  const unsigned hv = WS_FH + (unsigned)((rb64 >> 1) * 1024 + lane * 16), ov = WS_FO + (unsigned)((rb64 >> 1) * 1024 + lane * 16);
  u32x4 o, fh;
  {
    f32x16 S[9];
    if (sb >= 8) {
      const float m = attn_scores<true>(0, qb, hd, lane, S, side);
      wait_vm<8>(); flag_store(rs_ws, WS_FH + (unsigned)((rb1 * 16 + g1) * 64 + w * 8), tg, lane);
      o = attn_pv<true>(0, qb, hd, lane, m, S);
    } else {
      const float m = attn_scores<false>(8 - sb, qb, hd, lane, S, side);
      wait_vm<8>(); flag_store(rs_ws, WS_FH + (unsigned)((rb1 * 16 + g1) * 64 + w * 8), tg, lane);
      o = attn_pv<false>(8 - sb, qb, hd, lane, m, S);
    }
  }
  if (w == 0) fh = ld16c_asm(rs_ws, hv, 0u);
  st16c(o, rs_ws, (unsigned)lane * 16u, WS_O + (unsigned)(sb * 32 + 2 * hp + hd) * 1024u);
  STAMP(11);
  if (w == 0) { wait_vm_tie<1>(fh); if (!flags_ok(fh, true, tg)) flags_spin(rs_ws, hv, true, tg); }
  bar();
  STAMP(13);
#pragma unroll
  for (int s = 0; s < 4; ++s) { pieceA(s, 0); pieceA(s, 1); }
  const int mb = w & 1, nb2 = (w >> 1) & 1, kh = w >> 2;
  const int h = lane >> 5, nsub = (lane >> 4) & 1, q4 = (lane & 15) >> 2, p4 = lane & 3;
  const unsigned aa = (unsigned)(mb * 8 + kh * 4) * 1024u + lane * 16u;
  const unsigned wa = (unsigned)(16 * kh + h) * 512u + (unsigned)(2 * nb2 + nsub) * 128u + q4 * 32u + p4 * 8u;
  const int row = 64 * rb64 + 32 * mb + (lane & 31);
  f32x16 acc;
#pragma unroll
  for (int i = 0; i < 16; ++i) acc[i] = 0.f;
  bf16x8 af[2][4], wfr[2][4];
  auto ld3 = [&](int st) {
#pragma unroll
    for (int ks = 0; ks < 4; ++ks) { af[st & 1][ks] = lds_b128(l3a(st & 3) + aa + ks * 1024u); const unsigned a = l3w(st & 3) + wa + ks * 2048u; wfr[st & 1][ks] = cat(lds_tr(a), lds_tr(a + 1024u)); }
  };
  wait_vm<6>();
  flag_store(rs_ws, WS_FO + (unsigned)((rb * 16 + hp) * 64 + w * 8), tg, lane);
  bar();
  STAMP(14);
  ld3(0);
  u32x4 fo, fe[4];
  const bool keeper = blockIdx.x == 0 && w == 0;
#pragma unroll
  for (int st = 0; st < 15; ++st) {
    if (st < 14) {
      if (st == 0) wait_vm<5>(); else if (st == 1) wait_vm<7>(); else if (st == 2) wait_vm<9>(); else if (st <= 11) wait_vm<8>(); else if (st == 12) wait_vm<4>(); else wait_vm<0>();
      if (st == 7 && w == 0) { tie(fo); if (!flags_ok(fo, true, tg)) flags_spin(rs_ws, ov, true, tg); }
      bar();
      STAMP2(40 + st);
      ld3(st + 1);
    }
    const bool more = st + 4 < 15;
    SB0();
    acc = mfma(wfr[st & 1][0], af[st & 1][0], acc);
    if (more) pieceA(st + 4, 0);
    acc = mfma(wfr[st & 1][1], af[st & 1][1], acc);
    if (more) pieceA(st + 4, 1);
    if (st != 10 || kh == 0) acc = mfma(wfr[st & 1][2], af[st & 1][2], acc);
    if (more) pieceW(st + 4, 0);
    if (st != 10 || kh == 0) acc = mfma(wfr[st & 1][3], af[st & 1][3], acc);
    if (more) pieceW(st + 4, 1);
    if (st == 4 && w == 0) fo = ld16c_asm(rs_ws, ov, 0u);
    if (st == 11 && keeper) {
#pragma unroll
      for (int i = 0; i < 4; ++i) fe[i] = ld16c_asm(rs_ws, WS_FQKV + (unsigned)(i * 64 + lane) * 64u, 0u);
    }
    SB0();
  }
  STAMP(15);
  const int col0 = 64 * cb + 32 * nb2 + 16 * kh + 4 * h;
  const unsigned short* xr = (const unsigned short*)P.x + (size_t)row * DM + col0;
  const u32x2 x0 = *(const GAS u32x2*)xr, x1 = *(const GAS u32x2*)(xr + 8);
  bar();
  {
    const unsigned base = L3_RED + (unsigned)((w & 3) * 2 + kh) * 2048u + lane * 16u;
    if (kh) { *(LAS f32x4*)(lds + base) = f32x4{acc[0], acc[1], acc[2], acc[3]}; *(LAS f32x4*)(lds + base + 1024u) = f32x4{acc[4], acc[5], acc[6], acc[7]}; }
    else { *(LAS f32x4*)(lds + base) = f32x4{acc[8], acc[9], acc[10], acc[11]}; *(LAS f32x4*)(lds + base + 1024u) = f32x4{acc[12], acc[13], acc[14], acc[15]}; }
  }
  bar();
  {
    const unsigned base = L3_RED + (unsigned)((w & 3) * 2 + (kh ^ 1)) * 2048u + lane * 16u;
    const f32x4 a0 = lds_f4(base), a1 = lds_f4(base + 1024u);
    unsigned short* orow = (unsigned short*)P.out + (size_t)row * DM + col0;
    float v[8];
#pragma unroll
    for (int e = 0; e < 4; ++e) { v[e] = (kh ? acc[8 + e] : acc[e]) + a0[e]; v[4 + e] = (kh ? acc[12 + e] : acc[4 + e]) + a1[e]; }
    v[0] += bf_lo(x0[0]); v[1] += bf_hi(x0[0]); v[2] += bf_lo(x0[1]); v[3] += bf_hi(x0[1]);
    v[4] += bf_lo(x1[0]); v[5] += bf_hi(x1[0]); v[6] += bf_lo(x1[1]); v[7] += bf_hi(x1[1]);
    *(GAS u32x2*)orow = u32x2{pk(v[0], v[1]), pk(v[2], v[3])};
    *(GAS u32x2*)(orow + 8) = u32x2{pk(v[4], v[5]), pk(v[6], v[7])};
  }
  if (keeper) {
#pragma unroll
    for (int i = 0; i < 4; ++i) { tie(fe[i]); if (!flags_ok(fe[i], true, tg)) flags_spin(rs_ws, WS_FQKV + (unsigned)(i * 64 + lane) * 64u, true, tg); }
    if (lane == 0) __hip_atomic_store((GAS u64*)(P.ws + WS_EPOCH), epoch, __ATOMIC_RELAXED, __HIP_MEMORY_SCOPE_AGENT);
  }
}

__global__ __launch_bounds__(512, 2) void mk_kernel(const void* x, const void* wqkv, const void* wo, const void* wg, const void* wu, const void* wd, void* out, char* ws) {
  const Ptrs P{x, wqkv, wo, wg, wu, wd, out, ws};
  Stamps T;
  STAMP(0); CSTAMP(20);
  const int b = blockIdx.x, xcd = b & 7, idx = b >> 3;
  const int w = __builtin_amdgcn_readfirstlane(threadIdx.x >> 6), lane = threadIdx.x & 63;
  Tag tg; u64 epoch;
  const int rb1 = (xcd >> 2) * 8 + (idx >> 2), g1 = (xcd & 3) * 4 + (idx & 3), rot1 = ((idx >> 2) + 2 * (idx & 3)) & 7;
  const int rb2 = idx & 15, hp2 = xcd * 2 + (idx >> 4);
  bool early;
  if (w < 4) early = phase1<0>(P, rb1, g1, rot1, w, lane, rb2, hp2, tg, epoch, T); else early = phase1<1>(P, rb1, g1, rot1, w - 4, lane, rb2, hp2, tg, epoch, T);
  const int rot3 = ((idx & 7) + 3 * (idx >> 3)) % 10;
  phase23(P, early, rb1, g1, epoch, rb2, hp2, xcd * 4 + (idx >> 3), idx & 7, rot3, w, lane, tg, T);
}

}

extern "C" void kernel_launch(void* const* d_in, const int* in_sizes, int n_in, void* d_out, int out_size, void* d_ws, size_t ws_size, hipStream_t stream) {
  const void *x = d_in[0], *wqkv = d_in[1], *wo = d_in[2], *wg = d_in[4], *wu = d_in[5], *wd = d_in[6];
  mk_kernel<<<256, 512, 0, stream>>>(x, wqkv, wo, wg, wu, wd, d_out, (char*)d_ws);
}
```

```cpp
#include <hip/hip_runtime.h>
#include <hip/hip_bf16.h>
#include <stdint.h>

namespace {

typedef short bf16x8 __attribute__((ext_vector_type(8)));
typedef short s16x4 __attribute__((ext_vector_type(4)));
typedef float f32x16 __attribute__((ext_vector_type(16)));
typedef float f32x4 __attribute__((ext_vector_type(4)));
typedef float f32x2 __attribute__((ext_vector_type(2)));
typedef unsigned u32x4 __attribute__((ext_vector_type(4)));
typedef unsigned u32x2 __attribute__((ext_vector_type(2)));
typedef __bf16 bf16x2_t __attribute__((ext_vector_type(2)));
typedef short v4i16_t __attribute__((ext_vector_type(4)));
typedef __amdgpu_buffer_rsrc_t rsrc_t;
#define LAS __attribute__((address_space(3)))
#define GAS __attribute__((address_space(1)))
#define DI __device__ __forceinline__
#define SB0() __builtin_amdgcn_sched_barrier(0)
typedef unsigned long long u64;

constexpr int SEQ = 2048, DM = 512, FF = 1376, NU = 86;
constexpr unsigned WS_EPOCH = 0;
constexpr unsigned WS_FQKV = 16384, WS_FH = 32768, WS_FO = 49152;
constexpr unsigned WS_RS = 65536;
constexpr unsigned WS_QKV = 1u << 20;
constexpr unsigned WS_H = 7u << 20;
constexpr unsigned WS_O = 13u << 20;
constexpr unsigned WS_BYTES = 16u << 20;
constexpr unsigned SLOT1 = 28672;
constexpr unsigned L_CSP = 143360;
constexpr unsigned L_CSW = 146432;
constexpr unsigned L_ONES = 163328;
constexpr unsigned L_WORD = 163584;
constexpr unsigned LDS_BYTES = 163840;
constexpr unsigned LA_Q = 28672, LA_K = LA_Q + 8192, LA_V = LA_K + 24576;
DI constexpr unsigned l3w(int i) { return i == 0 ? 0u : 86016u + (unsigned)(i - 1) * 16384u; }
DI constexpr unsigned l3a(int i) { return i == 0 ? 135168u : 28672u + (unsigned)(i - 1) * 16384u; }
constexpr unsigned L3_RED = 0;

__shared__ __attribute__((aligned(1024))) char lds[LDS_BYTES];

DI unsigned pk(float a, float b) { f32x2 v = {a, b}; bf16x2_t r = __builtin_convertvector(v, bf16x2_t); return __builtin_bit_cast(unsigned, r); }
DI float dot2(unsigned a, unsigned b, float c) { return __builtin_amdgcn_fdot2_f32_bf16(__builtin_bit_cast(bf16x2_t, a), __builtin_bit_cast(bf16x2_t, b), c, false); }
DI rsrc_t mk_rsrc(const void* p, unsigned bytes) { return __builtin_amdgcn_make_buffer_rsrc((void*)p, 0, bytes, 0x00020000); }
DI void dma16(unsigned ldsaddr, unsigned voff, rsrc_t rs, unsigned soff) {
  asm volatile("s_mov_b32 m0, %0\n\ts_nop 0\n\tbuffer_load_dwordx4 %1, %2, %3 offen lds" ::"s"(ldsaddr), "v"(voff), "s"(rs), "s"(soff) : "memory");
}
DI void dma16c(unsigned ldsaddr, unsigned voff, rsrc_t rs, unsigned soff) {
  asm volatile("s_mov_b32 m0, %0\n\ts_nop 0\n\tbuffer_load_dwordx4 %1, %2, %3 offen sc1 lds" ::"s"(ldsaddr), "v"(voff), "s"(rs), "s"(soff) : "memory");
}
DI void pf_lines(unsigned ldsaddr, unsigned voff, rsrc_t rs) {
  asm volatile("s_mov_b32 m0, %0\n\ts_nop 0\n\tbuffer_load_dword %1, %2, 0 offen lds" ::"s"(ldsaddr), "v"(voff), "s"(rs) : "memory");
}
template <int N> DI void wait_vm() { asm volatile("s_waitcnt vmcnt(%0)" ::"n"(N) : "memory"); }
template <int N> DI void wait_vm_tie(u32x4& v) { asm volatile("s_waitcnt vmcnt(%1)" : "+v"(v) : "n"(N) : "memory"); }
template <int V> struct IC { static constexpr int value = V; };
template <int I, int N, class F> DI void static_for(F&& f) { if constexpr (I < N) { f(IC<I>{}); static_for<I + 1, N>(f); } }
DI void tie(u32x4& v) { asm volatile("" : "+v"(v) :: "memory"); }
DI void bar() { asm volatile("s_waitcnt lgkmcnt(0)\n\ts_barrier" ::: "memory"); }
DI f32x16 mfma(bf16x8 a, bf16x8 b, f32x16 c) { return __builtin_amdgcn_mfma_f32_32x32x16_bf16(a, b, c, 0, 0, 0); }
DI bf16x8 lds_b128(unsigned a) { return *(const LAS bf16x8*)(lds + a); }
#define MK_REAL 1
DI s16x4 lds_tr(unsigned a) { return __builtin_amdgcn_ds_read_tr16_b64_v4i16((LAS v4i16_t*)(lds + a)); }
DI f32x4 mfma16(bf16x8 a, bf16x8 b, f32x4 c) { return __builtin_amdgcn_mfma_f32_16x16x32_bf16(a, b, c, 0, 0, 0); }
DI f32x4 lds_f4(unsigned a) { return *(const LAS f32x4*)(lds + a); }
DI bf16x8 cat(s16x4 lo, s16x4 hi) { return __builtin_shufflevector(lo, hi, 0, 1, 2, 3, 4, 5, 6, 7); }
DI float xhalf_sum(float v) { auto r = __builtin_amdgcn_permlane32_swap(__float_as_uint(v), __float_as_uint(v), false, false); return __uint_as_float(r[0]) + __uint_as_float(r[1]); }
DI float xhalf_max(float v) { auto r = __builtin_amdgcn_permlane32_swap(__float_as_uint(v), __float_as_uint(v), false, false); return fmaxf(__uint_as_float(r[0]), __uint_as_float(r[1])); }
DI void st16c(u32x4 v, rsrc_t rs, unsigned voff, unsigned soff) { __builtin_amdgcn_raw_buffer_store_b128(v, rs, voff + soff, 0, 16); }
DI void st8c(u32x2 v, rsrc_t rs, unsigned voff, unsigned soff) { __builtin_amdgcn_raw_buffer_store_b64(v, rs, voff, soff, 16); }
constexpr unsigned ONES2 = 0x3F803F80u;
template <int CTRL> DI float dpp_add(float v) { return v + __int_as_float(__builtin_amdgcn_update_dpp(0, __float_as_int(v), CTRL, 0xf, 0xf, true)); }
DI float wave_sum(float v) {
  v = dpp_add<0xB1>(v); v = dpp_add<0x4E>(v); v = dpp_add<0x141>(v); v = dpp_add<0x140>(v);
  auto a = __builtin_amdgcn_permlane16_swap(__float_as_uint(v), __float_as_uint(v), false, false);
  v = __uint_as_float(a[0]) + __uint_as_float(a[1]);
  return xhalf_sum(v);
}
DI float bf_lo(unsigned u) { return __uint_as_float(u << 16); }
DI float bf_hi(unsigned u) { return __uint_as_float(u & 0xffff0000u); }
DI u32x4 ld16_asm(rsrc_t rs, unsigned voff, unsigned soff) { u32x4 v; asm volatile("buffer_load_dwordx4 %0, %1, %2, %3 offen" : "=v"(v) : "v"(voff), "s"(rs), "s"(soff) : "memory"); return v; }
DI u32x4 ld16c_asm(rsrc_t rs, unsigned voff, unsigned soff) { u32x4 v; asm volatile("buffer_load_dwordx4 %0, %1, %2, %3 offen sc1" : "=v"(v) : "v"(voff), "s"(rs), "s"(soff) : "memory"); return v; }
#define STAMP2(i) do {} while (0)
struct Stamps {};
#define STAMP(i) do {} while (0)
#define CSTAMP(i) do {} while (0)
#define NOTE(i, v) do {} while (0)

struct Ptrs { const void *x, *wqkv, *wo, *wg, *wu, *wd; void* out; char* ws; u64 pc; };
struct Tag { unsigned a, b; };

DI void flag_store(rsrc_t rs_ws, unsigned off, Tag tg, int lane) { if (lane == 0) st8c(u32x2{tg.a, tg.b}, rs_ws, 0u, off); }
DI bool flags_ok(u32x4 v, bool act, Tag tg) { return __all(!act || (v[0] == tg.a && v[1] == tg.b && v[2] == tg.a && v[3] == tg.b)); }
DI void flags_spin(rsrc_t rs_ws, unsigned voff, bool act, Tag tg) {
  for (unsigned spins = 0; spins < (1u << 20); ++spins) {
    u32x4 v = ld16c_asm(rs_ws, voff, 0u); wait_vm_tie<0>(v);
    if (flags_ok(v, act, tg)) break;
    __builtin_amdgcn_s_sleep(1);
  }
}
DI void word_set(int i, unsigned v) { *(volatile LAS unsigned*)(lds + L_WORD + i * 4) = v; }
DI unsigned word_get(int i) { return __builtin_amdgcn_readfirstlane(*(volatile LAS unsigned*)(lds + L_WORD + i * 4)); }

struct AttnSrc { unsigned q, kv; int sb0; };
DI AttnSrc attn_src(int rb, int hp, int w) {
  AttnSrc a;
  a.q = __builtin_amdgcn_readfirstlane(WS_QKV + (unsigned)((2 * hp + (w >> 2)) * 64 + 4 * rb + (w & 3)) * 1024u);
  a.kv = __builtin_amdgcn_readfirstlane(WS_QKV + (unsigned)(((1 + (w >> 2)) * 32 + 2 * hp + ((w >> 1) & 1)) * 64) * 1024u);
  a.sb0 = __builtin_amdgcn_readfirstlane(4 * rb - 8 + 6 * (w & 1));
  return a;
}
DI void attn_issue(rsrc_t rs_ws, const AttnSrc& a, int w, int lane, int piece, bool go) {
  const unsigned vo = (unsigned)lane * 16u + (go ? 0u : 0x40000000u);
  if (piece == 0) { dma16c(LA_Q + (unsigned)w * 1024u, vo, rs_ws, a.q); return; }
  const int sbk = a.sb0 + piece - 1;
  dma16c(LA_K + (unsigned)((w >> 1) * 12 + 6 * (w & 1) + piece - 1) * 1024u, vo, rs_ws, a.kv + (unsigned)(sbk < 0 ? 0 : sbk) * 1024u);
}
DI unsigned qkv_flag_voff(int rb, int hp, int lane, bool& act) {
  const int c = lane >> 2, kind = c == 0 ? 0 : (c <= 3 ? 1 : 2), d = c == 0 ? 0 : (c - 1) % 3, rr = rb - d;
  act = lane < 28 && rr >= 0;
  return act ? WS_FQKV + (unsigned)((rr * 16 + (kind * 16 + hp) / 3) * 64 + (lane & 3) * 16) : 0u;
}

constexpr unsigned L_DUMMY = 152576;
#define P1_HOLD 12
constexpr int OP_NONE = -1, OP_PFW = 200, OP_ATT = 400;
template <int NJ> struct Sched1 {
  static constexpr int NA = NJ ? 4 : 3, NST = NJ ? 2 : 4;
  static constexpr int n(int sg) { return sg < 8 ? NA : 3; }
  static constexpr int TB(int s) { return s < 8 ? 4 * s + 2 : 4 * s + 3; }
  static constexpr int extras(int T) { return (T == 5 ? 1 : 0)   + (T == 22 ? 1 : 0)   + (T == 39 ? 1 : 0)  ; }
  static constexpr int NPRO = 4;
  static constexpr int PRO = 1 + NPRO * NA + 1;
  static constexpr int HOLD = P1_HOLD;
  int op[64][2] = {}, before[65] = {}, last[16] = {};
  constexpr Sched1() {
    int sg = NPRO, j = 0, pf = 0, att = 0, c = PRO;
    last[0] = 1 + NA;
    for (int i = 1; i < NPRO; ++i) last[i] = 1 + NA + 1 + i * NA;
    for (int T = 0; T < 64; ++T) {
      before[T] = c;
      for (int k = 0; k < 2; ++k) {
        int o = OP_NONE;
        if (k == 0 || T < 14) {
          if (sg <= 15 && (sg < 5 || T >= TB(sg - 5)) && (sg < 8 || T >= 4 * sg - HOLD)) { o = sg * 8 + j; if (++j == n(sg)) { last[sg] = c + 1; ++sg; j = 0; } }
          else if (pf == 0 && T >= 4) o = OP_PFW + pf++;
        }
        if (T >= 59 && att < 7 && (k == 0 || T >= 61)) o = OP_ATT + att++;
        op[T][k] = o;
        if (o != OP_NONE) ++c;
      }
      c += extras(T);
      if (T == 31) c += NST;
    }
    before[64] = c;
  }
  constexpr int wait_stage(int T, int sg) const { return before[T] - last[sg]; }
};

template <int NJ>
DI bool phase1(const Ptrs& P, int rb, int g, int rot, int mi, int lane, int rb2, int hp2, Tag& tg, u64& epoch, Stamps& T_) {
  Stamps& T = T_;
  typedef Sched1<NJ> L;
  static constexpr Sched1<NJ> SC{};
  const int w = NJ * 4 + mi;
  u64 e0 = 0;
  if (w == 0) asm volatile("s_load_dwordx2 %0, %1, 0x0" : "=s"(e0) : "s"(P.ws) : "memory");
  const rsrc_t rs_x = mk_rsrc(P.x, SEQ * DM * 2), rs_q = mk_rsrc(P.wqkv, DM * 1536 * 2);
  const rsrc_t rs_f = mk_rsrc(NJ ? P.wu : P.wg, DM * FF * 2), rs_ws = mk_rsrc(P.ws, WS_BYTES);
  const unsigned sx0 = (unsigned)rb * 131072u, sq0 = (unsigned)g * 192u;
  constexpr int NQ = NJ ? 2 : 1;
  constexpr int NA = 2 + NQ;
  unsigned vx[2], vq[2], vf[3];
#pragma unroll
  for (int j = 0; j < 2; ++j) vx[j] = (unsigned)(16 * w + 8 * j + (lane >> 3)) * 1024u + (unsigned)(((lane & 7) ^ (4 * j + (lane >> 4))) * 16);
#pragma unroll
  for (int j = 0; j < NQ; ++j) { const int ins = NJ ? 2 * mi + j : 8 + mi; const int G = ins * 64 + lane;
    vq[j] = (unsigned)(G / 12) * 3072u + (unsigned)(G % 12) * 16u; }
  const int u0 = (g * NU) >> 4, nu = (((g + 1) * NU) >> 4) - u0;
  const unsigned sf0 = (unsigned)u0 * 32u;
  auto piece = [&](int gs, int j) {
    const unsigned slot = (unsigned)(gs % 5) * SLOT1;
    const unsigned c = (unsigned)(gs + rot) & 7u;
    if (gs < 8) {
      if (j < 2) dma16(slot + (unsigned)(2 * w + j) * 1024u, vx[j], rs_x, sx0 + c * 128u);
      else dma16(slot + 16384u + (unsigned)(NJ ? 2 * mi + j - 2 : 8 + mi) * 1024u, vq[j - 2], rs_q, sq0 + c * (64u * 3072u));
    } else dma16(slot + NJ * 12288u + (unsigned)(3 * mi + j) * 1024u, vf[j], rs_f, sf0 + c * (64u * 2752u));
  };
#pragma unroll
  for (int j = 0; j < NA; ++j) piece(0, j);
  const unsigned myrow = (unsigned)(128 * rb + 8 * g + w);
  u32x4 xrow = ld16_asm(rs_x, (unsigned)lane * 16u, myrow * 1024u);
#pragma unroll
  for (int sg = 1; sg < L::NPRO; ++sg)
#pragma unroll
    for (int j = 0; j < NA; ++j) piece(sg, j);
  SB0();
  STAMP(1);
#pragma unroll
  for (int j = 0; j < 3; ++j) { const int G = (3 * mi + j) * 64 + lane, kg = G / 48, rr = G % 48, unit = (rr >> 3) ^ NJ;
    vf[j] = unit < nu ? (unsigned)(4 * kg + ((rr & 7) >> 1)) * 2752u + (unsigned)(unit * 32 + (rr & 1) * 16) : 0x40000000u; }
  const int r = lane & 31, h = lane >> 5, nsub = (lane >> 4) & 1, q4 = (lane & 15) >> 2, p4 = lane & 3;
  unsigned xa[4], ro[4];
#pragma unroll
  for (int p = 0; p < 4; ++p) { const int ks = (p + mi) & 3;
    xa[p] = (unsigned)(32 * mi + r) * 128u + (unsigned)(((2 * ks + h) ^ ((r >> 1) & 7)) * 16);
    ro[p] = (unsigned)ks * 3072u; }
  const unsigned wl = (unsigned)(2 * h) * 768u + q4 * 32u + p4 * 8u;
  const unsigned wa = 16384u + (unsigned)(2 * h) * 768u + q4 * 192u + nsub * 32u + p4 * 8u;
  const unsigned wbE = nsub * 12288u + wl + nsub * 128u;
  const unsigned wbO = nsub * 12288u + wl - nsub * 128u;
  const AttnSrc asrc = attn_src(rb2, hp2, w);
  bool early = false;
  auto warm = [&](int i) {
    const unsigned xl = blockIdx.x >> 3, dst = L_DUMMY + (unsigned)w * 256u;
    if (i == 0) {
      const unsigned q = (unsigned)(w & 1) * 64u + (unsigned)lane, rr = q / 7u, ln = q - rr * 7u;
      const unsigned ub = (unsigned)((((blockIdx.x & 3) * 4 * NU) >> 4) * 32) & ~127u;
      pf_lines(dst, (w < 4 && q < 112u) ? (16u * xl + rr) * 2752u + ub + ln * 128u : 0x40000000u, mk_rsrc((w & 2) ? P.wu : P.wg, DM * FF * 2));
    } else if (i == 1) {
      pf_lines(dst, lane < 43 ? ((xl * 8u + (unsigned)w) * 43u + (unsigned)lane) * 128u : 0x40000000u, mk_rsrc(P.wd, FF * DM * 2));
    } else {
      pf_lines(dst, lane < 16 ? ((xl * 8u + (unsigned)w) * 16u + (unsigned)lane) * 128u : 0x40000000u, mk_rsrc(P.wo, DM * DM * 2));
    }
  };
  auto op_at = [&](auto tc, auto kc) {
    constexpr int o = SC.op[decltype(tc)::value][decltype(kc)::value];
    if constexpr (o >= OP_ATT) attn_issue(rs_ws, asrc, w, lane, o - OP_ATT, early);
    else if constexpr (o >= OP_PFW) warm(o - OP_PFW);
    else if constexpr (o >= 0) piece(o >> 3, o & 7);
  };
  bf16x8 sel;
  { const unsigned v = (lane & 15) == ((lane >> 4) & 1) ? ONES2 : 0u; sel = __builtin_bit_cast(bf16x8, u32x4{v, v, v, v}); }
  if (w == 0) {
    asm volatile("s_waitcnt lgkmcnt(0)" : "+s"(e0) :: "memory");
    word_set(2, (unsigned)e0); word_set(3, (unsigned)(e0 >> 32));
  }

  bf16x8 xf[32];
  constexpr int NB1 = NJ ? 1 : 2;
  f32x16 acc[3];
  f32x4 cs[3];
#pragma unroll
  for (int b = 0; b < 3; ++b) { cs[b] = f32x4{0.f, 0.f, 0.f, 0.f};
#pragma unroll
    for (int i = 0; i < 16; ++i) acc[b][i] = 0.f; }
  bf16x8 wf[3][NB1], wg[2][3];
  auto ldx = [&](int t) { xf[t] = lds_b128((unsigned)((t >> 2) % 5) * SLOT1 + xa[t & 3]); };
  auto ld1a = [&](int t, int b) {
    const unsigned a = (unsigned)((t >> 2) % 5) * SLOT1 + wa + ro[t & 3] + (NJ ? 2 : b) * 64u; wf[t % 3][b] = cat(lds_tr(a), lds_tr(a + 768u));
  };
  auto ld1b = [&](int t, int b) {
    const int u = NJ * 3 + b;
    const unsigned a = (unsigned)((8 + (t >> 2)) % 5) * SLOT1 + ((u & 1) ? wbO : wbE) + ro[t & 3] + u * 128u; wg[t & 1][b] = cat(lds_tr(a), lds_tr(a + 768u));
  };
  auto cs_put = [&](int nb, int b0) {
#pragma unroll
    for (int b = 0; b < 3; ++b) if (b < nb && lane < 16) { *(LAS float*)(lds + L_CSP + (((b0 + b) * 32 + lane) * 4 + mi) * 4) = cs[b][0]; *(LAS float*)(lds + L_CSP + (((b0 + b) * 32 + 16 + lane) * 4 + mi) * 4) = cs[b][1]; }
  };
  auto cs_get = [&](int nb, int b0) {
#pragma unroll
    for (int b = 0; b < 3; ++b) if (b < nb) { const f32x4 c4 = lds_f4(L_CSP + ((b0 + b) * 32 + r) * 16);
      *(LAS float*)(lds + L_CSW + w * 384 + b * 128 + r * 4) = (c4[0] + c4[1]) + (c4[2] + c4[3]); }
  };
  wait_vm<(L::NPRO - 1) * NA>();
  bar();
  e0 = (u64)word_get(2) | ((u64)word_get(3) << 32);
  epoch = e0 + 1ull;
  tg.a = (unsigned)epoch; tg.b = ((unsigned)(e0 >> 32) ^ 0x5bd1e995u) + tg.a;
  STAMP(2); CSTAMP(23);
  ldx(0); ldx(1);
#pragma unroll
  for (int b = 0; b < NB1; ++b) { ld1a(0, b); ld1a(1, b); }
  u32x4 sv;
  float rmean = 0.f, rrstd = 0.f;
  const unsigned sv_off = WS_RS + (unsigned)(128 * rb + 32 * mi) * 16u;
  static_for<0, 32>([&](auto tc) {
    constexpr int t = decltype(tc)::value, s = t >> 2, p = t & 3;
    if constexpr (p == 2) {
      if (s == 7) cs_put(NB1, NJ ? 2 : 0);
      wait_vm<SC.wait_stage(t, s + 1)>();
      bar();
      STAMP2(24 + s);
      if (s == 7) cs_get(NB1, NJ ? 2 : 0);
      if (s == 1) CSTAMP(60); if (s == 4) CSTAMP(61); if (s == 7) CSTAMP(62);
    }
    if (NJ) op_at(tc, IC<0>{});
    static_for<0, NB1>([&](auto bc) {
      constexpr int b = decltype(bc)::value;
      acc[b] = mfma(wf[t % 3][b], xf[t], acc[b]);
      if (t < 30) { if (b == 0) ldx(t + 2); ld1a(t + 2, b); }
      if (t == 31) { ld1b(0, b); if (b == NB1 - 1) { for (int bb = NB1; bb < 3; ++bb) ld1b(0, bb); } }
      if (p == (NJ ? 1 : b)) cs[b] = mfma16(sel, wf[t % 3][b], cs[b]);
      if (b == 0) {
        if (t == 2 + NJ) {
          tie(xrow);
          float e[8];
#pragma unroll
          for (int i = 0; i < 4; ++i) { e[2 * i] = bf_lo(xrow[i]); e[2 * i + 1] = bf_hi(xrow[i]); }
          rmean = wave_sum(((e[0] + e[1]) + (e[2] + e[3])) + ((e[4] + e[5]) + (e[6] + e[7]))) * (1.f / 512.f);
          asm volatile("" : "+v"(rmean));
        }
        if (t == 4) {
          float q = 0.f;
#pragma unroll
          for (int i = 0; i < 4; ++i) { const float d0 = bf_lo(xrow[i]) - rmean, d1 = bf_hi(xrow[i]) - rmean; q = fmaf(d0, d0, q); q = fmaf(d1, d1, q); }
          rrstd = __builtin_amdgcn_rsqf(wave_sum(q) * (1.f / 512.f) + 1e-5f);
          asm volatile("" : "+v"(rrstd));
        }
      }
      SB0();
      if (b == 0) op_at(tc, IC<NJ ? 1 : 0>{});
    });
    if (!NJ) op_at(tc, IC<1>{});
    if (t == 22) sv = ld16c_asm(rs_ws, (unsigned)r * 16u, sv_off);
    if (t == 5 && lane == 0) st16c(u32x4{__float_as_uint(rmean), tg.a, __float_as_uint(rrstd), tg.b}, rs_ws, 0u, WS_RS + myrow * 16u);
    SB0();
  });
  STAMP(3); CSTAMP(22);
  { constexpr int after = SC.before[32] - L::NST - SC.before[23]; wait_vm_tie<after>(sv); }
  NOTE(53, __all(sv[1] == tg.a && sv[3] == tg.b) ? 1 : 2);
  for (unsigned spins = 0; !__all(sv[1] == tg.a && sv[3] == tg.b) && spins < (1u << 20); ++spins) {
    __builtin_amdgcn_s_sleep(1);
    sv = ld16c_asm(rs_ws, (unsigned)r * 16u, sv_off); wait_vm_tie<0>(sv);
  }
  const float mean = __uint_as_float(sv[0]), rstd = __uint_as_float(sv[2]);
  const int sb = rb * 4 + mi;
#pragma unroll
  for (int b = 0; b < NB1; ++b) {
    const int blk = 3 * g + (NJ ? 2 : b), which = blk >> 4, hp = blk & 15;
    const float sc = which == 0 ? rstd * (0.25f * 1.4426950408889634f) : rstd;
    float v[16];
#pragma unroll
    for (int i = 0; i < 4; ++i) { const f32x4 c4 = lds_f4(L_CSW + w * 384 + b * 128 + (8 * i + 4 * h) * 4);
#pragma unroll
      for (int e = 0; e < 4; ++e) v[4 * i + e] = (acc[b][4 * i + e] - mean * c4[e]) * sc; }
    const unsigned vo = which == 2 ? (unsigned)(h * 512 + ((r ^ (4 * h)) * 16)) : (unsigned)(lane * 16);
#pragma unroll
    for (int hd = 0; hd < 2; ++hd) {
      const u32x4 o = {pk(v[8 * hd], v[8 * hd + 1]), pk(v[8 * hd + 2], v[8 * hd + 3]), pk(v[8 * hd + 4], v[8 * hd + 5]), pk(v[8 * hd + 6], v[8 * hd + 7])};
      st16c(o, rs_ws, vo, WS_QKV + (unsigned)((which * 32 + 2 * hp + hd) * 64 + sb) * 1024u);
    }
  }
  STAMP(4); CSTAMP(18);
#pragma unroll
  for (int b = 0; b < 3; ++b) { cs[b] = f32x4{0.f, 0.f, 0.f, 0.f};
#pragma unroll
    for (int i = 0; i < 16; ++i) acc[b][i] = 0.f; }
  bool act;
  const unsigned qv = qkv_flag_voff(rb2, hp2, lane, act);
  u32x4 fq;
  SB0();
  static_for<32, 64>([&](auto tc) {
    constexpr int Tg = decltype(tc)::value, t = Tg - 32, s = Tg >> 2, p = t & 3;
    if constexpr (p == 3) {
      if (s == 15) cs_put(3, NJ * 3);
      if constexpr (s == 9) wait_vm<(SC.before[Tg] - SC.before[32] < SC.wait_stage(Tg, 10) ? SC.before[Tg] - SC.before[32] : SC.wait_stage(Tg, 10))>();
      else if constexpr (s == 14) { wait_vm<0>(); if (w == 0) { tie(fq); word_set(0, flags_ok(fq, act, tg) ? 1u : 0u); } }
      else if constexpr (s <= 13) wait_vm<SC.wait_stage(Tg, s + 1)>();
      bar();
      STAMP2(24 + s);
      if (s == 8) CSTAMP(56); if (s == 11) CSTAMP(57); if (s == 13) CSTAMP(58); if (s == 15) CSTAMP(59);
      if (s == 14) early = word_get(0) != 0u;
      if (s == 15) cs_get(3, NJ * 3);
    }
    if (NJ) op_at(tc, IC<0>{});
    static_for<0, 3>([&](auto bc) {
      constexpr int b = decltype(bc)::value;
      acc[b] = mfma(wg[t & 1][b], xf[t], acc[b]);
      if (t < 31) ld1b(t + 1, b);
      if (p == ((b + 2 * NJ) & 3) || (s == 15 && p == 2 && ((b + 2 * NJ) & 3) == 3)) { if (!(s == 15 && p == 3)) cs[b] = mfma16(sel, wg[t & 1][b], cs[b]); }
      SB0();
    });
    if (!NJ) op_at(tc, IC<0>{});
    op_at(tc, IC<1>{});
    if (Tg == 39) flag_store(rs_ws, WS_FQKV + (unsigned)((rb * 16 + g) * 64 + w * 8), tg, lane);
    if (Tg == 55 && w == 0) fq = ld16c_asm(rs_ws, qv, 0u);
    SB0();
  });
  STAMP(5); CSTAMP(19);
  const float r2 = rstd * rstd;
#pragma unroll
  for (int b = 0; b < 3; ++b) {
    float v[16];
#pragma unroll
    for (int i = 0; i < 4; ++i) { const f32x4 c4 = lds_f4(L_CSW + w * 384 + b * 128 + (8 * i + 4 * h) * 4);
#pragma unroll
      for (int e = 0; e < 4; ++e) v[4 * i + e] = acc[b][4 * i + e] - mean * c4[e]; }
    float hv[8];
#pragma unroll
    for (int i = 0; i < 8; ++i) hv[i] = fmaxf(v[i], 0.f) * v[8 + i] * r2;
    const u32x4 o = {pk(hv[0], hv[1]), pk(hv[2], hv[3]), pk(hv[4], hv[5]), pk(hv[6], hv[7])};
    if (NJ * 3 + b < nu) st16c(o, rs_ws, (unsigned)lane * 16u, WS_H + (unsigned)(sb * NU + u0 + NJ * 3 + b) * 1024u);
  }
  STAMP(6);
  if (NJ == 0 || nu == 6) wait_vm<3>(); else wait_vm<2>();
  return early;
}

template <bool FULL, class F1, class FM, class F2>
DI u32x4 attn_task(int j0, int qb, int hd, int lane, F1 side1, FM mid, F2 side2) {
  const int q = lane & 31, hh = lane >> 5, tq = q - 4 * hh;
  const bf16x8 qf = lds_b128(LA_Q + (unsigned)(hd * 4 + qb) * 1024u + lane * 16u);
  const unsigned kbase = LA_K + (unsigned)(hd * 12 + qb) * 1024u + lane * 16u;
  const float NEG = -__builtin_inff();
  bf16x8 kf[9];
#pragma unroll
  for (int j = 0; j < 9; ++j) if (FULL || j >= j0) kf[j] = lds_b128(kbase + j * 1024u);
  f32x16 c;
#pragma unroll
  for (int i = 0; i < 16; ++i) c[i] = 0.f;
  float m;
  {
    const f32x16 s8 = mfma(kf[8], qf, c);
    f32x16 s0;
    if (FULL) s0 = mfma(kf[0], qf, c);
    m = NEG;
#pragma unroll
    for (int i = 0; i < 16; i += 2) {
      const int c0 = (i & 3) + 8 * (i >> 2);
      const float a = c0 <= tq ? s8[i] : (FULL ? s0[i] : NEG), b = c0 + 1 <= tq ? s8[i + 1] : (FULL ? s0[i + 1] : NEG);
      m = fmaxf(fmaxf(m, a), b);
    }
    side1(0);
  }
#pragma unroll
  for (int j = 1; j < 8; ++j) {
    if (FULL || j >= j0) {
      const f32x16 s = mfma(kf[j], qf, c);
#pragma unroll
      for (int i = 0; i < 16; i += 2) m = fmaxf(fmaxf(m, s[i]), s[i + 1]);
    }
    side1(j);
  }
  m = xhalf_max(m);
  mid();
#pragma unroll
  for (int i = 0; i < 16; ++i) c[i] = -m;
  const int grp = (lane >> 4) & 1, q4 = (lane & 15) >> 2, p4 = lane & 3;
  const unsigned vlane = (unsigned)((p4 & 1) * 512 + (((4 * hh + q4) ^ (4 * (p4 & 1))) * 16) + (p4 >> 1) * 8);
  const unsigned vb0 = grp ? L_ONES + 128u : LA_V + (unsigned)(hd * 12 + qb) * 1024u + vlane;
  const unsigned vb1 = grp ? L_ONES : LA_V + (unsigned)(hd * 12 + qb) * 1024u + vlane + 128u;
  const unsigned vstr = grp ? 0u : 1024u, vs1 = grp ? 0u : 256u;
  f32x16 acc;
#pragma unroll
  for (int i = 0; i < 16; ++i) acc[i] = 0.f;
  auto pv = [&](int j, const unsigned (&pp)[8]) {
#pragma unroll
    for (int s = 0; s < 2; ++s) {
      const bf16x8 vf = cat(lds_tr(vb0 + j * vstr + s * vs1), lds_tr(vb1 + j * vstr + s * vs1));
      const u32x4 pu = {pp[4 * s], pp[4 * s + 1], pp[4 * s + 2], pp[4 * s + 3]};
      acc = mfma(vf, __builtin_bit_cast(bf16x8, pu), acc);
    }
  };
#pragma unroll
  for (int j = 1; j < 8; ++j) {
    if (FULL || j >= j0) {
      const f32x16 s = mfma(kf[j], qf, c);
      unsigned pp[8];
#pragma unroll
      for (int i = 0; i < 8; ++i) pp[i] = pk(__builtin_amdgcn_exp2f(s[2 * i]), __builtin_amdgcn_exp2f(s[2 * i + 1]));
      pv(j, pp);
    }
    side2(j);
  }
  {
    const f32x16 s8 = mfma(kf[8], qf, c);
    f32x16 s0;
    if (FULL) s0 = mfma(kf[0], qf, c);
    unsigned p8[8], p0[8];
#pragma unroll
    for (int i = 0; i < 8; ++i) {
      const int c0 = ((2 * i) & 3) + 8 * (i >> 1);
      const bool va = c0 <= tq, vb = c0 + 1 <= tq;
      const float a = va ? s8[2 * i] : (FULL ? s0[2 * i] : NEG), b = vb ? s8[2 * i + 1] : (FULL ? s0[2 * i + 1] : NEG);
      const unsigned pp = pk(__builtin_amdgcn_exp2f(a), __builtin_amdgcn_exp2f(b));
      const unsigned mk = (va ? 0xffffu : 0u) | (vb ? 0xffff0000u : 0u);
      p8[i] = pp & mk; p0[i] = pp & ~mk;
    }
    pv(8, p8);
    if (FULL) pv(0, p0);
  }
  const float inv = __builtin_amdgcn_rcpf(acc[8]);
  return u32x4{pk(acc[0] * inv, acc[1] * inv), pk(acc[2] * inv, acc[3] * inv), pk(acc[4] * inv, acc[5] * inv), pk(acc[6] * inv, acc[7] * inv)};
}

struct Sched3 {
  static constexpr int a_stage(int st) { return st <= 6 ? st + 4 : (st >= 8 && st <= 11 ? st + 3 : -1); }
  static constexpr int w_stage(int st) { return st <= 10 ? st + 4 : -1; }
  int before[16] = {}, lastA[15] = {}, lastW[15] = {};
  constexpr Sched3() {
    int c = 9;
    for (int i = 0; i < 4; ++i) { lastA[i] = 2 * i + 2; lastW[i] = 0; }
    for (int st = 0; st < 15; ++st) {
      before[st] = c;
      if (st == 3) c += 1;
      if (a_stage(st) >= 0) { c += 2; lastA[a_stage(st)] = c; }
      if (w_stage(st) >= 0) { c += 2; lastW[w_stage(st)] = c; }
      if (st == 8) c += 2;
    }
    before[15] = c;
  }
  constexpr int wait(int st) const { return before[st] - (lastA[st + 1] > lastW[st + 1] ? lastA[st + 1] : lastW[st + 1]); }
};

DI void phase23(const Ptrs& P, bool early, int rb1, int g1, u64 epoch, int rb, int hp, int rb64, int cb, int rot, int w, int lane, Tag tg, Stamps& T) {
  const rsrc_t rs_ws = mk_rsrc(P.ws, WS_BYTES), rs_d = mk_rsrc(P.wd, FF * DM * 2), rs_o = mk_rsrc(P.wo, DM * DM * 2);
  if (threadIdx.x < 64) *(LAS unsigned*)(lds + L_ONES + threadIdx.x * 4) = ONES2;
  NOTE(63, early ? 1 : 2);
  if (!early) {
    bool act; const unsigned qv = qkv_flag_voff(rb, hp, lane, act);
    if (w == 0) flags_spin(rs_ws, qv, act, tg);
    bar();
    const AttnSrc asrc = attn_src(rb, hp, w);
#pragma unroll
    for (int i = 0; i < 7; ++i) attn_issue(rs_ws, asrc, w, lane, i, true);
    wait_vm<0>();
  }
  bar();
  STAMP(9);
  unsigned vw[2];
#pragma unroll
  for (int j = 0; j < 2; ++j) { const int G = (2 * w + j) * 64 + lane, row = G >> 3;
    vw[j] = (unsigned)row * 1024u + (unsigned)(((G & 7) ^ (((row >> 1) & 1) << 2)) * 16); }
  const int sbl = w >> 2, sbg = 2 * rb64 + sbl;
  auto chunk = [&](int st) { return st < 10 ? (st + rot >= 10 ? st + rot - 10 : st + rot) : (st == 10 ? 10 : ((st - 11 + rot) & 3)); };
  auto pieceA = [&](int st, int j, bool go) {
    const int c = chunk(st), ks = 2 * (w & 3) + j;
    const unsigned src = st < 11 ? WS_H + (unsigned)(sbg * NU + c * 8 + ks) * 1024u : WS_O + (unsigned)(sbg * 32 + c * 8 + ks) * 1024u;
    dma16c(l3a(st & 3) + (unsigned)(sbl * 8 + ks) * 1024u, (unsigned)lane * 16u + (go ? 0u : 0x40000000u), rs_ws, src);
  };
  auto pieceW = [&](int st, int j) {
    dma16(l3w(st & 3) + (unsigned)(2 * w + j) * 1024u, vw[j], st < 11 ? rs_d : rs_o, (unsigned)cb * 128u + (unsigned)chunk(st) * 131072u);
  };
  auto side = [&](int j) { pieceW(j >> 1, j & 1); };
  const int qb = w & 3, hd = w >> 2, sb = 4 * rb + qb;
  const unsigned hv = WS_FH + (unsigned)((rb64 >> 1) * 1024 + lane * 16), ov = WS_FO + (unsigned)((rb64 >> 1) * 1024 + lane * 16);
  u32x4 o, fh;
  auto side2 = [&](int j) { if (j == 7) fh = ld16c_asm(rs_ws, hv, 0u); };
  auto mid = [&]() { STAMP(10); wait_vm<8>(); flag_store(rs_ws, WS_FH + (unsigned)((rb1 * 16 + g1) * 64 + w * 8), tg, lane); };
  if (sb >= 8) o = attn_task<true>(0, qb, hd, lane, side, mid, side2); else o = attn_task<false>(8 - sb, qb, hd, lane, side, mid, side2);
  STAMP(11);
  wait_vm_tie<0>(fh);
  NOTE(55, flags_ok(fh, true, tg) ? 1 : 2);
  if (!flags_ok(fh, true, tg)) flags_spin(rs_ws, hv, true, tg);
  pieceA(0, 0, true); pieceA(0, 1, true);
  bar();
  STAMP(13);
#pragma unroll
  for (int s = 1; s < 4; ++s) { pieceA(s, 0, true); pieceA(s, 1, true); }
  st16c(o, rs_ws, (unsigned)lane * 16u, WS_O + (unsigned)(sb * 32 + 2 * hp + hd) * 1024u);
  const int mb = w & 1, nb2 = (w >> 1) & 1, kh = w >> 2;
  const int h = lane >> 5, nsub = (lane >> 4) & 1, q4 = (lane & 15) >> 2, p4 = lane & 3;
  const unsigned aa = (unsigned)(mb * 8 + kh * 4) * 1024u + lane * 16u;
  const unsigned wa = (unsigned)(16 * kh + h) * 512u + q4 * 128u + (unsigned)((nb2 ^ (q4 >> 1)) * 64) + nsub * 32u + p4 * 8u;
  const int row = 64 * rb64 + 32 * mb + (lane & 31);
  f32x16 acc;
#pragma unroll
  for (int i = 0; i < 16; ++i) acc[i] = 0.f;
  bf16x8 af[2][4], wfr[2][4];
  auto ld3 = [&](int st, int ks) {
    af[st & 1][ks] = lds_b128(l3a(st & 3) + aa + ks * 1024u); const unsigned a = l3w(st & 3) + wa + ks * 2048u; wfr[st & 1][ks] = cat(lds_tr(a), lds_tr(a + 1024u));
  };
  const int col0 = 64 * cb + 32 * nb2 + 16 * kh + 4 * h;
  wait_vm<7>();
  bar();
  STAMP(14);
#pragma unroll
  for (int ks = 0; ks < 4; ++ks) ld3(0, ks);
  u32x4 fo, fe[4];
  const unsigned short* xr = (const unsigned short*)P.x + (size_t)row * DM + col0;
  u32x2 x0, x1;
  const bool keeper = blockIdx.x == 0 && w == 0;
  static constexpr Sched3 S3{};
  static_for<0, 15>([&](auto sc) {
    constexpr int st = decltype(sc)::value;
    if constexpr (st < 14) {
      wait_vm<S3.wait(st)>();
      if (st == 8 && w == 0) { wait_vm_tie<2>(fo); NOTE(54, flags_ok(fo, true, tg) ? 1 : 2); if (!flags_ok(fo, true, tg)) flags_spin(rs_ws, ov, true, tg); }
      bar();
      STAMP2(40 + st);
    }
    if (st == 3) flag_store(rs_ws, WS_FO + (unsigned)((rb * 16 + hp) * 64 + w * 8), tg, lane);
    SB0();
#pragma unroll
    for (int ks = 0; ks < 4; ++ks) {
      if (ks < 2 || st != 10 || kh == 0) acc = mfma(wfr[st & 1][ks], af[st & 1][ks], acc);
      if (st < 14) ld3(st + 1, ks);
      SB0();
      if (ks < 2) { if (Sched3::a_stage(st) >= 0) pieceA(Sched3::a_stage(st), ks, true); }
      else if (Sched3::w_stage(st) >= 0) pieceW(Sched3::w_stage(st), ks - 2);
      SB0();
    }
    if (st == 6 && w == 0) fo = ld16c_asm(rs_ws, ov, 0u);
    if (st == 8) { x0 = *(const GAS u32x2*)xr; x1 = *(const GAS u32x2*)(xr + 8); }
    if (st == 9 && keeper) {
#pragma unroll
      for (int i = 0; i < 4; ++i) fe[i] = ld16c_asm(rs_ws, WS_FQKV + (unsigned)(i * 64 + lane) * 64u, 0u);
    }
    SB0();
  });
  STAMP(15);
  bar();
  {
    const unsigned base = L3_RED + (unsigned)((w & 3) * 2 + kh) * 2048u + lane * 16u;
    if (kh) { *(LAS f32x4*)(lds + base) = f32x4{acc[0], acc[1], acc[2], acc[3]}; *(LAS f32x4*)(lds + base + 1024u) = f32x4{acc[4], acc[5], acc[6], acc[7]}; }
    else { *(LAS f32x4*)(lds + base) = f32x4{acc[8], acc[9], acc[10], acc[11]}; *(LAS f32x4*)(lds + base + 1024u) = f32x4{acc[12], acc[13], acc[14], acc[15]}; }
  }
  bar();
  {
    const unsigned base = L3_RED + (unsigned)((w & 3) * 2 + (kh ^ 1)) * 2048u + lane * 16u;
    const f32x4 a0 = lds_f4(base), a1 = lds_f4(base + 1024u);
    unsigned short* orow = (unsigned short*)P.out + (size_t)row * DM + col0;
    float v[8];
#pragma unroll
    for (int e = 0; e < 4; ++e) { v[e] = (kh ? acc[8 + e] : acc[e]) + a0[e]; v[4 + e] = (kh ? acc[12 + e] : acc[4 + e]) + a1[e]; }
    v[0] += bf_lo(x0[0]); v[1] += bf_hi(x0[0]); v[2] += bf_lo(x0[1]); v[3] += bf_hi(x0[1]);
    v[4] += bf_lo(x1[0]); v[5] += bf_hi(x1[0]); v[6] += bf_lo(x1[1]); v[7] += bf_hi(x1[1]);
    *(GAS u32x2*)orow = u32x2{pk(v[0], v[1]), pk(v[2], v[3])};
    *(GAS u32x2*)(orow + 8) = u32x2{pk(v[4], v[5]), pk(v[6], v[7])};
  }
  if (keeper) {
#pragma unroll
    for (int i = 0; i < 4; ++i) { tie(fe[i]); if (!flags_ok(fe[i], true, tg)) flags_spin(rs_ws, WS_FQKV + (unsigned)(i * 64 + lane) * 64u, true, tg); }
    if (lane == 0) __hip_atomic_store((GAS u64*)(P.ws + WS_EPOCH), epoch, __ATOMIC_RELAXED, __HIP_MEMORY_SCOPE_AGENT);
  }
}

__global__ __launch_bounds__(512, 2) void mk_kernel(const void* x, const void* wqkv, const void* wo, const void* wg, const void* wu, const void* wd, void* out, char* ws) {
  u64 pc0 = 0;
  asm volatile("mk_code_begin:\n\ts_getpc_b64 %0" : "=s"(pc0));
  {
    unsigned len;
    asm volatile("s_mov_b32 %0, mk_code_end-mk_code_begin" : "=s"(len));
    const unsigned wv = __builtin_amdgcn_readfirstlane(threadIdx.x >> 6);
    const unsigned per = ((len >> 5) + 1023u) & ~1023u;
    const unsigned o = (blockIdx.x >> 3) * per + wv * 1024u;
    dma16(L_DUMMY + wv * 1024u, (threadIdx.x & 63u) * 16u, mk_rsrc((const void*)(pc0 & ~255ull), len & ~15u), wv * 1024u < per ? o : 0x40000000u);
  }
  const Ptrs P{x, wqkv, wo, wg, wu, wd, out, ws, pc0};
  Stamps T;
  STAMP(0); CSTAMP(20);
  __builtin_amdgcn_s_setprio(1);
  const int b = blockIdx.x, xcd = b & 7, idx = b >> 3;
  const int w = __builtin_amdgcn_readfirstlane(threadIdx.x >> 6), lane = threadIdx.x & 63;
  Tag tg; u64 epoch;
  const int rb = 2 * (idx >> 2) + (xcd >> 2), g = (xcd & 3) * 4 + (idx & 3), rot1 = ((idx >> 2) + 2 * (idx & 3)) & 7;
  bool early;
  if (w < 4) early = phase1<0>(P, rb, g, rot1, w, lane, rb, g, tg, epoch, T); else early = phase1<1>(P, rb, g, rot1, w - 4, lane, rb, g, tg, epoch, T);
  const int rot3 = ((idx & 7) + 3 * (idx >> 3)) % 10;
  phase23(P, early, rb, g, epoch, rb, g, xcd * 4 + (idx >> 3), idx & 7, rot3, w, lane, tg, T);
  asm volatile("mk_code_end:");
}

}

extern "C" void kernel_launch(void* const* d_in, const int* in_sizes, int n_in, void* d_out, int out_size, void* d_ws, size_t ws_size, hipStream_t stream) {
  const void *x = d_in[0], *wqkv = d_in[1], *wo = d_in[2], *wg = d_in[4], *wu = d_in[5], *wd = d_in[6];
  mk_kernel<<<256, 512, 0, stream>>>(x, wqkv, wo, wg, wu, wd, d_out, (char*)d_ws);
}
```

```cpp
#include <hip/hip_runtime.h>
#include <hip/hip_bf16.h>
#include <stdint.h>

namespace {

typedef short bf16x8 __attribute__((ext_vector_type(8)));
typedef short s16x4 __attribute__((ext_vector_type(4)));
typedef float f32x16 __attribute__((ext_vector_type(16)));
typedef float f32x4 __attribute__((ext_vector_type(4)));
typedef float f32x2 __attribute__((ext_vector_type(2)));
typedef unsigned u32x4 __attribute__((ext_vector_type(4)));
typedef unsigned u32x2 __attribute__((ext_vector_type(2)));
typedef __bf16 bf16x2_t __attribute__((ext_vector_type(2)));
typedef short v4i16_t __attribute__((ext_vector_type(4)));
typedef __amdgpu_buffer_rsrc_t rsrc_t;
#define LAS __attribute__((address_space(3)))
#define GAS __attribute__((address_space(1)))
#define DI __device__ __forceinline__
#define SB0() __builtin_amdgcn_sched_barrier(0)
typedef unsigned long long u64;

constexpr int SEQ = 2048, DM = 512, FF = 1376, NU = 86;
constexpr unsigned WS_EPOCH = 0;
constexpr unsigned WS_FQKV = 16384, WS_FH = 32768, WS_FO = 49152;
constexpr unsigned WS_RS = 65536;
constexpr unsigned WS_QKV = 1u << 20;
constexpr unsigned WS_H = 7u << 20;
constexpr unsigned WS_O = 13u << 20;
constexpr unsigned WS_BYTES = 16u << 20;
constexpr unsigned SLOT1 = 28672;
constexpr unsigned L_CSP = 143360;
constexpr unsigned L_CSW = 146432;
constexpr unsigned L_ONES = 163328;
constexpr unsigned L_WORD = 163584;
constexpr unsigned LDS_BYTES = 163840;
constexpr unsigned LA_Q = 28672, LA_K = LA_Q + 8192, LA_V = LA_K + 24576;
DI constexpr unsigned l3w(int i) { return i == 0 ? 0u : 86016u + (unsigned)(i - 1) * 16384u; }
DI constexpr unsigned l3a(int i) { return i == 0 ? 135168u : 28672u + (unsigned)(i - 1) * 16384u; }
constexpr unsigned L3_RED = 0;

__shared__ __attribute__((aligned(1024))) char lds[LDS_BYTES];

DI unsigned pk(float a, float b) { f32x2 v = {a, b}; bf16x2_t r = __builtin_convertvector(v, bf16x2_t); return __builtin_bit_cast(unsigned, r); }
DI float dot2(unsigned a, unsigned b, float c) { return __builtin_amdgcn_fdot2_f32_bf16(__builtin_bit_cast(bf16x2_t, a), __builtin_bit_cast(bf16x2_t, b), c, false); }
DI rsrc_t mk_rsrc(const void* p, unsigned bytes) { return __builtin_amdgcn_make_buffer_rsrc((void*)p, 0, bytes, 0x00020000); }
DI void dma16(unsigned ldsaddr, unsigned voff, rsrc_t rs, unsigned soff) {
  asm volatile("s_mov_b32 m0, %0\n\ts_nop 0\n\tbuffer_load_dwordx4 %1, %2, %3 offen lds" ::"s"(ldsaddr), "v"(voff), "s"(rs), "s"(soff) : "memory");
}
DI void dma16c(unsigned ldsaddr, unsigned voff, rsrc_t rs, unsigned soff) {
  asm volatile("s_mov_b32 m0, %0\n\ts_nop 0\n\tbuffer_load_dwordx4 %1, %2, %3 offen sc1 lds" ::"s"(ldsaddr), "v"(voff), "s"(rs), "s"(soff) : "memory");
}
DI void pf_lines(unsigned ldsaddr, unsigned voff, rsrc_t rs) {
  asm volatile("s_mov_b32 m0, %0\n\ts_nop 0\n\tbuffer_load_dword %1, %2, 0 offen lds" ::"s"(ldsaddr), "v"(voff), "s"(rs) : "memory");
}
template <int N> DI void wait_vm() { asm volatile("s_waitcnt vmcnt(%0)" ::"n"(N) : "memory"); }
template <int N> DI void wait_vm_tie(u32x4& v) { asm volatile("s_waitcnt vmcnt(%1)" : "+v"(v) : "n"(N) : "memory"); }
template <int V> struct IC { static constexpr int value = V; };
template <int I, int N, class F> DI void static_for(F&& f) { if constexpr (I < N) { f(IC<I>{}); static_for<I + 1, N>(f); } }
DI void tie(u32x4& v) { asm volatile("" : "+v"(v) :: "memory"); }
DI void bar() { asm volatile("s_waitcnt lgkmcnt(0)\n\ts_barrier" ::: "memory"); }
DI f32x16 mfma(bf16x8 a, bf16x8 b, f32x16 c) { return __builtin_amdgcn_mfma_f32_32x32x16_bf16(a, b, c, 0, 0, 0); }
DI bf16x8 lds_b128(unsigned a) { return *(const LAS bf16x8*)(lds + a); }
#define MK_REAL 1
DI s16x4 lds_tr(unsigned a) { return __builtin_amdgcn_ds_read_tr16_b64_v4i16((LAS v4i16_t*)(lds + a)); }
DI f32x4 mfma16(bf16x8 a, bf16x8 b, f32x4 c) { return __builtin_amdgcn_mfma_f32_16x16x32_bf16(a, b, c, 0, 0, 0); }
DI f32x4 lds_f4(unsigned a) { return *(const LAS f32x4*)(lds + a); }
DI bf16x8 cat(s16x4 lo, s16x4 hi) { return __builtin_shufflevector(lo, hi, 0, 1, 2, 3, 4, 5, 6, 7); }
DI float xhalf_sum(float v) { auto r = __builtin_amdgcn_permlane32_swap(__float_as_uint(v), __float_as_uint(v), false, false); return __uint_as_float(r[0]) + __uint_as_float(r[1]); }
DI float xhalf_max(float v) { auto r = __builtin_amdgcn_permlane32_swap(__float_as_uint(v), __float_as_uint(v), false, false); return fmaxf(__uint_as_float(r[0]), __uint_as_float(r[1])); }
DI void st16c(u32x4 v, rsrc_t rs, unsigned voff, unsigned soff) { __builtin_amdgcn_raw_buffer_store_b128(v, rs, voff + soff, 0, 16); }
DI void st8c(u32x2 v, rsrc_t rs, unsigned voff, unsigned soff) { __builtin_amdgcn_raw_buffer_store_b64(v, rs, voff, soff, 16); }
constexpr unsigned ONES2 = 0x3F803F80u;
template <int CTRL> DI float dpp_add(float v) { return v + __int_as_float(__builtin_amdgcn_update_dpp(0, __float_as_int(v), CTRL, 0xf, 0xf, true)); }
DI float wave_sum(float v) {
  v = dpp_add<0xB1>(v); v = dpp_add<0x4E>(v); v = dpp_add<0x141>(v); v = dpp_add<0x140>(v);
  auto a = __builtin_amdgcn_permlane16_swap(__float_as_uint(v), __float_as_uint(v), false, false);
  v = __uint_as_float(a[0]) + __uint_as_float(a[1]);
  return xhalf_sum(v);
}
DI float bf_lo(unsigned u) { return __uint_as_float(u << 16); }
DI float bf_hi(unsigned u) { return __uint_as_float(u & 0xffff0000u); }
DI u32x4 ld16_asm(rsrc_t rs, unsigned voff, unsigned soff) { u32x4 v; asm volatile("buffer_load_dwordx4 %0, %1, %2, %3 offen" : "=v"(v) : "v"(voff), "s"(rs), "s"(soff) : "memory"); return v; }
DI u32x4 ld16c_asm(rsrc_t rs, unsigned voff, unsigned soff) { u32x4 v; asm volatile("buffer_load_dwordx4 %0, %1, %2, %3 offen sc1" : "=v"(v) : "v"(voff), "s"(rs), "s"(soff) : "memory"); return v; }
#define STAMP2(i) do {} while (0)
struct Stamps {};
#define STAMP(i) do {} while (0)
#define CSTAMP(i) do {} while (0)
#define NOTE(i, v) do {} while (0)

struct Ptrs { const void *x, *wqkv, *wo, *wg, *wu, *wd; void* out; char* ws; u64 pc; };
struct Tag { unsigned a, b; };

DI void flag_store(rsrc_t rs_ws, unsigned off, Tag tg, int lane) { if (lane == 0) st8c(u32x2{tg.a, tg.b}, rs_ws, 0u, off); }
DI bool flags_ok(u32x4 v, bool act, Tag tg) { return __all(!act || (v[0] == tg.a && v[1] == tg.b && v[2] == tg.a && v[3] == tg.b)); }
DI void flags_spin(rsrc_t rs_ws, unsigned voff, bool act, Tag tg) {
  for (unsigned spins = 0; spins < (1u << 20); ++spins) {
    u32x4 v = ld16c_asm(rs_ws, voff, 0u); wait_vm_tie<0>(v);
    if (flags_ok(v, act, tg)) break;
    __builtin_amdgcn_s_sleep(1);
  }
}
DI void word_set(int i, unsigned v) { *(volatile LAS unsigned*)(lds + L_WORD + i * 4) = v; }
DI unsigned word_get(int i) { return __builtin_amdgcn_readfirstlane(*(volatile LAS unsigned*)(lds + L_WORD + i * 4)); }

struct AttnSrc { unsigned q, kv; int sb0; };
DI AttnSrc attn_src(int rb, int hp, int w) {
  AttnSrc a;
  a.q = __builtin_amdgcn_readfirstlane(WS_QKV + (unsigned)((2 * hp + (w >> 2)) * 64 + 4 * rb + (w & 3)) * 1024u);
  a.kv = __builtin_amdgcn_readfirstlane(WS_QKV + (unsigned)(((1 + (w >> 2)) * 32 + 2 * hp + ((w >> 1) & 1)) * 64) * 1024u);
  a.sb0 = __builtin_amdgcn_readfirstlane(4 * rb - 8 + 6 * (w & 1));
  return a;
}
DI void attn_issue(rsrc_t rs_ws, const AttnSrc& a, int w, int lane, int piece, bool go) {
  const unsigned vo = (unsigned)lane * 16u + (go ? 0u : 0x40000000u);
  if (piece == 0) { dma16c(LA_Q + (unsigned)w * 1024u, vo, rs_ws, a.q); return; }
  const int sbk = a.sb0 + piece - 1;
  dma16c(LA_K + (unsigned)((w >> 1) * 12 + 6 * (w & 1) + piece - 1) * 1024u, vo, rs_ws, a.kv + (unsigned)(sbk < 0 ? 0 : sbk) * 1024u);
}
DI unsigned qkv_flag_voff(int rb, int hp, int lane, bool& act) {
  const int c = lane >> 2, kind = c == 0 ? 0 : (c <= 3 ? 1 : 2), d = c == 0 ? 0 : (c - 1) % 3, rr = rb - d;
  act = lane < 28 && rr >= 0;
  return act ? WS_FQKV + (unsigned)((rr * 16 + (kind * 16 + hp) / 3) * 64 + (lane & 3) * 16) : 0u;
}

constexpr unsigned L_DUMMY = 152576;
#define TLBW_N 0
#define P1_HOLD 12
constexpr int OP_NONE = -1, OP_PFW = 200, OP_ATT = 400;
template <int NJ> struct Sched1 {
  static constexpr int NA = NJ ? 4 : 3, NST = NJ ? 2 : 4;
  static constexpr int n(int sg) { return sg < 8 ? NA : 3; }
  static constexpr int TB(int s) { return s < 8 ? 4 * s + 2 : 4 * s + 3; }
  static constexpr int extras(int T) { return (T == 5 ? 1 : 0)   + (T == 22 ? 1 : 0)   + (T == 39 ? 1 : 0)  ; }
  static constexpr int NPRO = 4;
  static constexpr int NTW = TLBW_N;
  static constexpr int PRO = 1 + NPRO * NA + 1 + NTW;
  static constexpr int HOLD = P1_HOLD;
  int op[64][2] = {}, before[65] = {}, last[16] = {};
  constexpr Sched1() {
    int sg = NPRO, j = 0, pf = 0, att = 0, c = PRO;
    last[0] = 1 + NA;
    for (int i = 1; i < NPRO; ++i) last[i] = 1 + NA + 1 + NTW + i * NA;
    for (int T = 0; T < 64; ++T) {
      before[T] = c;
      for (int k = 0; k < 2; ++k) {
        int o = OP_NONE;
        if (k == 0 || T < 14) {
          if (sg <= 15 && (sg < 5 || T >= TB(sg - 5)) && (sg < 8 || T >= 4 * sg - HOLD)) { o = sg * 8 + j; if (++j == n(sg)) { last[sg] = c + 1; ++sg; j = 0; } }
          else if (pf == 0 && T >= 4) o = OP_PFW + pf++;
        }
        if (T >= 59 && att < 7 && (k == 0 || T >= 61)) o = OP_ATT + att++;
        op[T][k] = o;
        if (o != OP_NONE) ++c;
      }
      c += extras(T);
      if (T == 31) c += NST;
    }
    before[64] = c;
  }
  constexpr int wait_stage(int T, int sg) const { return before[T] - last[sg]; }
};

template <int NJ>
DI bool phase1(const Ptrs& P, int rb, int g, int rot, int mi, int lane, int rb2, int hp2, Tag& tg, u64& epoch, Stamps& T_) {
  Stamps& T = T_;
  typedef Sched1<NJ> L;
  static constexpr Sched1<NJ> SC{};
  const int w = NJ * 4 + mi;
  u64 e0 = 0;
  if (w == 0) asm volatile("s_load_dwordx2 %0, %1, 0x0" : "=s"(e0) : "s"(P.ws) : "memory");
  const rsrc_t rs_x = mk_rsrc(P.x, SEQ * DM * 2), rs_q = mk_rsrc(P.wqkv, DM * 1536 * 2);
  const rsrc_t rs_f = mk_rsrc(NJ ? P.wu : P.wg, DM * FF * 2), rs_ws = mk_rsrc(P.ws, WS_BYTES);
  const unsigned sx0 = (unsigned)rb * 131072u, sq0 = (unsigned)g * 192u;
  constexpr int NQ = NJ ? 2 : 1;
  constexpr int NA = 2 + NQ;
  unsigned vx[2], vq[2], vf[3];
#pragma unroll
  for (int j = 0; j < 2; ++j) vx[j] = (unsigned)(16 * w + 8 * j + (lane >> 3)) * 1024u + (unsigned)(((lane & 7) ^ (4 * j + (lane >> 4))) * 16);
#pragma unroll
  for (int j = 0; j < NQ; ++j) { const int ins = NJ ? 2 * mi + j : 8 + mi; const int G = ins * 64 + lane;
    vq[j] = (unsigned)(G / 12) * 3072u + (unsigned)(G % 12) * 16u; }
  const int u0 = (g * NU) >> 4, nu = (((g + 1) * NU) >> 4) - u0;
  const unsigned sf0 = (unsigned)u0 * 32u;
  auto piece = [&](int gs, int j) {
    const unsigned slot = (unsigned)(gs % 5) * SLOT1;
    const unsigned c = (unsigned)(gs + rot) & 7u;
    if (gs < 8) {
      if (j < 2) dma16(slot + (unsigned)(2 * w + j) * 1024u, vx[j], rs_x, sx0 + c * 128u);
      else dma16(slot + 16384u + (unsigned)(NJ ? 2 * mi + j - 2 : 8 + mi) * 1024u, vq[j - 2], rs_q, sq0 + c * (64u * 3072u));
    } else dma16(slot + NJ * 12288u + (unsigned)(3 * mi + j) * 1024u, vf[j], rs_f, sf0 + c * (64u * 2752u));
  };
#pragma unroll
  for (int j = 0; j < NA; ++j) piece(0, j);
  const unsigned myrow = (unsigned)(128 * rb + 8 * g + w);
  u32x4 xrow = ld16_asm(rs_x, (unsigned)lane * 16u, myrow * 1024u);
#pragma unroll
  for (int sg = 1; sg < L::NPRO; ++sg)
#pragma unroll
    for (int j = 0; j < NA; ++j) piece(sg, j);
  SB0();
  STAMP(1);
#pragma unroll
  for (int j = 0; j < 3; ++j) { const int G = (3 * mi + j) * 64 + lane, kg = G / 48, rr = G % 48, unit = (rr >> 3) ^ NJ;
    vf[j] = unit < nu ? (unsigned)(4 * kg + ((rr & 7) >> 1)) * 2752u + (unsigned)(unit * 32 + (rr & 1) * 16) : 0x40000000u; }
  const int r = lane & 31, h = lane >> 5, nsub = (lane >> 4) & 1, q4 = (lane & 15) >> 2, p4 = lane & 3;
  unsigned xa[4], ro[4];
#pragma unroll
  for (int p = 0; p < 4; ++p) { const int ks = (p + mi) & 3;
    xa[p] = (unsigned)(32 * mi + r) * 128u + (unsigned)(((2 * ks + h) ^ ((r >> 1) & 7)) * 16);
    ro[p] = (unsigned)ks * 3072u; }
  const unsigned wl = (unsigned)(2 * h) * 768u + q4 * 32u + p4 * 8u;
  const unsigned wa = 16384u + (unsigned)(2 * h) * 768u + q4 * 192u + nsub * 32u + p4 * 8u;
  const unsigned wbE = nsub * 12288u + wl + nsub * 128u;
  const unsigned wbO = nsub * 12288u + wl - nsub * 128u;
  const AttnSrc asrc = attn_src(rb2, hp2, w);
  bool early = false;
  auto warm = [&](int i) {
    const unsigned xl = blockIdx.x >> 3, dst = L_DUMMY + (unsigned)w * 256u;
    if (i == 0) {
      const unsigned q = (unsigned)(w & 1) * 64u + (unsigned)lane, rr = q / 7u, ln = q - rr * 7u;
      const unsigned ub = (unsigned)((((blockIdx.x & 3) * 4 * NU) >> 4) * 32) & ~127u;
      pf_lines(dst, (w < 4 && q < 112u) ? (16u * xl + rr) * 2752u + ub + ln * 128u : 0x40000000u, mk_rsrc((w & 2) ? P.wu : P.wg, DM * FF * 2));
    } else if (i == 1) {
      pf_lines(dst, lane < 43 ? ((xl * 8u + (unsigned)w) * 43u + (unsigned)lane) * 128u : 0x40000000u, mk_rsrc(P.wd, FF * DM * 2));
    } else {
      pf_lines(dst, lane < 16 ? ((xl * 8u + (unsigned)w) * 16u + (unsigned)lane) * 128u : 0x40000000u, mk_rsrc(P.wo, DM * DM * 2));
    }
  };
  auto op_at = [&](auto tc, auto kc) {
    constexpr int o = SC.op[decltype(tc)::value][decltype(kc)::value];
    if constexpr (o >= OP_ATT) attn_issue(rs_ws, asrc, w, lane, o - OP_ATT, early);
    else if constexpr (o >= OP_PFW) warm(o - OP_PFW);
    else if constexpr (o >= 0) piece(o >> 3, o & 7);
  };
  bf16x8 sel;
  { const unsigned v = (lane & 15) == ((lane >> 4) & 1) ? ONES2 : 0u; sel = __builtin_bit_cast(bf16x8, u32x4{v, v, v, v}); }
  if (w == 0) {
    asm volatile("s_waitcnt lgkmcnt(0)" : "+s"(e0) :: "memory");
    word_set(2, (unsigned)e0); word_set(3, (unsigned)(e0 >> 32));
  }

  bf16x8 xf[32];
  constexpr int NB1 = NJ ? 1 : 2;
  f32x16 acc[3];
  f32x4 cs[3];
#pragma unroll
  for (int b = 0; b < 3; ++b) { cs[b] = f32x4{0.f, 0.f, 0.f, 0.f};
#pragma unroll
    for (int i = 0; i < 16; ++i) acc[b][i] = 0.f; }
  bf16x8 wf[3][NB1], wg[2][3];
  auto ldx = [&](int t) { xf[t] = lds_b128((unsigned)((t >> 2) % 5) * SLOT1 + xa[t & 3]); };
  auto ld1a = [&](int t, int b) {
    const unsigned a = (unsigned)((t >> 2) % 5) * SLOT1 + wa + ro[t & 3] + (NJ ? 2 : b) * 64u; wf[t % 3][b] = cat(lds_tr(a), lds_tr(a + 768u));
  };
  auto ld1b = [&](int t, int b) {
    const int u = NJ * 3 + b;
    const unsigned a = (unsigned)((8 + (t >> 2)) % 5) * SLOT1 + ((u & 1) ? wbO : wbE) + ro[t & 3] + u * 128u; wg[t & 1][b] = cat(lds_tr(a), lds_tr(a + 768u));
  };
  auto cs_put = [&](int nb, int b0) {
#pragma unroll
    for (int b = 0; b < 3; ++b) if (b < nb && lane < 16) { *(LAS float*)(lds + L_CSP + (((b0 + b) * 32 + lane) * 4 + mi) * 4) = cs[b][0]; *(LAS float*)(lds + L_CSP + (((b0 + b) * 32 + 16 + lane) * 4 + mi) * 4) = cs[b][1]; }
  };
  auto cs_get = [&](int nb, int b0) {
#pragma unroll
    for (int b = 0; b < 3; ++b) if (b < nb) { const f32x4 c4 = lds_f4(L_CSP + ((b0 + b) * 32 + r) * 16);
      *(LAS float*)(lds + L_CSW + w * 384 + b * 128 + r * 4) = (c4[0] + c4[1]) + (c4[2] + c4[3]); }
  };
  wait_vm<(L::NPRO - 1) * NA + L::NTW>();
  bar();
  e0 = (u64)word_get(2) | ((u64)word_get(3) << 32);
  epoch = e0 + 1ull;
  tg.a = (unsigned)epoch; tg.b = ((unsigned)(e0 >> 32) ^ 0x5bd1e995u) + tg.a;
  STAMP(2); CSTAMP(23);
  ldx(0); ldx(1);
#pragma unroll
  for (int b = 0; b < NB1; ++b) { ld1a(0, b); ld1a(1, b); }
  u32x4 sv;
  float rmean = 0.f, rrstd = 0.f;
  const unsigned sv_off = WS_RS + (unsigned)(128 * rb + 32 * mi) * 16u;
  static_for<0, 32>([&](auto tc) {
    constexpr int t = decltype(tc)::value, s = t >> 2, p = t & 3;
    if constexpr (p == 2) {
      if (s == 7) cs_put(NB1, NJ ? 2 : 0);
      wait_vm<SC.wait_stage(t, s + 1)>();
      bar();
      STAMP2(24 + s);
      if (s == 7) cs_get(NB1, NJ ? 2 : 0);
      if (s == 1) CSTAMP(60); if (s == 4) CSTAMP(61); if (s == 7) CSTAMP(62);
    }
    if (NJ) op_at(tc, IC<0>{});
    static_for<0, NB1>([&](auto bc) {
      constexpr int b = decltype(bc)::value;
      acc[b] = mfma(wf[t % 3][b], xf[t], acc[b]);
      if (t < 30) { if (b == 0) ldx(t + 2); ld1a(t + 2, b); }
      if (t == 31) { ld1b(0, b); if (b == NB1 - 1) { for (int bb = NB1; bb < 3; ++bb) ld1b(0, bb); } }
      if (p == (NJ ? 1 : b)) cs[b] = mfma16(sel, wf[t % 3][b], cs[b]);
      if (b == 0) {
        if (t == 2 + NJ) {
          tie(xrow);
          float e[8];
#pragma unroll
          for (int i = 0; i < 4; ++i) { e[2 * i] = bf_lo(xrow[i]); e[2 * i + 1] = bf_hi(xrow[i]); }
          rmean = wave_sum(((e[0] + e[1]) + (e[2] + e[3])) + ((e[4] + e[5]) + (e[6] + e[7]))) * (1.f / 512.f);
          asm volatile("" : "+v"(rmean));
        }
        if (t == 4) {
          float q = 0.f;
#pragma unroll
          for (int i = 0; i < 4; ++i) { const float d0 = bf_lo(xrow[i]) - rmean, d1 = bf_hi(xrow[i]) - rmean; q = fmaf(d0, d0, q); q = fmaf(d1, d1, q); }
          rrstd = __builtin_amdgcn_rsqf(wave_sum(q) * (1.f / 512.f) + 1e-5f);
          asm volatile("" : "+v"(rrstd));
        }
      }
      SB0();
      if (b == 0) op_at(tc, IC<NJ ? 1 : 0>{});
    });
    if (!NJ) op_at(tc, IC<1>{});
    if (t == 22) sv = ld16c_asm(rs_ws, (unsigned)r * 16u, sv_off);
    if (t == 5 && lane == 0) st16c(u32x4{__float_as_uint(rmean), tg.a, __float_as_uint(rrstd), tg.b}, rs_ws, 0u, WS_RS + myrow * 16u);
    SB0();
  });
  STAMP(3); CSTAMP(22);
  { constexpr int after = SC.before[32] - L::NST - SC.before[23]; wait_vm_tie<after>(sv); }
  NOTE(53, __all(sv[1] == tg.a && sv[3] == tg.b) ? 1 : 2);
  for (unsigned spins = 0; !__all(sv[1] == tg.a && sv[3] == tg.b) && spins < (1u << 20); ++spins) {
    __builtin_amdgcn_s_sleep(1);
    sv = ld16c_asm(rs_ws, (unsigned)r * 16u, sv_off); wait_vm_tie<0>(sv);
  }
  const float mean = __uint_as_float(sv[0]), rstd = __uint_as_float(sv[2]);
  const int sb = rb * 4 + mi;
#pragma unroll
  for (int b = 0; b < NB1; ++b) {
    const int blk = 3 * g + (NJ ? 2 : b), which = blk >> 4, hp = blk & 15;
    const float sc = which == 0 ? rstd * (0.25f * 1.4426950408889634f) : rstd;
    float v[16];
#pragma unroll
    for (int i = 0; i < 4; ++i) { const f32x4 c4 = lds_f4(L_CSW + w * 384 + b * 128 + (8 * i + 4 * h) * 4);
#pragma unroll
      for (int e = 0; e < 4; ++e) v[4 * i + e] = (acc[b][4 * i + e] - mean * c4[e]) * sc; }
    const unsigned vo = which == 2 ? (unsigned)(h * 512 + ((r ^ (4 * h)) * 16)) : (unsigned)(lane * 16);
#pragma unroll
    for (int hd = 0; hd < 2; ++hd) {
      const u32x4 o = {pk(v[8 * hd], v[8 * hd + 1]), pk(v[8 * hd + 2], v[8 * hd + 3]), pk(v[8 * hd + 4], v[8 * hd + 5]), pk(v[8 * hd + 6], v[8 * hd + 7])};
      st16c(o, rs_ws, vo, WS_QKV + (unsigned)((which * 32 + 2 * hp + hd) * 64 + sb) * 1024u);
    }
  }
  STAMP(4); CSTAMP(18);
#pragma unroll
  for (int b = 0; b < 3; ++b) { cs[b] = f32x4{0.f, 0.f, 0.f, 0.f};
#pragma unroll
    for (int i = 0; i < 16; ++i) acc[b][i] = 0.f; }
  bool act;
  const unsigned qv = qkv_flag_voff(rb2, hp2, lane, act);
  u32x4 fq;
  SB0();
  static_for<32, 64>([&](auto tc) {
    constexpr int Tg = decltype(tc)::value, t = Tg - 32, s = Tg >> 2, p = t & 3;
    if constexpr (p == 3) {
      if (s == 15) cs_put(3, NJ * 3);
      if constexpr (s == 9) wait_vm<(SC.before[Tg] - SC.before[32] < SC.wait_stage(Tg, 10) ? SC.before[Tg] - SC.before[32] : SC.wait_stage(Tg, 10))>();
      else if constexpr (s == 14) { wait_vm<0>(); if (w == 0) { tie(fq); word_set(0, flags_ok(fq, act, tg) ? 1u : 0u); } }
      else if constexpr (s <= 13) wait_vm<SC.wait_stage(Tg, s + 1)>();
      bar();
      STAMP2(24 + s);
      if (s == 8) CSTAMP(56); if (s == 11) CSTAMP(57); if (s == 13) CSTAMP(58); if (s == 15) CSTAMP(59);
      if (s == 14) early = word_get(0) != 0u;
      if (s == 15) cs_get(3, NJ * 3);
    }
    if (NJ) op_at(tc, IC<0>{});
    static_for<0, 3>([&](auto bc) {
      constexpr int b = decltype(bc)::value;
      acc[b] = mfma(wg[t & 1][b], xf[t], acc[b]);
      if (t < 31) ld1b(t + 1, b);
      if (p == ((b + 2 * NJ) & 3) || (s == 15 && p == 2 && ((b + 2 * NJ) & 3) == 3)) { if (!(s == 15 && p == 3)) cs[b] = mfma16(sel, wg[t & 1][b], cs[b]); }
      SB0();
    });
    if (!NJ) op_at(tc, IC<0>{});
    op_at(tc, IC<1>{});
    if (Tg == 39) flag_store(rs_ws, WS_FQKV + (unsigned)((rb * 16 + g) * 64 + w * 8), tg, lane);
    if (Tg == 55 && w == 0) fq = ld16c_asm(rs_ws, qv, 0u);
    SB0();
  });
  STAMP(5); CSTAMP(19);
  const float r2 = rstd * rstd;
#pragma unroll
  for (int b = 0; b < 3; ++b) {
    float v[16];
#pragma unroll
    for (int i = 0; i < 4; ++i) { const f32x4 c4 = lds_f4(L_CSW + w * 384 + b * 128 + (8 * i + 4 * h) * 4);
#pragma unroll
      for (int e = 0; e < 4; ++e) v[4 * i + e] = acc[b][4 * i + e] - mean * c4[e]; }
    float hv[8];
#pragma unroll
    for (int i = 0; i < 8; ++i) hv[i] = fmaxf(v[i], 0.f) * v[8 + i] * r2;
    const u32x4 o = {pk(hv[0], hv[1]), pk(hv[2], hv[3]), pk(hv[4], hv[5]), pk(hv[6], hv[7])};
    if (NJ * 3 + b < nu) st16c(o, rs_ws, (unsigned)lane * 16u, WS_H + (unsigned)(sb * NU + u0 + NJ * 3 + b) * 1024u);
  }
  STAMP(6);
  if (NJ == 0 || nu == 6) wait_vm<3>(); else wait_vm<2>();
  return early;
}

template <bool FULL, class F1, class FM, class F2>
DI u32x4 attn_task(int j0, int qb, int hd, int lane, F1 side1, FM mid, F2 side2) {
  const int q = lane & 31, hh = lane >> 5, tq = q - 4 * hh;
  const bf16x8 qf = lds_b128(LA_Q + (unsigned)(hd * 4 + qb) * 1024u + lane * 16u);
  const unsigned kbase = LA_K + (unsigned)(hd * 12 + qb) * 1024u + lane * 16u;
  const float NEG = -__builtin_inff();
  bf16x8 kf[9];
#pragma unroll
  for (int j = 0; j < 9; ++j) if (FULL || j >= j0) kf[j] = lds_b128(kbase + j * 1024u);
  f32x16 c;
#pragma unroll
  for (int i = 0; i < 16; ++i) c[i] = 0.f;
  float m;
  {
    const f32x16 s8 = mfma(kf[8], qf, c);
    f32x16 s0;
    if (FULL) s0 = mfma(kf[0], qf, c);
    m = NEG;
#pragma unroll
    for (int i = 0; i < 16; i += 2) {
      const int c0 = (i & 3) + 8 * (i >> 2);
      const float a = c0 <= tq ? s8[i] : (FULL ? s0[i] : NEG), b = c0 + 1 <= tq ? s8[i + 1] : (FULL ? s0[i + 1] : NEG);
      m = fmaxf(fmaxf(m, a), b);
    }
    side1(0);
  }
#pragma unroll
  for (int j = 1; j < 8; ++j) {
    if (FULL || j >= j0) {
      const f32x16 s = mfma(kf[j], qf, c);
#pragma unroll
      for (int i = 0; i < 16; i += 2) m = fmaxf(fmaxf(m, s[i]), s[i + 1]);
    }
    side1(j);
  }
  m = xhalf_max(m);
  mid();
#pragma unroll
  for (int i = 0; i < 16; ++i) c[i] = -m;
  const int grp = (lane >> 4) & 1, q4 = (lane & 15) >> 2, p4 = lane & 3;
  const unsigned vlane = (unsigned)((p4 & 1) * 512 + (((4 * hh + q4) ^ (4 * (p4 & 1))) * 16) + (p4 >> 1) * 8);
  const unsigned vb0 = grp ? L_ONES + 128u : LA_V + (unsigned)(hd * 12 + qb) * 1024u + vlane;
  const unsigned vb1 = grp ? L_ONES : LA_V + (unsigned)(hd * 12 + qb) * 1024u + vlane + 128u;
  const unsigned vstr = grp ? 0u : 1024u, vs1 = grp ? 0u : 256u;
  f32x16 acc;
#pragma unroll
  for (int i = 0; i < 16; ++i) acc[i] = 0.f;
  auto pv = [&](int j, const unsigned (&pp)[8]) {
#pragma unroll
    for (int s = 0; s < 2; ++s) {
      const bf16x8 vf = cat(lds_tr(vb0 + j * vstr + s * vs1), lds_tr(vb1 + j * vstr + s * vs1));
      const u32x4 pu = {pp[4 * s], pp[4 * s + 1], pp[4 * s + 2], pp[4 * s + 3]};
      acc = mfma(vf, __builtin_bit_cast(bf16x8, pu), acc);
    }
  };
#pragma unroll
  for (int j = 1; j < 8; ++j) {
    if (FULL || j >= j0) {
      const f32x16 s = mfma(kf[j], qf, c);
      unsigned pp[8];
#pragma unroll
      for (int i = 0; i < 8; ++i) pp[i] = pk(__builtin_amdgcn_exp2f(s[2 * i]), __builtin_amdgcn_exp2f(s[2 * i + 1]));
      pv(j, pp);
    }
    side2(j);
  }
  {
    const f32x16 s8 = mfma(kf[8], qf, c);
    f32x16 s0;
    if (FULL) s0 = mfma(kf[0], qf, c);
    unsigned p8[8], p0[8];
#pragma unroll
    for (int i = 0; i < 8; ++i) {
      const int c0 = ((2 * i) & 3) + 8 * (i >> 1);
      const bool va = c0 <= tq, vb = c0 + 1 <= tq;
      const float a = va ? s8[2 * i] : (FULL ? s0[2 * i] : NEG), b = vb ? s8[2 * i + 1] : (FULL ? s0[2 * i + 1] : NEG);
      const unsigned pp = pk(__builtin_amdgcn_exp2f(a), __builtin_amdgcn_exp2f(b));
      const unsigned mk = (va ? 0xffffu : 0u) | (vb ? 0xffff0000u : 0u);
      p8[i] = pp & mk; p0[i] = pp & ~mk;
    }
    pv(8, p8);
    if (FULL) pv(0, p0);
  }
  const float inv = __builtin_amdgcn_rcpf(acc[8]);
  return u32x4{pk(acc[0] * inv, acc[1] * inv), pk(acc[2] * inv, acc[3] * inv), pk(acc[4] * inv, acc[5] * inv), pk(acc[6] * inv, acc[7] * inv)};
}

struct Sched3 {
  static constexpr int a_stage(int st) { return st <= 6 ? st + 4 : (st >= 8 && st <= 11 ? st + 3 : -1); }
  static constexpr int w_stage(int st) { return st <= 10 ? st + 4 : -1; }
  int before[16] = {}, lastA[15] = {}, lastW[15] = {};
  constexpr Sched3() {
    int c = 9;
    for (int i = 0; i < 4; ++i) { lastA[i] = 2 * i + 2; lastW[i] = 0; }
    for (int st = 0; st < 15; ++st) {
      before[st] = c;
      if (st == 3) c += 1;
      if (a_stage(st) >= 0) { c += 2; lastA[a_stage(st)] = c; }
      if (w_stage(st) >= 0) { c += 2; lastW[w_stage(st)] = c; }
      if (st == 8) c += 2;
    }
    before[15] = c;
  }
  constexpr int wait(int st) const { return before[st] - (lastA[st + 1] > lastW[st + 1] ? lastA[st + 1] : lastW[st + 1]); }
};

DI void phase23(const Ptrs& P, bool early, int rb1, int g1, u64 epoch, int rb, int hp, int rb64, int cb, int rot, int w, int lane, Tag tg, Stamps& T) {
  const rsrc_t rs_ws = mk_rsrc(P.ws, WS_BYTES), rs_d = mk_rsrc(P.wd, FF * DM * 2), rs_o = mk_rsrc(P.wo, DM * DM * 2);
  if (threadIdx.x < 64) *(LAS unsigned*)(lds + L_ONES + threadIdx.x * 4) = ONES2;
  NOTE(63, early ? 1 : 2);
  if (!early) {
    bool act; const unsigned qv = qkv_flag_voff(rb, hp, lane, act);
    if (w == 0) flags_spin(rs_ws, qv, act, tg);
    bar();
    const AttnSrc asrc = attn_src(rb, hp, w);
#pragma unroll
    for (int i = 0; i < 7; ++i) attn_issue(rs_ws, asrc, w, lane, i, true);
    wait_vm<0>();
  }
  bar();
  STAMP(9);
  unsigned vw[2];
#pragma unroll
  for (int j = 0; j < 2; ++j) { const int G = (2 * w + j) * 64 + lane, row = G >> 3;
    vw[j] = (unsigned)row * 1024u + (unsigned)(((G & 7) ^ (((row >> 1) & 1) << 2)) * 16); }
  const int sbl = w >> 2, sbg = 2 * rb64 + sbl;
  auto chunk = [&](int st) { return st < 10 ? (st + rot >= 10 ? st + rot - 10 : st + rot) : (st == 10 ? 10 : ((st - 11 + rot) & 3)); };
  auto pieceA = [&](int st, int j, bool go) {
    const int c = chunk(st), ks = 2 * (w & 3) + j;
    const unsigned src = st < 11 ? WS_H + (unsigned)(sbg * NU + c * 8 + ks) * 1024u : WS_O + (unsigned)(sbg * 32 + c * 8 + ks) * 1024u;
    dma16c(l3a(st & 3) + (unsigned)(sbl * 8 + ks) * 1024u, (unsigned)lane * 16u + (go ? 0u : 0x40000000u), rs_ws, src);
  };
  auto pieceW = [&](int st, int j) {
    dma16(l3w(st & 3) + (unsigned)(2 * w + j) * 1024u, vw[j], st < 11 ? rs_d : rs_o, (unsigned)cb * 128u + (unsigned)chunk(st) * 131072u);
  };
  auto side = [&](int j) { pieceW(j >> 1, j & 1); };
  const int qb = w & 3, hd = w >> 2, sb = 4 * rb + qb;
  const unsigned hv = WS_FH + (unsigned)((rb64 >> 1) * 1024 + lane * 16), ov = WS_FO + (unsigned)((rb64 >> 1) * 1024 + lane * 16);
  u32x4 o, fh;
  auto side2 = [&](int j) { if (j == 7) fh = ld16c_asm(rs_ws, hv, 0u); };
  auto mid = [&]() { STAMP(10); wait_vm<8>(); flag_store(rs_ws, WS_FH + (unsigned)((rb1 * 16 + g1) * 64 + w * 8), tg, lane); };
  if (sb >= 8) o = attn_task<true>(0, qb, hd, lane, side, mid, side2); else o = attn_task<false>(8 - sb, qb, hd, lane, side, mid, side2);
  STAMP(11);
  wait_vm_tie<0>(fh);
  NOTE(55, flags_ok(fh, true, tg) ? 1 : 2);
  if (!flags_ok(fh, true, tg)) flags_spin(rs_ws, hv, true, tg);
  pieceA(0, 0, true); pieceA(0, 1, true);
  bar();
  STAMP(13);
#pragma unroll
  for (int s = 1; s < 4; ++s) { pieceA(s, 0, true); pieceA(s, 1, true); }
  st16c(o, rs_ws, (unsigned)lane * 16u, WS_O + (unsigned)(sb * 32 + 2 * hp + hd) * 1024u);
  const int mb = w & 1, nb2 = (w >> 1) & 1, kh = w >> 2;
  const int h = lane >> 5, nsub = (lane >> 4) & 1, q4 = (lane & 15) >> 2, p4 = lane & 3;
  const unsigned aa = (unsigned)(mb * 8 + kh * 4) * 1024u + lane * 16u;
  const unsigned wa = (unsigned)(16 * kh + h) * 512u + q4 * 128u + (unsigned)((nb2 ^ (q4 >> 1)) * 64) + nsub * 32u + p4 * 8u;
  const int row = 64 * rb64 + 32 * mb + (lane & 31);
  f32x16 acc;
#pragma unroll
  for (int i = 0; i < 16; ++i) acc[i] = 0.f;
  bf16x8 af[2][4], wfr[2][4];
  auto ld3 = [&](int st, int ks) {
    af[st & 1][ks] = lds_b128(l3a(st & 3) + aa + ks * 1024u); const unsigned a = l3w(st & 3) + wa + ks * 2048u; wfr[st & 1][ks] = cat(lds_tr(a), lds_tr(a + 1024u));
  };
  const int col0 = 64 * cb + 32 * nb2 + 16 * kh + 4 * h;
  wait_vm<7>();
  bar();
  STAMP(14);
#pragma unroll
  for (int ks = 0; ks < 4; ++ks) ld3(0, ks);
  u32x4 fo, fe[4];
  const unsigned short* xr = (const unsigned short*)P.x + (size_t)row * DM + col0;
  u32x2 x0, x1;
  const bool keeper = blockIdx.x == 0 && w == 0;
  static constexpr Sched3 S3{};
  static_for<0, 15>([&](auto sc) {
    constexpr int st = decltype(sc)::value;
    if constexpr (st < 14) {
      wait_vm<S3.wait(st)>();
      if (st == 8 && w == 0) { wait_vm_tie<2>(fo); NOTE(54, flags_ok(fo, true, tg) ? 1 : 2); if (!flags_ok(fo, true, tg)) flags_spin(rs_ws, ov, true, tg); }
      bar();
      STAMP2(40 + st);
    }
    if (st == 3) flag_store(rs_ws, WS_FO + (unsigned)((rb * 16 + hp) * 64 + w * 8), tg, lane);
    SB0();
#pragma unroll
    for (int ks = 0; ks < 4; ++ks) {
      if (ks < 2 || st != 10 || kh == 0) acc = mfma(wfr[st & 1][ks], af[st & 1][ks], acc);
      if (st < 14) ld3(st + 1, ks);
      SB0();
      if (ks < 2) { if (Sched3::a_stage(st) >= 0) pieceA(Sched3::a_stage(st), ks, true); }
      else if (Sched3::w_stage(st) >= 0) pieceW(Sched3::w_stage(st), ks - 2);
      SB0();
    }
    if (st == 6 && w == 0) fo = ld16c_asm(rs_ws, ov, 0u);
    if (st == 8) { x0 = *(const GAS u32x2*)xr; x1 = *(const GAS u32x2*)(xr + 8); }
    if (st == 9 && keeper) {
#pragma unroll
      for (int i = 0; i < 4; ++i) fe[i] = ld16c_asm(rs_ws, WS_FQKV + (unsigned)(i * 64 + lane) * 64u, 0u);
    }
    SB0();
  });
  STAMP(15);
  bar();
  {
    const unsigned base = L3_RED + (unsigned)((w & 3) * 2 + kh) * 2048u + lane * 16u;
    if (kh) { *(LAS f32x4*)(lds + base) = f32x4{acc[0], acc[1], acc[2], acc[3]}; *(LAS f32x4*)(lds + base + 1024u) = f32x4{acc[4], acc[5], acc[6], acc[7]}; }
    else { *(LAS f32x4*)(lds + base) = f32x4{acc[8], acc[9], acc[10], acc[11]}; *(LAS f32x4*)(lds + base + 1024u) = f32x4{acc[12], acc[13], acc[14], acc[15]}; }
  }
  bar();
  {
    const unsigned base = L3_RED + (unsigned)((w & 3) * 2 + (kh ^ 1)) * 2048u + lane * 16u;
    const f32x4 a0 = lds_f4(base), a1 = lds_f4(base + 1024u);
    unsigned short* orow = (unsigned short*)P.out + (size_t)row * DM + col0;
    float v[8];
#pragma unroll
    for (int e = 0; e < 4; ++e) { v[e] = (kh ? acc[8 + e] : acc[e]) + a0[e]; v[4 + e] = (kh ? acc[12 + e] : acc[4 + e]) + a1[e]; }
    v[0] += bf_lo(x0[0]); v[1] += bf_hi(x0[0]); v[2] += bf_lo(x0[1]); v[3] += bf_hi(x0[1]);
    v[4] += bf_lo(x1[0]); v[5] += bf_hi(x1[0]); v[6] += bf_lo(x1[1]); v[7] += bf_hi(x1[1]);
    *(GAS u32x2*)orow = u32x2{pk(v[0], v[1]), pk(v[2], v[3])};
    *(GAS u32x2*)(orow + 8) = u32x2{pk(v[4], v[5]), pk(v[6], v[7])};
  }
  if (keeper) {
#pragma unroll
    for (int i = 0; i < 4; ++i) { tie(fe[i]); if (!flags_ok(fe[i], true, tg)) flags_spin(rs_ws, WS_FQKV + (unsigned)(i * 64 + lane) * 64u, true, tg); }
    if (lane == 0) __hip_atomic_store((GAS u64*)(P.ws + WS_EPOCH), epoch, __ATOMIC_RELAXED, __HIP_MEMORY_SCOPE_AGENT);
  }
}

__global__ __launch_bounds__(512, 2) void mk_kernel(const void* x, const void* wqkv, const void* wo, const void* wg, const void* wu, const void* wd, void* out, char* ws) {
  u64 pc0 = 0;
  asm volatile("mk_code_begin:\n\ts_getpc_b64 %0" : "=s"(pc0));
  {
    unsigned len;
    asm volatile("s_mov_b32 %0, mk_code_end-mk_code_begin" : "=s"(len));
    const unsigned wv = __builtin_amdgcn_readfirstlane(threadIdx.x >> 6);
    const unsigned per = ((len >> 5) + 1023u) & ~1023u;
    const unsigned o = (blockIdx.x >> 3) * per + wv * 1024u;
    dma16(L_DUMMY + wv * 1024u, (threadIdx.x & 63u) * 16u, mk_rsrc((const void*)(pc0 & ~255ull), len & ~15u), wv * 1024u < per ? o : 0x40000000u);
  }
  const Ptrs P{x, wqkv, wo, wg, wu, wd, out, ws, pc0};
  Stamps T;
  STAMP(0); CSTAMP(20);
  __builtin_amdgcn_s_setprio(1);
  const int b = blockIdx.x, xcd = b & 7, idx = b >> 3;
  const int w = __builtin_amdgcn_readfirstlane(threadIdx.x >> 6), lane = threadIdx.x & 63;
  Tag tg; u64 epoch;
  const int rb = 2 * (idx >> 2) + (xcd >> 2), g = (xcd & 3) * 4 + (idx & 3), rot1 = ((idx >> 2) + 2 * (idx & 3)) & 7;
  bool early;
  if (w < 4) early = phase1<0>(P, rb, g, rot1, w, lane, rb, g, tg, epoch, T); else early = phase1<1>(P, rb, g, rot1, w - 4, lane, rb, g, tg, epoch, T);
  const int rot3 = ((idx & 7) + 3 * (idx >> 3)) % 10;
  phase23(P, early, rb, g, epoch, rb, g, xcd * 4 + (idx >> 3), idx & 7, rot3, w, lane, tg, T);
  asm volatile("mk_code_end:");
}

}

extern "C" void kernel_launch(void* const* d_in, const int* in_sizes, int n_in, void* d_out, int out_size, void* d_ws, size_t ws_size, hipStream_t stream) {
  const void *x = d_in[0], *wqkv = d_in[1], *wo = d_in[2], *wg = d_in[4], *wu = d_in[5], *wd = d_in[6];
  mk_kernel<<<256, 512, 0, stream>>>(x, wqkv, wo, wg, wu, wd, d_out, (char*)d_ws);
}
```

```cpp
#define PROBE_AT 4
#include <hip/hip_runtime.h>
#include <hip/hip_bf16.h>
#include <stdint.h>

namespace {

typedef short bf16x8 __attribute__((ext_vector_type(8)));
typedef short s16x4 __attribute__((ext_vector_type(4)));
typedef float f32x16 __attribute__((ext_vector_type(16)));
typedef float f32x4 __attribute__((ext_vector_type(4)));
typedef float f32x2 __attribute__((ext_vector_type(2)));
typedef unsigned u32x4 __attribute__((ext_vector_type(4)));
typedef unsigned u32x2 __attribute__((ext_vector_type(2)));
typedef __bf16 bf16x2_t __attribute__((ext_vector_type(2)));
typedef short v4i16_t __attribute__((ext_vector_type(4)));
typedef __amdgpu_buffer_rsrc_t rsrc_t;
#define LAS __attribute__((address_space(3)))
#define GAS __attribute__((address_space(1)))
#define DI __device__ __forceinline__
#define SB0() __builtin_amdgcn_sched_barrier(0)
typedef unsigned long long u64;

constexpr int SEQ = 2048, DM = 512, FF = 1376, NU = 86;
constexpr unsigned WS_EPOCH = 0;
constexpr unsigned WS_FQKV = 16384, WS_FH = 32768, WS_FO = 49152;
constexpr unsigned WS_RS = 65536;
constexpr unsigned WS_QKV = 1u << 20;
constexpr unsigned WS_H = 7u << 20;
constexpr unsigned WS_O = 13u << 20;
constexpr unsigned WS_BYTES = 16u << 20;
constexpr unsigned SLOT1 = 28672;
constexpr unsigned L_CSP = 143360;
constexpr unsigned L_CSW = 146432;
constexpr unsigned L_ONES = 163328;
constexpr unsigned L_WORD = 163584;
constexpr unsigned LDS_BYTES = 163840;
constexpr unsigned LA_Q = 28672, LA_K = LA_Q + 8192, LA_V = LA_K + 24576;
DI constexpr unsigned l3w(int i) { return i == 0 ? 0u : 86016u + (unsigned)(i - 1) * 16384u; }
DI constexpr unsigned l3a(int i) { return i == 0 ? 135168u : 28672u + (unsigned)(i - 1) * 16384u; }
constexpr unsigned L3_RED = 0;

__shared__ __attribute__((aligned(1024))) char lds[LDS_BYTES];

DI unsigned pk(float a, float b) { f32x2 v = {a, b}; bf16x2_t r = __builtin_convertvector(v, bf16x2_t); return __builtin_bit_cast(unsigned, r); }
DI float dot2(unsigned a, unsigned b, float c) { return __builtin_amdgcn_fdot2_f32_bf16(__builtin_bit_cast(bf16x2_t, a), __builtin_bit_cast(bf16x2_t, b), c, false); }
DI rsrc_t mk_rsrc(const void* p, unsigned bytes) { return __builtin_amdgcn_make_buffer_rsrc((void*)p, 0, bytes, 0x00020000); }
DI void dma16(unsigned ldsaddr, unsigned voff, rsrc_t rs, unsigned soff) {
  asm volatile("s_mov_b32 m0, %0\n\ts_nop 0\n\tbuffer_load_dwordx4 %1, %2, %3 offen lds" ::"s"(ldsaddr), "v"(voff), "s"(rs), "s"(soff) : "memory");
}
DI void dma16c(unsigned ldsaddr, unsigned voff, rsrc_t rs, unsigned soff) {
  asm volatile("s_mov_b32 m0, %0\n\ts_nop 0\n\tbuffer_load_dwordx4 %1, %2, %3 offen sc1 lds" ::"s"(ldsaddr), "v"(voff), "s"(rs), "s"(soff) : "memory");
}
DI void pf_lines(unsigned ldsaddr, unsigned voff, rsrc_t rs) {
  asm volatile("s_mov_b32 m0, %0\n\ts_nop 0\n\tbuffer_load_dword %1, %2, 0 offen lds" ::"s"(ldsaddr), "v"(voff), "s"(rs) : "memory");
}
template <int N> DI void wait_vm() { asm volatile("s_waitcnt vmcnt(%0)" ::"n"(N) : "memory"); }
template <int N> DI void wait_vm_tie(u32x4& v) { asm volatile("s_waitcnt vmcnt(%1)" : "+v"(v) : "n"(N) : "memory"); }
template <int V> struct IC { static constexpr int value = V; };
template <int I, int N, class F> DI void static_for(F&& f) { if constexpr (I < N) { f(IC<I>{}); static_for<I + 1, N>(f); } }
DI void tie(u32x4& v) { asm volatile("" : "+v"(v) :: "memory"); }
DI void bar() { asm volatile("s_waitcnt lgkmcnt(0)\n\ts_barrier" ::: "memory"); }
DI f32x16 mfma(bf16x8 a, bf16x8 b, f32x16 c) { return __builtin_amdgcn_mfma_f32_32x32x16_bf16(a, b, c, 0, 0, 0); }
DI bf16x8 lds_b128(unsigned a) { return *(const LAS bf16x8*)(lds + a); }
#define MK_REAL 1
DI s16x4 lds_tr(unsigned a) { return __builtin_amdgcn_ds_read_tr16_b64_v4i16((LAS v4i16_t*)(lds + a)); }
DI f32x4 mfma16(bf16x8 a, bf16x8 b, f32x4 c) { return __builtin_amdgcn_mfma_f32_16x16x32_bf16(a, b, c, 0, 0, 0); }
DI f32x4 lds_f4(unsigned a) { return *(const LAS f32x4*)(lds + a); }
DI bf16x8 cat(s16x4 lo, s16x4 hi) { return __builtin_shufflevector(lo, hi, 0, 1, 2, 3, 4, 5, 6, 7); }
DI float xhalf_sum(float v) { auto r = __builtin_amdgcn_permlane32_swap(__float_as_uint(v), __float_as_uint(v), false, false); return __uint_as_float(r[0]) + __uint_as_float(r[1]); }
DI float xhalf_max(float v) { auto r = __builtin_amdgcn_permlane32_swap(__float_as_uint(v), __float_as_uint(v), false, false); return fmaxf(__uint_as_float(r[0]), __uint_as_float(r[1])); }
DI void st16c(u32x4 v, rsrc_t rs, unsigned voff, unsigned soff) { __builtin_amdgcn_raw_buffer_store_b128(v, rs, voff + soff, 0, 16); }
DI void st8c(u32x2 v, rsrc_t rs, unsigned voff, unsigned soff) { __builtin_amdgcn_raw_buffer_store_b64(v, rs, voff, soff, 16); }
constexpr unsigned ONES2 = 0x3F803F80u;
template <int CTRL> DI float dpp_add(float v) { return v + __int_as_float(__builtin_amdgcn_update_dpp(0, __float_as_int(v), CTRL, 0xf, 0xf, true)); }
DI float wave_sum(float v) {
  v = dpp_add<0xB1>(v); v = dpp_add<0x4E>(v); v = dpp_add<0x141>(v); v = dpp_add<0x140>(v);
  auto a = __builtin_amdgcn_permlane16_swap(__float_as_uint(v), __float_as_uint(v), false, false);
  v = __uint_as_float(a[0]) + __uint_as_float(a[1]);
  return xhalf_sum(v);
}
DI float bf_lo(unsigned u) { return __uint_as_float(u << 16); }
DI float bf_hi(unsigned u) { return __uint_as_float(u & 0xffff0000u); }
DI u32x4 ld16_asm(rsrc_t rs, unsigned voff, unsigned soff) { u32x4 v; asm volatile("buffer_load_dwordx4 %0, %1, %2, %3 offen" : "=v"(v) : "v"(voff), "s"(rs), "s"(soff) : "memory"); return v; }
DI u32x4 ld16c_asm(rsrc_t rs, unsigned voff, unsigned soff) { u32x4 v; asm volatile("buffer_load_dwordx4 %0, %1, %2, %3 offen sc1" : "=v"(v) : "v"(voff), "s"(rs), "s"(soff) : "memory"); return v; }
#define STAMP2(i) do {} while (0)
struct Stamps {};
#define STAMP(i) do {} while (0)
#define CSTAMP(i) do {} while (0)
#define NOTE(i, v) do {} while (0)

__device__ __forceinline__ void probe(int id, u64 t0) { if (id == PROBE_AT) { while (__builtin_amdgcn_s_memrealtime() - t0 < 4000ull) __builtin_amdgcn_s_sleep(4); } }
#define PROBE(i) probe(i, P.t0)
struct Ptrs { const void *x, *wqkv, *wo, *wg, *wu, *wd; void* out; char* ws; u64 pc; u64 t0; };
struct Tag { unsigned a, b; };

DI void flag_store(rsrc_t rs_ws, unsigned off, Tag tg, int lane) { if (lane == 0) st8c(u32x2{tg.a, tg.b}, rs_ws, 0u, off); }
DI bool flags_ok(u32x4 v, bool act, Tag tg) { return __all(!act || (v[0] == tg.a && v[1] == tg.b && v[2] == tg.a && v[3] == tg.b)); }
DI void flags_spin(rsrc_t rs_ws, unsigned voff, bool act, Tag tg) {
  for (unsigned spins = 0; spins < (1u << 20); ++spins) {
    u32x4 v = ld16c_asm(rs_ws, voff, 0u); wait_vm_tie<0>(v);
    if (flags_ok(v, act, tg)) break;
    __builtin_amdgcn_s_sleep(1);
  }
}
DI void word_set(int i, unsigned v) { *(volatile LAS unsigned*)(lds + L_WORD + i * 4) = v; }
DI unsigned word_get(int i) { return __builtin_amdgcn_readfirstlane(*(volatile LAS unsigned*)(lds + L_WORD + i * 4)); }

struct AttnSrc { unsigned q, kv; int sb0; };
DI AttnSrc attn_src(int rb, int hp, int w) {
  AttnSrc a;
  a.q = __builtin_amdgcn_readfirstlane(WS_QKV + (unsigned)((2 * hp + (w >> 2)) * 64 + 4 * rb + (w & 3)) * 1024u);
  a.kv = __builtin_amdgcn_readfirstlane(WS_QKV + (unsigned)(((1 + (w >> 2)) * 32 + 2 * hp + ((w >> 1) & 1)) * 64) * 1024u);
  a.sb0 = __builtin_amdgcn_readfirstlane(4 * rb - 8 + 6 * (w & 1));
  return a;
}
DI void attn_issue(rsrc_t rs_ws, const AttnSrc& a, int w, int lane, int piece, bool go) {
  const unsigned vo = (unsigned)lane * 16u + (go ? 0u : 0x40000000u);
  if (piece == 0) { dma16c(LA_Q + (unsigned)w * 1024u, vo, rs_ws, a.q); return; }
  const int sbk = a.sb0 + piece - 1;
  dma16c(LA_K + (unsigned)((w >> 1) * 12 + 6 * (w & 1) + piece - 1) * 1024u, vo, rs_ws, a.kv + (unsigned)(sbk < 0 ? 0 : sbk) * 1024u);
}
DI unsigned qkv_flag_voff(int rb, int hp, int lane, bool& act) {
  const int c = lane >> 2, kind = c == 0 ? 0 : (c <= 3 ? 1 : 2), d = c == 0 ? 0 : (c - 1) % 3, rr = rb - d;
  act = lane < 28 && rr >= 0;
  return act ? WS_FQKV + (unsigned)((rr * 16 + (kind * 16 + hp) / 3) * 64 + (lane & 3) * 16) : 0u;
}

constexpr unsigned L_DUMMY = 152576;
#define TLBW_N 0
#define P1_HOLD 12
constexpr int OP_NONE = -1, OP_PFW = 200, OP_ATT = 400;
template <int NJ> struct Sched1 {
  static constexpr int NA = NJ ? 4 : 3, NST = NJ ? 2 : 4;
  static constexpr int n(int sg) { return sg < 8 ? NA : 3; }
  static constexpr int TB(int s) { return s < 8 ? 4 * s + 2 : 4 * s + 3; }
  static constexpr int extras(int T) { return (T == 5 ? 1 : 0)   + (T == 22 ? 1 : 0)   + (T == 39 ? 1 : 0)  ; }
  static constexpr int NPRO = 4;
  static constexpr int NTW = TLBW_N;
  static constexpr int PRO = 1 + NPRO * NA + 1 + NTW;
  static constexpr int HOLD = P1_HOLD;
  int op[64][2] = {}, before[65] = {}, last[16] = {};
  constexpr Sched1() {
    int sg = NPRO, j = 0, pf = 0, att = 0, c = PRO;
    last[0] = 1 + NA;
    for (int i = 1; i < NPRO; ++i) last[i] = 1 + NA + 1 + NTW + i * NA;
    for (int T = 0; T < 64; ++T) {
      before[T] = c;
      for (int k = 0; k < 2; ++k) {
        int o = OP_NONE;
        if (k == 0 || T < 14) {
          if (sg <= 15 && (sg < 5 || T >= TB(sg - 5)) && (sg < 8 || T >= 4 * sg - HOLD)) { o = sg * 8 + j; if (++j == n(sg)) { last[sg] = c + 1; ++sg; j = 0; } }
          else if (pf == 0 && T >= 4) o = OP_PFW + pf++;
        }
        if (T >= 59 && att < 7 && (k == 0 || T >= 61)) o = OP_ATT + att++;
        op[T][k] = o;
        if (o != OP_NONE) ++c;
      }
      c += extras(T);
      if (T == 31) c += NST;
    }
    before[64] = c;
  }
  constexpr int wait_stage(int T, int sg) const { return before[T] - last[sg]; }
};

template <int NJ>
DI bool phase1(const Ptrs& P, int rb, int g, int rot, int mi, int lane, int rb2, int hp2, Tag& tg, u64& epoch, Stamps& T_) {
  Stamps& T = T_;
  typedef Sched1<NJ> L;
  static constexpr Sched1<NJ> SC{};
  const int w = NJ * 4 + mi;
  u64 e0 = 0;
  if (w == 0) asm volatile("s_load_dwordx2 %0, %1, 0x0" : "=s"(e0) : "s"(P.ws) : "memory");
  const rsrc_t rs_x = mk_rsrc(P.x, SEQ * DM * 2), rs_q = mk_rsrc(P.wqkv, DM * 1536 * 2);
  const rsrc_t rs_f = mk_rsrc(NJ ? P.wu : P.wg, DM * FF * 2), rs_ws = mk_rsrc(P.ws, WS_BYTES);
  const unsigned sx0 = (unsigned)rb * 131072u, sq0 = (unsigned)g * 192u;
  constexpr int NQ = NJ ? 2 : 1;
  constexpr int NA = 2 + NQ;
  unsigned vx[2], vq[2], vf[3];
#pragma unroll
  for (int j = 0; j < 2; ++j) vx[j] = (unsigned)(16 * w + 8 * j + (lane >> 3)) * 1024u + (unsigned)(((lane & 7) ^ (4 * j + (lane >> 4))) * 16);
#pragma unroll
  for (int j = 0; j < NQ; ++j) { const int ins = NJ ? 2 * mi + j : 8 + mi; const int G = ins * 64 + lane;
    vq[j] = (unsigned)(G / 12) * 3072u + (unsigned)(G % 12) * 16u; }
  const int u0 = (g * NU) >> 4, nu = (((g + 1) * NU) >> 4) - u0;
  const unsigned sf0 = (unsigned)u0 * 32u;
  auto piece = [&](int gs, int j) {
    const unsigned slot = (unsigned)(gs % 5) * SLOT1;
    const unsigned c = (unsigned)(gs + rot) & 7u;
    if (gs < 8) {
      if (j < 2) dma16(slot + (unsigned)(2 * w + j) * 1024u, vx[j], rs_x, sx0 + c * 128u);
      else dma16(slot + 16384u + (unsigned)(NJ ? 2 * mi + j - 2 : 8 + mi) * 1024u, vq[j - 2], rs_q, sq0 + c * (64u * 3072u));
    } else dma16(slot + NJ * 12288u + (unsigned)(3 * mi + j) * 1024u, vf[j], rs_f, sf0 + c * (64u * 2752u));
  };
#pragma unroll
  for (int j = 0; j < NA; ++j) piece(0, j);
  const unsigned myrow = (unsigned)(128 * rb + 8 * g + w);
  u32x4 xrow = ld16_asm(rs_x, (unsigned)lane * 16u, myrow * 1024u);
#pragma unroll
  for (int sg = 1; sg < L::NPRO; ++sg)
#pragma unroll
    for (int j = 0; j < NA; ++j) piece(sg, j);
  SB0();
  STAMP(1);
#pragma unroll
  for (int j = 0; j < 3; ++j) { const int G = (3 * mi + j) * 64 + lane, kg = G / 48, rr = G % 48, unit = (rr >> 3) ^ NJ;
    vf[j] = unit < nu ? (unsigned)(4 * kg + ((rr & 7) >> 1)) * 2752u + (unsigned)(unit * 32 + (rr & 1) * 16) : 0x40000000u; }
  const int r = lane & 31, h = lane >> 5, nsub = (lane >> 4) & 1, q4 = (lane & 15) >> 2, p4 = lane & 3;
  unsigned xa[4], ro[4];
#pragma unroll
  for (int p = 0; p < 4; ++p) { const int ks = (p + mi) & 3;
    xa[p] = (unsigned)(32 * mi + r) * 128u + (unsigned)(((2 * ks + h) ^ ((r >> 1) & 7)) * 16);
    ro[p] = (unsigned)ks * 3072u; }
  const unsigned wl = (unsigned)(2 * h) * 768u + q4 * 32u + p4 * 8u;
  const unsigned wa = 16384u + (unsigned)(2 * h) * 768u + q4 * 192u + nsub * 32u + p4 * 8u;
  const unsigned wbE = nsub * 12288u + wl + nsub * 128u;
  const unsigned wbO = nsub * 12288u + wl - nsub * 128u;
  const AttnSrc asrc = attn_src(rb2, hp2, w);
  bool early = false;
  auto warm = [&](int i) {
    const unsigned xl = blockIdx.x >> 3, dst = L_DUMMY + (unsigned)w * 256u;
    if (i == 0) {
      const unsigned q = (unsigned)(w & 1) * 64u + (unsigned)lane, rr = q / 7u, ln = q - rr * 7u;
      const unsigned ub = (unsigned)((((blockIdx.x & 3) * 4 * NU) >> 4) * 32) & ~127u;
      pf_lines(dst, (w < 4 && q < 112u) ? (16u * xl + rr) * 2752u + ub + ln * 128u : 0x40000000u, mk_rsrc((w & 2) ? P.wu : P.wg, DM * FF * 2));
    } else if (i == 1) {
      pf_lines(dst, lane < 43 ? ((xl * 8u + (unsigned)w) * 43u + (unsigned)lane) * 128u : 0x40000000u, mk_rsrc(P.wd, FF * DM * 2));
    } else {
      pf_lines(dst, lane < 16 ? ((xl * 8u + (unsigned)w) * 16u + (unsigned)lane) * 128u : 0x40000000u, mk_rsrc(P.wo, DM * DM * 2));
    }
  };
  auto op_at = [&](auto tc, auto kc) {
    constexpr int o = SC.op[decltype(tc)::value][decltype(kc)::value];
    if constexpr (o >= OP_ATT) attn_issue(rs_ws, asrc, w, lane, o - OP_ATT, early);
    else if constexpr (o >= OP_PFW) warm(o - OP_PFW);
    else if constexpr (o >= 0) piece(o >> 3, o & 7);
  };
  bf16x8 sel;
  { const unsigned v = (lane & 15) == ((lane >> 4) & 1) ? ONES2 : 0u; sel = __builtin_bit_cast(bf16x8, u32x4{v, v, v, v}); }
  if (w == 0) {
    asm volatile("s_waitcnt lgkmcnt(0)" : "+s"(e0) :: "memory");
    word_set(2, (unsigned)e0); word_set(3, (unsigned)(e0 >> 32));
  }

  bf16x8 xf[32];
  constexpr int NB1 = NJ ? 1 : 2;
  f32x16 acc[3];
  f32x4 cs[3];
#pragma unroll
  for (int b = 0; b < 3; ++b) { cs[b] = f32x4{0.f, 0.f, 0.f, 0.f};
#pragma unroll
    for (int i = 0; i < 16; ++i) acc[b][i] = 0.f; }
  bf16x8 wf[3][NB1], wg[2][3];
  auto ldx = [&](int t) { xf[t] = lds_b128((unsigned)((t >> 2) % 5) * SLOT1 + xa[t & 3]); };
  auto ld1a = [&](int t, int b) {
    const unsigned a = (unsigned)((t >> 2) % 5) * SLOT1 + wa + ro[t & 3] + (NJ ? 2 : b) * 64u; wf[t % 3][b] = cat(lds_tr(a), lds_tr(a + 768u));
  };
  auto ld1b = [&](int t, int b) {
    const int u = NJ * 3 + b;
    const unsigned a = (unsigned)((8 + (t >> 2)) % 5) * SLOT1 + ((u & 1) ? wbO : wbE) + ro[t & 3] + u * 128u; wg[t & 1][b] = cat(lds_tr(a), lds_tr(a + 768u));
  };
  auto cs_put = [&](int nb, int b0) {
#pragma unroll
    for (int b = 0; b < 3; ++b) if (b < nb && lane < 16) { *(LAS float*)(lds + L_CSP + (((b0 + b) * 32 + lane) * 4 + mi) * 4) = cs[b][0]; *(LAS float*)(lds + L_CSP + (((b0 + b) * 32 + 16 + lane) * 4 + mi) * 4) = cs[b][1]; }
  };
  auto cs_get = [&](int nb, int b0) {
#pragma unroll
    for (int b = 0; b < 3; ++b) if (b < nb) { const f32x4 c4 = lds_f4(L_CSP + ((b0 + b) * 32 + r) * 16);
      *(LAS float*)(lds + L_CSW + w * 384 + b * 128 + r * 4) = (c4[0] + c4[1]) + (c4[2] + c4[3]); }
  };
  wait_vm<(L::NPRO - 1) * NA + L::NTW>();
  bar();
  e0 = (u64)word_get(2) | ((u64)word_get(3) << 32);
  epoch = e0 + 1ull;
  tg.a = (unsigned)epoch; tg.b = ((unsigned)(e0 >> 32) ^ 0x5bd1e995u) + tg.a;
  PROBE(1);
  STAMP(2); CSTAMP(23);
  ldx(0); ldx(1);
#pragma unroll
  for (int b = 0; b < NB1; ++b) { ld1a(0, b); ld1a(1, b); }
  u32x4 sv;
  float rmean = 0.f, rrstd = 0.f;
  const unsigned sv_off = WS_RS + (unsigned)(128 * rb + 32 * mi) * 16u;
  static_for<0, 32>([&](auto tc) {
    constexpr int t = decltype(tc)::value, s = t >> 2, p = t & 3;
    if constexpr (p == 2) {
      if (s == 7) cs_put(NB1, NJ ? 2 : 0);
      wait_vm<SC.wait_stage(t, s + 1)>();
      bar();
      STAMP2(24 + s);
      if (s == 7) cs_get(NB1, NJ ? 2 : 0);
      if (s == 1) CSTAMP(60); if (s == 4) CSTAMP(61); if (s == 7) CSTAMP(62);
    }
    if (NJ) op_at(tc, IC<0>{});
    static_for<0, NB1>([&](auto bc) {
      constexpr int b = decltype(bc)::value;
      acc[b] = mfma(wf[t % 3][b], xf[t], acc[b]);
      if (t < 30) { if (b == 0) ldx(t + 2); ld1a(t + 2, b); }
      if (t == 31) { ld1b(0, b); if (b == NB1 - 1) { for (int bb = NB1; bb < 3; ++bb) ld1b(0, bb); } }
      if (p == (NJ ? 1 : b)) cs[b] = mfma16(sel, wf[t % 3][b], cs[b]);
      if (b == 0) {
        if (t == 2 + NJ) {
          tie(xrow);
          float e[8];
#pragma unroll
          for (int i = 0; i < 4; ++i) { e[2 * i] = bf_lo(xrow[i]); e[2 * i + 1] = bf_hi(xrow[i]); }
          rmean = wave_sum(((e[0] + e[1]) + (e[2] + e[3])) + ((e[4] + e[5]) + (e[6] + e[7]))) * (1.f / 512.f);
          asm volatile("" : "+v"(rmean));
        }
        if (t == 4) {
          float q = 0.f;
#pragma unroll
          for (int i = 0; i < 4; ++i) { const float d0 = bf_lo(xrow[i]) - rmean, d1 = bf_hi(xrow[i]) - rmean; q = fmaf(d0, d0, q); q = fmaf(d1, d1, q); }
          rrstd = __builtin_amdgcn_rsqf(wave_sum(q) * (1.f / 512.f) + 1e-5f);
          asm volatile("" : "+v"(rrstd));
        }
      }
      SB0();
      if (b == 0) op_at(tc, IC<NJ ? 1 : 0>{});
    });
    if (!NJ) op_at(tc, IC<1>{});
    if (t == 22) sv = ld16c_asm(rs_ws, (unsigned)r * 16u, sv_off);
    if (t == 5 && lane == 0) st16c(u32x4{__float_as_uint(rmean), tg.a, __float_as_uint(rrstd), tg.b}, rs_ws, 0u, WS_RS + myrow * 16u);
    SB0();
  });
  STAMP(3); CSTAMP(22);
  { constexpr int after = SC.before[32] - L::NST - SC.before[23]; wait_vm_tie<after>(sv); }
  NOTE(53, __all(sv[1] == tg.a && sv[3] == tg.b) ? 1 : 2);
  for (unsigned spins = 0; !__all(sv[1] == tg.a && sv[3] == tg.b) && spins < (1u << 20); ++spins) {
    __builtin_amdgcn_s_sleep(1);
    sv = ld16c_asm(rs_ws, (unsigned)r * 16u, sv_off); wait_vm_tie<0>(sv);
  }
  const float mean = __uint_as_float(sv[0]), rstd = __uint_as_float(sv[2]);
  const int sb = rb * 4 + mi;
#pragma unroll
  for (int b = 0; b < NB1; ++b) {
    const int blk = 3 * g + (NJ ? 2 : b), which = blk >> 4, hp = blk & 15;
    const float sc = which == 0 ? rstd * (0.25f * 1.4426950408889634f) : rstd;
    float v[16];
#pragma unroll
    for (int i = 0; i < 4; ++i) { const f32x4 c4 = lds_f4(L_CSW + w * 384 + b * 128 + (8 * i + 4 * h) * 4);
#pragma unroll
      for (int e = 0; e < 4; ++e) v[4 * i + e] = (acc[b][4 * i + e] - mean * c4[e]) * sc; }
    const unsigned vo = which == 2 ? (unsigned)(h * 512 + ((r ^ (4 * h)) * 16)) : (unsigned)(lane * 16);
#pragma unroll
    for (int hd = 0; hd < 2; ++hd) {
      const u32x4 o = {pk(v[8 * hd], v[8 * hd + 1]), pk(v[8 * hd + 2], v[8 * hd + 3]), pk(v[8 * hd + 4], v[8 * hd + 5]), pk(v[8 * hd + 6], v[8 * hd + 7])};
      st16c(o, rs_ws, vo, WS_QKV + (unsigned)((which * 32 + 2 * hp + hd) * 64 + sb) * 1024u);
    }
  }
  PROBE(2);
  STAMP(4); CSTAMP(18);
#pragma unroll
  for (int b = 0; b < 3; ++b) { cs[b] = f32x4{0.f, 0.f, 0.f, 0.f};
#pragma unroll
    for (int i = 0; i < 16; ++i) acc[b][i] = 0.f; }
  bool act;
  const unsigned qv = qkv_flag_voff(rb2, hp2, lane, act);
  u32x4 fq;
  SB0();
  static_for<32, 64>([&](auto tc) {
    constexpr int Tg = decltype(tc)::value, t = Tg - 32, s = Tg >> 2, p = t & 3;
    if constexpr (p == 3) {
      if (s == 15) cs_put(3, NJ * 3);
      if constexpr (s == 9) wait_vm<(SC.before[Tg] - SC.before[32] < SC.wait_stage(Tg, 10) ? SC.before[Tg] - SC.before[32] : SC.wait_stage(Tg, 10))>();
      else if constexpr (s == 14) { wait_vm<0>(); if (w == 0) { tie(fq); word_set(0, flags_ok(fq, act, tg) ? 1u : 0u); } }
      else if constexpr (s <= 13) wait_vm<SC.wait_stage(Tg, s + 1)>();
      bar();
      STAMP2(24 + s);
      if (s == 8) CSTAMP(56); if (s == 11) CSTAMP(57); if (s == 13) CSTAMP(58); if (s == 15) CSTAMP(59);
      if (s == 14) early = word_get(0) != 0u;
      if (s == 15) cs_get(3, NJ * 3);
    }
    if (NJ) op_at(tc, IC<0>{});
    static_for<0, 3>([&](auto bc) {
      constexpr int b = decltype(bc)::value;
      acc[b] = mfma(wg[t & 1][b], xf[t], acc[b]);
      if (t < 31) ld1b(t + 1, b);
      if (p == ((b + 2 * NJ) & 3) || (s == 15 && p == 2 && ((b + 2 * NJ) & 3) == 3)) { if (!(s == 15 && p == 3)) cs[b] = mfma16(sel, wg[t & 1][b], cs[b]); }
      SB0();
    });
    if (!NJ) op_at(tc, IC<0>{});
    op_at(tc, IC<1>{});
    if (Tg == 39) flag_store(rs_ws, WS_FQKV + (unsigned)((rb * 16 + g) * 64 + w * 8), tg, lane);
    if (Tg == 55 && w == 0) fq = ld16c_asm(rs_ws, qv, 0u);
    SB0();
  });
  STAMP(5); CSTAMP(19);
  const float r2 = rstd * rstd;
#pragma unroll
  for (int b = 0; b < 3; ++b) {
    float v[16];
#pragma unroll
    for (int i = 0; i < 4; ++i) { const f32x4 c4 = lds_f4(L_CSW + w * 384 + b * 128 + (8 * i + 4 * h) * 4);
#pragma unroll
      for (int e = 0; e < 4; ++e) v[4 * i + e] = acc[b][4 * i + e] - mean * c4[e]; }
    float hv[8];
#pragma unroll
    for (int i = 0; i < 8; ++i) hv[i] = fmaxf(v[i], 0.f) * v[8 + i] * r2;
    const u32x4 o = {pk(hv[0], hv[1]), pk(hv[2], hv[3]), pk(hv[4], hv[5]), pk(hv[6], hv[7])};
    if (NJ * 3 + b < nu) st16c(o, rs_ws, (unsigned)lane * 16u, WS_H + (unsigned)(sb * NU + u0 + NJ * 3 + b) * 1024u);
  }
  PROBE(3);
  STAMP(6);
  if (NJ == 0 || nu == 6) wait_vm<3>(); else wait_vm<2>();
  return early;
}

template <bool FULL, class F1, class FM, class F2>
DI u32x4 attn_task(int j0, int qb, int hd, int lane, F1 side1, FM mid, F2 side2) {
  const int q = lane & 31, hh = lane >> 5, tq = q - 4 * hh;
  const bf16x8 qf = lds_b128(LA_Q + (unsigned)(hd * 4 + qb) * 1024u + lane * 16u);
  const unsigned kbase = LA_K + (unsigned)(hd * 12 + qb) * 1024u + lane * 16u;
  const float NEG = -__builtin_inff();
  bf16x8 kf[9];
#pragma unroll
  for (int j = 0; j < 9; ++j) if (FULL || j >= j0) kf[j] = lds_b128(kbase + j * 1024u);
  f32x16 c;
#pragma unroll
  for (int i = 0; i < 16; ++i) c[i] = 0.f;
  float m;
  {
    const f32x16 s8 = mfma(kf[8], qf, c);
    f32x16 s0;
    if (FULL) s0 = mfma(kf[0], qf, c);
    m = NEG;
#pragma unroll
    for (int i = 0; i < 16; i += 2) {
      const int c0 = (i & 3) + 8 * (i >> 2);
      const float a = c0 <= tq ? s8[i] : (FULL ? s0[i] : NEG), b = c0 + 1 <= tq ? s8[i + 1] : (FULL ? s0[i + 1] : NEG);
      m = fmaxf(fmaxf(m, a), b);
    }
    side1(0);
  }
#pragma unroll
  for (int j = 1; j < 8; ++j) {
    if (FULL || j >= j0) {
      const f32x16 s = mfma(kf[j], qf, c);
#pragma unroll
      for (int i = 0; i < 16; i += 2) m = fmaxf(fmaxf(m, s[i]), s[i + 1]);
    }
    side1(j);
  }
  m = xhalf_max(m);
  mid();
#pragma unroll
  for (int i = 0; i < 16; ++i) c[i] = -m;
  const int grp = (lane >> 4) & 1, q4 = (lane & 15) >> 2, p4 = lane & 3;
  const unsigned vlane = (unsigned)((p4 & 1) * 512 + (((4 * hh + q4) ^ (4 * (p4 & 1))) * 16) + (p4 >> 1) * 8);
  const unsigned vb0 = grp ? L_ONES + 128u : LA_V + (unsigned)(hd * 12 + qb) * 1024u + vlane;
  const unsigned vb1 = grp ? L_ONES : LA_V + (unsigned)(hd * 12 + qb) * 1024u + vlane + 128u;
  const unsigned vstr = grp ? 0u : 1024u, vs1 = grp ? 0u : 256u;
  f32x16 acc;
#pragma unroll
  for (int i = 0; i < 16; ++i) acc[i] = 0.f;
  auto pv = [&](int j, const unsigned (&pp)[8]) {
#pragma unroll
    for (int s = 0; s < 2; ++s) {
      const bf16x8 vf = cat(lds_tr(vb0 + j * vstr + s * vs1), lds_tr(vb1 + j * vstr + s * vs1));
      const u32x4 pu = {pp[4 * s], pp[4 * s + 1], pp[4 * s + 2], pp[4 * s + 3]};
      acc = mfma(vf, __builtin_bit_cast(bf16x8, pu), acc);
    }
  };
#pragma unroll
  for (int j = 1; j < 8; ++j) {
    if (FULL || j >= j0) {
      const f32x16 s = mfma(kf[j], qf, c);
      unsigned pp[8];
#pragma unroll
      for (int i = 0; i < 8; ++i) pp[i] = pk(__builtin_amdgcn_exp2f(s[2 * i]), __builtin_amdgcn_exp2f(s[2 * i + 1]));
      pv(j, pp);
    }
    side2(j);
  }
  {
    const f32x16 s8 = mfma(kf[8], qf, c);
    f32x16 s0;
    if (FULL) s0 = mfma(kf[0], qf, c);
    unsigned p8[8], p0[8];
#pragma unroll
    for (int i = 0; i < 8; ++i) {
      const int c0 = ((2 * i) & 3) + 8 * (i >> 1);
      const bool va = c0 <= tq, vb = c0 + 1 <= tq;
      const float a = va ? s8[2 * i] : (FULL ? s0[2 * i] : NEG), b = vb ? s8[2 * i + 1] : (FULL ? s0[2 * i + 1] : NEG);
      const unsigned pp = pk(__builtin_amdgcn_exp2f(a), __builtin_amdgcn_exp2f(b));
      const unsigned mk = (va ? 0xffffu : 0u) | (vb ? 0xffff0000u : 0u);
      p8[i] = pp & mk; p0[i] = pp & ~mk;
    }
    pv(8, p8);
    if (FULL) pv(0, p0);
  }
  const float inv = __builtin_amdgcn_rcpf(acc[8]);
  return u32x4{pk(acc[0] * inv, acc[1] * inv), pk(acc[2] * inv, acc[3] * inv), pk(acc[4] * inv, acc[5] * inv), pk(acc[6] * inv, acc[7] * inv)};
}

struct Sched3 {
  static constexpr int a_stage(int st) { return st <= 6 ? st + 4 : (st >= 8 && st <= 11 ? st + 3 : -1); }
  static constexpr int w_stage(int st) { return st <= 10 ? st + 4 : -1; }
  int before[16] = {}, lastA[15] = {}, lastW[15] = {};
  constexpr Sched3() {
    int c = 9;
    for (int i = 0; i < 4; ++i) { lastA[i] = 2 * i + 2; lastW[i] = 0; }
    for (int st = 0; st < 15; ++st) {
      before[st] = c;
      if (st == 3) c += 1;
      if (a_stage(st) >= 0) { c += 2; lastA[a_stage(st)] = c; }
      if (w_stage(st) >= 0) { c += 2; lastW[w_stage(st)] = c; }
      if (st == 8) c += 2;
    }
    before[15] = c;
  }
  constexpr int wait(int st) const { return before[st] - (lastA[st + 1] > lastW[st + 1] ? lastA[st + 1] : lastW[st + 1]); }
};

DI void phase23(const Ptrs& P, bool early, int rb1, int g1, u64 epoch, int rb, int hp, int rb64, int cb, int rot, int w, int lane, Tag tg, Stamps& T) {
  const rsrc_t rs_ws = mk_rsrc(P.ws, WS_BYTES), rs_d = mk_rsrc(P.wd, FF * DM * 2), rs_o = mk_rsrc(P.wo, DM * DM * 2);
  if (threadIdx.x < 64) *(LAS unsigned*)(lds + L_ONES + threadIdx.x * 4) = ONES2;
  NOTE(63, early ? 1 : 2);
  if (!early) {
    bool act; const unsigned qv = qkv_flag_voff(rb, hp, lane, act);
    if (w == 0) flags_spin(rs_ws, qv, act, tg);
    bar();
    const AttnSrc asrc = attn_src(rb, hp, w);
#pragma unroll
    for (int i = 0; i < 7; ++i) attn_issue(rs_ws, asrc, w, lane, i, true);
    wait_vm<0>();
  }
  bar();
  STAMP(9);
  unsigned vw[2];
#pragma unroll
  for (int j = 0; j < 2; ++j) { const int G = (2 * w + j) * 64 + lane, row = G >> 3;
    vw[j] = (unsigned)row * 1024u + (unsigned)(((G & 7) ^ (((row >> 1) & 1) << 2)) * 16); }
  const int sbl = w >> 2, sbg = 2 * rb64 + sbl;
  auto chunk = [&](int st) { return st < 10 ? (st + rot >= 10 ? st + rot - 10 : st + rot) : (st == 10 ? 10 : ((st - 11 + rot) & 3)); };
  auto pieceA = [&](int st, int j, bool go) {
    const int c = chunk(st), ks = 2 * (w & 3) + j;
    const unsigned src = st < 11 ? WS_H + (unsigned)(sbg * NU + c * 8 + ks) * 1024u : WS_O + (unsigned)(sbg * 32 + c * 8 + ks) * 1024u;
    dma16c(l3a(st & 3) + (unsigned)(sbl * 8 + ks) * 1024u, (unsigned)lane * 16u + (go ? 0u : 0x40000000u), rs_ws, src);
  };
  auto pieceW = [&](int st, int j) {
    dma16(l3w(st & 3) + (unsigned)(2 * w + j) * 1024u, vw[j], st < 11 ? rs_d : rs_o, (unsigned)cb * 128u + (unsigned)chunk(st) * 131072u);
  };
  auto side = [&](int j) { pieceW(j >> 1, j & 1); };
  const int qb = w & 3, hd = w >> 2, sb = 4 * rb + qb;
  const unsigned hv = WS_FH + (unsigned)((rb64 >> 1) * 1024 + lane * 16), ov = WS_FO + (unsigned)((rb64 >> 1) * 1024 + lane * 16);
  u32x4 o, fh;
  auto side2 = [&](int j) { if (j == 7) fh = ld16c_asm(rs_ws, hv, 0u); };
  auto mid = [&]() { STAMP(10); wait_vm<8>(); flag_store(rs_ws, WS_FH + (unsigned)((rb1 * 16 + g1) * 64 + w * 8), tg, lane); };
  if (sb >= 8) o = attn_task<true>(0, qb, hd, lane, side, mid, side2); else o = attn_task<false>(8 - sb, qb, hd, lane, side, mid, side2);
  PROBE(4);
  STAMP(11);
  wait_vm_tie<0>(fh);
  NOTE(55, flags_ok(fh, true, tg) ? 1 : 2);
  if (!flags_ok(fh, true, tg)) flags_spin(rs_ws, hv, true, tg);
  pieceA(0, 0, true); pieceA(0, 1, true);
  bar();
  STAMP(13);
#pragma unroll
  for (int s = 1; s < 4; ++s) { pieceA(s, 0, true); pieceA(s, 1, true); }
  st16c(o, rs_ws, (unsigned)lane * 16u, WS_O + (unsigned)(sb * 32 + 2 * hp + hd) * 1024u);
  const int mb = w & 1, nb2 = (w >> 1) & 1, kh = w >> 2;
  const int h = lane >> 5, nsub = (lane >> 4) & 1, q4 = (lane & 15) >> 2, p4 = lane & 3;
  const unsigned aa = (unsigned)(mb * 8 + kh * 4) * 1024u + lane * 16u;
  const unsigned wa = (unsigned)(16 * kh + h) * 512u + q4 * 128u + (unsigned)((nb2 ^ (q4 >> 1)) * 64) + nsub * 32u + p4 * 8u;
  const int row = 64 * rb64 + 32 * mb + (lane & 31);
  f32x16 acc;
#pragma unroll
  for (int i = 0; i < 16; ++i) acc[i] = 0.f;
  bf16x8 af[2][4], wfr[2][4];
  auto ld3 = [&](int st, int ks) {
    af[st & 1][ks] = lds_b128(l3a(st & 3) + aa + ks * 1024u); const unsigned a = l3w(st & 3) + wa + ks * 2048u; wfr[st & 1][ks] = cat(lds_tr(a), lds_tr(a + 1024u));
  };
  const int col0 = 64 * cb + 32 * nb2 + 16 * kh + 4 * h;
  wait_vm<7>();
  bar();
  STAMP(14);
#pragma unroll
  for (int ks = 0; ks < 4; ++ks) ld3(0, ks);
  u32x4 fo, fe[4];
  const unsigned short* xr = (const unsigned short*)P.x + (size_t)row * DM + col0;
  u32x2 x0, x1;
  const bool keeper = blockIdx.x == 0 && w == 0;
  static constexpr Sched3 S3{};
  static_for<0, 15>([&](auto sc) {
    constexpr int st = decltype(sc)::value;
    if constexpr (st < 14) {
      wait_vm<S3.wait(st)>();
      if (st == 8 && w == 0) { wait_vm_tie<2>(fo); NOTE(54, flags_ok(fo, true, tg) ? 1 : 2); if (!flags_ok(fo, true, tg)) flags_spin(rs_ws, ov, true, tg); }
      bar();
      STAMP2(40 + st);
    }
    if (st == 3) flag_store(rs_ws, WS_FO + (unsigned)((rb * 16 + hp) * 64 + w * 8), tg, lane);
    SB0();
#pragma unroll
    for (int ks = 0; ks < 4; ++ks) {
      if (ks < 2 || st != 10 || kh == 0) acc = mfma(wfr[st & 1][ks], af[st & 1][ks], acc);
      if (st < 14) ld3(st + 1, ks);
      SB0();
      if (ks < 2) { if (Sched3::a_stage(st) >= 0) pieceA(Sched3::a_stage(st), ks, true); }
      else if (Sched3::w_stage(st) >= 0) pieceW(Sched3::w_stage(st), ks - 2);
      SB0();
    }
    if (st == 6 && w == 0) fo = ld16c_asm(rs_ws, ov, 0u);
    if (st == 8) { x0 = *(const GAS u32x2*)xr; x1 = *(const GAS u32x2*)(xr + 8); }
    if (st == 9 && keeper) {
#pragma unroll
      for (int i = 0; i < 4; ++i) fe[i] = ld16c_asm(rs_ws, WS_FQKV + (unsigned)(i * 64 + lane) * 64u, 0u);
    }
    SB0();
  });
  STAMP(15);
  bar();
  {
    const unsigned base = L3_RED + (unsigned)((w & 3) * 2 + kh) * 2048u + lane * 16u;
    if (kh) { *(LAS f32x4*)(lds + base) = f32x4{acc[0], acc[1], acc[2], acc[3]}; *(LAS f32x4*)(lds + base + 1024u) = f32x4{acc[4], acc[5], acc[6], acc[7]}; }
    else { *(LAS f32x4*)(lds + base) = f32x4{acc[8], acc[9], acc[10], acc[11]}; *(LAS f32x4*)(lds + base + 1024u) = f32x4{acc[12], acc[13], acc[14], acc[15]}; }
  }
  bar();
  {
    const unsigned base = L3_RED + (unsigned)((w & 3) * 2 + (kh ^ 1)) * 2048u + lane * 16u;
    const f32x4 a0 = lds_f4(base), a1 = lds_f4(base + 1024u);
    unsigned short* orow = (unsigned short*)P.out + (size_t)row * DM + col0;
    float v[8];
#pragma unroll
    for (int e = 0; e < 4; ++e) { v[e] = (kh ? acc[8 + e] : acc[e]) + a0[e]; v[4 + e] = (kh ? acc[12 + e] : acc[4 + e]) + a1[e]; }
    v[0] += bf_lo(x0[0]); v[1] += bf_hi(x0[0]); v[2] += bf_lo(x0[1]); v[3] += bf_hi(x0[1]);
    v[4] += bf_lo(x1[0]); v[5] += bf_hi(x1[0]); v[6] += bf_lo(x1[1]); v[7] += bf_hi(x1[1]);
    *(GAS u32x2*)orow = u32x2{pk(v[0], v[1]), pk(v[2], v[3])};
    *(GAS u32x2*)(orow + 8) = u32x2{pk(v[4], v[5]), pk(v[6], v[7])};
  }
  if (keeper) {
#pragma unroll
    for (int i = 0; i < 4; ++i) { tie(fe[i]); if (!flags_ok(fe[i], true, tg)) flags_spin(rs_ws, WS_FQKV + (unsigned)(i * 64 + lane) * 64u, true, tg); }
    if (lane == 0) __hip_atomic_store((GAS u64*)(P.ws + WS_EPOCH), epoch, __ATOMIC_RELAXED, __HIP_MEMORY_SCOPE_AGENT);
  }
}

__global__ __launch_bounds__(512, 2) void mk_kernel(const void* x, const void* wqkv, const void* wo, const void* wg, const void* wu, const void* wd, void* out, char* ws) {
  u64 pc0 = 0, t00 = 0;
  t00 = __builtin_amdgcn_s_memrealtime();
  if (PROBE_AT == 0) { while (__builtin_amdgcn_s_memrealtime() - t00 < 4000ull) __builtin_amdgcn_s_sleep(4); }
  asm volatile("mk_code_begin:\n\ts_getpc_b64 %0" : "=s"(pc0));
  {
    unsigned len;
    asm volatile("s_mov_b32 %0, mk_code_end-mk_code_begin" : "=s"(len));
    const unsigned wv = __builtin_amdgcn_readfirstlane(threadIdx.x >> 6);
    const unsigned per = ((len >> 5) + 1023u) & ~1023u;
    const unsigned o = (blockIdx.x >> 3) * per + wv * 1024u;
    dma16(L_DUMMY + wv * 1024u, (threadIdx.x & 63u) * 16u, mk_rsrc((const void*)(pc0 & ~255ull), len & ~15u), wv * 1024u < per ? o : 0x40000000u);
  }
  const Ptrs P{x, wqkv, wo, wg, wu, wd, out, ws, pc0, t00};
  PROBE(5);
  Stamps T;
  STAMP(0); CSTAMP(20);
  __builtin_amdgcn_s_setprio(1);
  const int b = blockIdx.x, xcd = b & 7, idx = b >> 3;
  const int w = __builtin_amdgcn_readfirstlane(threadIdx.x >> 6), lane = threadIdx.x & 63;
  Tag tg; u64 epoch;
  const int rb = 2 * (idx >> 2) + (xcd >> 2), g = (xcd & 3) * 4 + (idx & 3), rot1 = ((idx >> 2) + 2 * (idx & 3)) & 7;
  bool early;
  if (w < 4) early = phase1<0>(P, rb, g, rot1, w, lane, rb, g, tg, epoch, T); else early = phase1<1>(P, rb, g, rot1, w - 4, lane, rb, g, tg, epoch, T);
  const int rot3 = ((idx & 7) + 3 * (idx >> 3)) % 10;
  phase23(P, early, rb, g, epoch, rb, g, xcd * 4 + (idx >> 3), idx & 7, rot3, w, lane, tg, T);
  asm volatile("mk_code_end:");
}

}

extern "C" void kernel_launch(void* const* d_in, const int* in_sizes, int n_in, void* d_out, int out_size, void* d_ws, size_t ws_size, hipStream_t stream) {
  const void *x = d_in[0], *wqkv = d_in[1], *wo = d_in[2], *wg = d_in[4], *wu = d_in[5], *wd = d_in[6];
  mk_kernel<<<256, 512, 0, stream>>>(x, wqkv, wo, wg, wu, wd, d_out, (char*)d_ws);
}
```

```cpp
#include <hip/hip_runtime.h>
#include <hip/hip_bf16.h>
#include <stdint.h>

namespace {

typedef short bf16x8 __attribute__((ext_vector_type(8)));
typedef short s16x4 __attribute__((ext_vector_type(4)));
typedef float f32x16 __attribute__((ext_vector_type(16)));
typedef float f32x4 __attribute__((ext_vector_type(4)));
typedef float f32x2 __attribute__((ext_vector_type(2)));
typedef unsigned u32x4 __attribute__((ext_vector_type(4)));
typedef unsigned u32x2 __attribute__((ext_vector_type(2)));
typedef __bf16 bf16x2_t __attribute__((ext_vector_type(2)));
typedef short v4i16_t __attribute__((ext_vector_type(4)));
typedef __amdgpu_buffer_rsrc_t rsrc_t;
#define LAS __attribute__((address_space(3)))
#define GAS __attribute__((address_space(1)))
#define DI __device__ __forceinline__
#define SB0() __builtin_amdgcn_sched_barrier(0)
typedef unsigned long long u64;

constexpr int SEQ = 2048, DM = 512, FF = 1376, NU = 86;
constexpr unsigned WS_EPOCH = 0;
constexpr unsigned WS_FQKV = 16384, WS_FH = 32768, WS_FO = 49152;
constexpr unsigned WS_RS = 65536;
constexpr unsigned WS_QKV = 1u << 20;
constexpr unsigned WS_H = 7u << 20;
constexpr unsigned WS_O = 13u << 20;
constexpr unsigned WS_BYTES = 16u << 20;
constexpr unsigned SLOT1 = 28672;
constexpr unsigned L_CSP = 143360;
constexpr unsigned L_CSW = 146432;
constexpr unsigned L_ONES = 152576;
constexpr unsigned ONES_BYTES = 9984;
constexpr unsigned L_WORD = 163584;
constexpr unsigned LDS_BYTES = 163840;
constexpr unsigned LA_Q = 28672, LA_K = LA_Q + 8192, LA_V = LA_K + 24576;
DI constexpr unsigned l3w(int i) { return i == 0 ? 0u : 86016u + (unsigned)(i - 1) * 16384u; }
DI constexpr unsigned l3a(int i) { return i == 0 ? 135168u : 28672u + (unsigned)(i - 1) * 16384u; }
constexpr unsigned L3_RED = 0;

__shared__ __attribute__((aligned(1024))) char lds[LDS_BYTES];

DI unsigned pk(float a, float b) { f32x2 v = {a, b}; bf16x2_t r = __builtin_convertvector(v, bf16x2_t); return __builtin_bit_cast(unsigned, r); }
DI float dot2(unsigned a, unsigned b, float c) { return __builtin_amdgcn_fdot2_f32_bf16(__builtin_bit_cast(bf16x2_t, a), __builtin_bit_cast(bf16x2_t, b), c, false); }
DI rsrc_t mk_rsrc(const void* p, unsigned bytes) { return __builtin_amdgcn_make_buffer_rsrc((void*)p, 0, bytes, 0x00020000); }
DI void dma16(unsigned ldsaddr, unsigned voff, rsrc_t rs, unsigned soff) {
  asm volatile("s_mov_b32 m0, %0\n\ts_nop 0\n\tbuffer_load_dwordx4 %1, %2, %3 offen lds" ::"s"(ldsaddr), "v"(voff), "s"(rs), "s"(soff) : "memory");
}
DI void dma16c(unsigned ldsaddr, unsigned voff, rsrc_t rs, unsigned soff) {
  asm volatile("s_mov_b32 m0, %0\n\ts_nop 0\n\tbuffer_load_dwordx4 %1, %2, %3 offen sc1 lds" ::"s"(ldsaddr), "v"(voff), "s"(rs), "s"(soff) : "memory");
}
DI void pf_lines(unsigned ldsaddr, unsigned voff, rsrc_t rs) {
  asm volatile("s_mov_b32 m0, %0\n\ts_nop 0\n\tbuffer_load_dword %1, %2, 0 offen lds" ::"s"(ldsaddr), "v"(voff), "s"(rs) : "memory");
}
template <int N> DI void wait_vm() { asm volatile("s_waitcnt vmcnt(%0)" ::"n"(N) : "memory"); }
template <int N> DI void wait_vm_tie(u32x4& v) { asm volatile("s_waitcnt vmcnt(%1)" : "+v"(v) : "n"(N) : "memory"); }
template <int V> struct IC { static constexpr int value = V; };
template <int I, int N, class F> DI void static_for(F&& f) { if constexpr (I < N) { f(IC<I>{}); static_for<I + 1, N>(f); } }
DI void bar_raw() { asm volatile("s_barrier" ::: "memory"); }
DI void tie(u32x4& v) { asm volatile("" : "+v"(v) :: "memory"); }
DI void bar() { asm volatile("s_waitcnt lgkmcnt(0)\n\ts_barrier" ::: "memory"); }
DI f32x16 mfma(bf16x8 a, bf16x8 b, f32x16 c) { return __builtin_amdgcn_mfma_f32_32x32x16_bf16(a, b, c, 0, 0, 0); }
DI bf16x8 lds_b128(unsigned a) { return *(const LAS bf16x8*)(lds + a); }
#define MK_REAL 1
DI s16x4 lds_tr(unsigned a) { return __builtin_amdgcn_ds_read_tr16_b64_v4i16((LAS v4i16_t*)(lds + a)); }
DI f32x4 mfma16(bf16x8 a, bf16x8 b, f32x4 c) { return __builtin_amdgcn_mfma_f32_16x16x32_bf16(a, b, c, 0, 0, 0); }
DI f32x4 lds_f4(unsigned a) { return *(const LAS f32x4*)(lds + a); }
DI bf16x8 cat(s16x4 lo, s16x4 hi) { return __builtin_shufflevector(lo, hi, 0, 1, 2, 3, 4, 5, 6, 7); }
DI float xhalf_sum(float v) { auto r = __builtin_amdgcn_permlane32_swap(__float_as_uint(v), __float_as_uint(v), false, false); return __uint_as_float(r[0]) + __uint_as_float(r[1]); }
DI float xhalf_max(float v) { auto r = __builtin_amdgcn_permlane32_swap(__float_as_uint(v), __float_as_uint(v), false, false); return fmaxf(__uint_as_float(r[0]), __uint_as_float(r[1])); }
DI void st16c(u32x4 v, rsrc_t rs, unsigned voff, unsigned soff) { __builtin_amdgcn_raw_buffer_store_b128(v, rs, voff + soff, 0, 16); }
DI void st8c(u32x2 v, rsrc_t rs, unsigned voff, unsigned soff) { __builtin_amdgcn_raw_buffer_store_b64(v, rs, voff, soff, 16); }
constexpr unsigned ONES2 = 0x3F803F80u;
template <int CTRL> DI float dpp_add(float v) { return v + __int_as_float(__builtin_amdgcn_update_dpp(0, __float_as_int(v), CTRL, 0xf, 0xf, true)); }
DI float wave_sum(float v) {
  v = dpp_add<0xB1>(v); v = dpp_add<0x4E>(v); v = dpp_add<0x141>(v); v = dpp_add<0x140>(v);
  auto a = __builtin_amdgcn_permlane16_swap(__float_as_uint(v), __float_as_uint(v), false, false);
  v = __uint_as_float(a[0]) + __uint_as_float(a[1]);
  return xhalf_sum(v);
}
DI float bf_lo(unsigned u) { return __uint_as_float(u << 16); }
DI float bf_hi(unsigned u) { return __uint_as_float(u & 0xffff0000u); }
DI u32x4 ld16_asm(rsrc_t rs, unsigned voff, unsigned soff) { u32x4 v; asm volatile("buffer_load_dwordx4 %0, %1, %2, %3 offen" : "=v"(v) : "v"(voff), "s"(rs), "s"(soff) : "memory"); return v; }
DI u32x4 ld16c_asm(rsrc_t rs, unsigned voff, unsigned soff) { u32x4 v; asm volatile("buffer_load_dwordx4 %0, %1, %2, %3 offen sc1" : "=v"(v) : "v"(voff), "s"(rs), "s"(soff) : "memory"); return v; }
#define STAMP2(i) do {} while (0)
struct Stamps {};
#define STAMP(i) do {} while (0)
#define CSTAMP(i) do {} while (0)
#define NOTE(i, v) do {} while (0)

struct Ptrs { const void *x, *wqkv, *wo, *wg, *wu, *wd; void* out; char* ws; u64 pc; };
struct Tag { unsigned a, b; };

DI void flag_store(rsrc_t rs_ws, unsigned off, Tag tg, int lane) { if (lane == 0) st8c(u32x2{tg.a, tg.b}, rs_ws, 0u, off); }
DI bool flags_ok(u32x4 v, bool act, Tag tg) { return __all(!act || (v[0] == tg.a && v[1] == tg.b && v[2] == tg.a && v[3] == tg.b)); }
DI void flags_spin(rsrc_t rs_ws, unsigned voff, bool act, Tag tg) {
  for (unsigned spins = 0; spins < (1u << 20); ++spins) {
    u32x4 v = ld16c_asm(rs_ws, voff, 0u); wait_vm_tie<0>(v);
    if (flags_ok(v, act, tg)) break;
    __builtin_amdgcn_s_sleep(1);
  }
}
DI void word_set(int i, unsigned v) { *(volatile LAS unsigned*)(lds + L_WORD + i * 4) = v; }
DI unsigned word_get(int i) { return __builtin_amdgcn_readfirstlane(*(volatile LAS unsigned*)(lds + L_WORD + i * 4)); }

struct AttnSrc { unsigned q, kv; int sb0; };
DI AttnSrc attn_src(int rb, int hp, int w) {
  AttnSrc a;
  a.q = __builtin_amdgcn_readfirstlane(WS_QKV + (unsigned)((2 * hp + (w >> 2)) * 64 + 4 * rb + (w & 3)) * 1024u);
  a.kv = __builtin_amdgcn_readfirstlane(WS_QKV + (unsigned)(((1 + (w >> 2)) * 32 + 2 * hp + ((w >> 1) & 1)) * 64) * 1024u);
  a.sb0 = __builtin_amdgcn_readfirstlane(4 * rb - 8 + 6 * (w & 1));
  return a;
}
DI void attn_issue(rsrc_t rs_ws, const AttnSrc& a, int w, int lane, int piece, bool go) {
  const unsigned vo = (unsigned)lane * 16u + (go ? 0u : 0x40000000u);
  if (piece == 0) { dma16c(LA_Q + (unsigned)w * 1024u, vo, rs_ws, a.q); return; }
  const int sbk = a.sb0 + piece - 1;
  dma16c(LA_K + (unsigned)((w >> 1) * 12 + 6 * (w & 1) + piece - 1) * 1024u, vo, rs_ws, a.kv + (unsigned)(sbk < 0 ? 0 : sbk) * 1024u);
}
DI unsigned qkv_flag_voff(int rb, int hp, int lane, bool& act) {
  const int c = lane >> 2, kind = c == 0 ? 0 : (c <= 3 ? 1 : 2), d = c == 0 ? 0 : (c - 1) % 3, rr = rb - d;
  act = lane < 28 && rr >= 0;
  return act ? WS_FQKV + (unsigned)((rr * 16 + (kind * 16 + hp) / 3) * 64 + (lane & 3) * 16) : 0u;
}

constexpr unsigned L_DUMMY = 152576;
#define TLBW_N 0
#define PFX_N 0
#define P1_HOLD 12
constexpr int OP_NONE = -1, OP_PFW = 200, OP_ATT = 400;
template <int NJ> struct Sched1 {
  static constexpr int NA = NJ ? 4 : 3, NST = NJ ? 2 : 4;
  static constexpr int n(int sg) { return sg < 8 ? NA : 3; }
  static constexpr int TB(int s) { return s < 8 ? 4 * s + 2 : 4 * s + 3; }
  static constexpr int extras(int T) { return (T == 5 ? 1 : 0)   + (T == 22 ? 1 : 0)   + (T == 39 ? 1 : 0)  ; }
  static constexpr int NPRO = 4;
  static constexpr int NTW = TLBW_N;
  static constexpr int NPX = PFX_N;
  static constexpr int PRO = 1 + NPX + NPRO * NA + 1 + NTW;
  static constexpr int HOLD = P1_HOLD;
  int op[64][2] = {}, before[65] = {}, last[16] = {};
  constexpr Sched1() {
    int sg = NPRO, j = 0, pf = 0, att = 0, c = PRO;
    last[0] = 1 + NPX + NA;
    for (int i = 1; i < NPRO; ++i) last[i] = 1 + NPX + NA + 1 + NTW + i * NA;
    for (int T = 0; T < 64; ++T) {
      before[T] = c;
      for (int k = 0; k < 2; ++k) {
        int o = OP_NONE;
        if (k == 0 || T < 14) {
          if (sg <= 15 && (sg < 5 || T >= TB(sg - 4)) && (sg < 8 || T >= 4 * sg - HOLD)) {   o = sg * 8 + j; if (++j == n(sg)) { last[sg] = c + 1; ++sg; j = 0; } }
          else if (pf == 0 && T >= 4) o = OP_PFW + pf++;
        }
        if (T >= 59 && att < 7 && (k == 0 || T >= 61)) o = OP_ATT + att++;
        op[T][k] = o;
        if (o != OP_NONE) ++c;
      }
      c += extras(T);
      if (T == 31) c += NST;
    }
    before[64] = c;
  }
  constexpr int wait_stage(int T, int sg) const { return before[T] - last[sg]; }
};

template <int NJ>
DI bool phase1(const Ptrs& P, int rb, int g, int rot, int mi, int lane, int rb2, int hp2, Tag& tg, u64& epoch, Stamps& T_) {
  Stamps& T = T_;
  typedef Sched1<NJ> L;
  static constexpr Sched1<NJ> SC{};
  const int w = NJ * 4 + mi;
  u64 e0 = 0;
  if (w == 0) asm volatile("s_load_dwordx2 %0, %1, 0x0" : "=s"(e0) : "s"(P.ws) : "memory");
  const rsrc_t rs_x = mk_rsrc(P.x, SEQ * DM * 2), rs_q = mk_rsrc(P.wqkv, DM * 1536 * 2);
  const rsrc_t rs_f = mk_rsrc(NJ ? P.wu : P.wg, DM * FF * 2), rs_ws = mk_rsrc(P.ws, WS_BYTES);
  const unsigned sx0 = (unsigned)rb * 131072u, sq0 = (unsigned)g * 192u;
  constexpr int NQ = NJ ? 2 : 1;
  constexpr int NA = 2 + NQ;
  unsigned vx[2], vq[2], vf[3];
#pragma unroll
  for (int j = 0; j < 2; ++j) vx[j] = (unsigned)(16 * w + 8 * j + (lane >> 3)) * 1024u + (unsigned)(((lane & 7) ^ (4 * j + (lane >> 4))) * 16);
#pragma unroll
  for (int j = 0; j < NQ; ++j) { const int ins = NJ ? 2 * mi + j : 8 + mi; const int G = ins * 64 + lane;
    vq[j] = (unsigned)(G / 12) * 3072u + (unsigned)(G % 12) * 16u; }
  const int u0 = (g * NU) >> 4, nu = (((g + 1) * NU) >> 4) - u0;
  const unsigned sf0 = (unsigned)u0 * 32u;
  auto piece = [&](int gs, int j) {
    const unsigned slot = (unsigned)(gs % 5) * SLOT1;
    const unsigned c = (unsigned)(gs + rot) & 7u;
    if (gs < 8) {
      if (j < 2) dma16(slot + (unsigned)(2 * w + j) * 1024u, vx[j], rs_x, sx0 + c * 128u);
      else dma16(slot + 16384u + (unsigned)(NJ ? 2 * mi + j - 2 : 8 + mi) * 1024u, vq[j - 2], rs_q, sq0 + c * (64u * 3072u));
    } else dma16(slot + NJ * 12288u + (unsigned)(3 * mi + j) * 1024u, vf[j], rs_f, sf0 + c * (64u * 2752u));
  };
#pragma unroll
  for (int j = 0; j < NA; ++j) piece(0, j);
  const unsigned myrow = (unsigned)(128 * rb + 8 * g + w);
  u32x4 xrow = ld16_asm(rs_x, (unsigned)lane * 16u, myrow * 1024u);
#pragma unroll
  for (int sg = 1; sg < L::NPRO; ++sg)
#pragma unroll
    for (int j = 0; j < NA; ++j) piece(sg, j);
  SB0();
  STAMP(1);
#pragma unroll
  for (int j = 0; j < 3; ++j) { const int G = (3 * mi + j) * 64 + lane, kg = G / 48, rr = G % 48, unit = (rr >> 3) ^ NJ;
    vf[j] = unit < nu ? (unsigned)(4 * kg + ((rr & 7) >> 1)) * 2752u + (unsigned)(unit * 32 + (rr & 1) * 16) : 0x40000000u; }
  const int r = lane & 31, h = lane >> 5, nsub = (lane >> 4) & 1, q4 = (lane & 15) >> 2, p4 = lane & 3;
  unsigned xa[4], ro[4];
#pragma unroll
  for (int p = 0; p < 4; ++p) { const int ks = (p + mi) & 3;
    xa[p] = (unsigned)(32 * mi + r) * 128u + (unsigned)(((2 * ks + h) ^ ((r >> 1) & 7)) * 16);
    ro[p] = (unsigned)ks * 3072u; }
  const unsigned wl = (unsigned)(2 * h) * 768u + q4 * 32u + p4 * 8u;
  const unsigned wa = 16384u + (unsigned)(2 * h) * 768u + q4 * 192u + nsub * 32u + p4 * 8u;
  const unsigned wbE = nsub * 12288u + wl + nsub * 128u;
  const unsigned wbO = nsub * 12288u + wl - nsub * 128u;
  const AttnSrc asrc = attn_src(rb2, hp2, w);
  bool early = false;
  auto warm = [&](int i) {
    const unsigned xl = blockIdx.x >> 3, dst = L_DUMMY + (unsigned)w * 256u;
    if (i == 0) {
      const unsigned q = (unsigned)(w & 1) * 64u + (unsigned)lane, rr = q / 7u, ln = q - rr * 7u;
      const unsigned ub = (unsigned)((((blockIdx.x & 3) * 4 * NU) >> 4) * 32) & ~127u;
      pf_lines(dst, (w < 4 && q < 112u) ? (16u * xl + rr) * 2752u + ub + ln * 128u : 0x40000000u, mk_rsrc((w & 2) ? P.wu : P.wg, DM * FF * 2));
    } else if (i == 1) {
      pf_lines(dst, lane < 43 ? ((xl * 8u + (unsigned)w) * 43u + (unsigned)lane) * 128u : 0x40000000u, mk_rsrc(P.wd, FF * DM * 2));
    } else {
      pf_lines(dst, lane < 16 ? ((xl * 8u + (unsigned)w) * 16u + (unsigned)lane) * 128u : 0x40000000u, mk_rsrc(P.wo, DM * DM * 2));
    }
  };
  auto op_at = [&](auto tc, auto kc) {
    constexpr int o = SC.op[decltype(tc)::value][decltype(kc)::value];
    if constexpr (o >= OP_ATT) attn_issue(rs_ws, asrc, w, lane, o - OP_ATT, early);
    else if constexpr (o >= OP_PFW) warm(o - OP_PFW);
    else if constexpr (o >= 0) piece(o >> 3, o & 7);
  };
  bf16x8 sel;
  { const unsigned v = (lane & 15) == ((lane >> 4) & 1) ? ONES2 : 0u; sel = __builtin_bit_cast(bf16x8, u32x4{v, v, v, v}); }
  if (w == 0) {
    asm volatile("s_waitcnt lgkmcnt(0)" : "+s"(e0) :: "memory");
    word_set(2, (unsigned)e0); word_set(3, (unsigned)(e0 >> 32));
  }

  bf16x8 xf[32];
  constexpr int NB1 = NJ ? 1 : 2;
  f32x16 acc[3];
  f32x4 cs[3];
#pragma unroll
  for (int b = 0; b < 3; ++b) { cs[b] = f32x4{0.f, 0.f, 0.f, 0.f};
#pragma unroll
    for (int i = 0; i < 16; ++i) acc[b][i] = 0.f; }
  bf16x8 wf[3][NB1], wg[2][3];
  auto ldx = [&](int t) { xf[t] = lds_b128((unsigned)((t >> 2) % 5) * SLOT1 + xa[t & 3]); };
  auto ld1a = [&](int t, int b) {
    const unsigned a = (unsigned)((t >> 2) % 5) * SLOT1 + wa + ro[t & 3] + (NJ ? 2 : b) * 64u; wf[t % 3][b] = cat(lds_tr(a), lds_tr(a + 768u));
  };
  auto ld1b = [&](int t, int b) {
    const int u = NJ * 3 + b;
    const unsigned a = (unsigned)((8 + (t >> 2)) % 5) * SLOT1 + ((u & 1) ? wbO : wbE) + ro[t & 3] + u * 128u; wg[t & 1][b] = cat(lds_tr(a), lds_tr(a + 768u));
  };
  auto cs_put = [&](int nb, int b0) {
#pragma unroll
    for (int b = 0; b < 3; ++b) if (b < nb && lane < 16) { *(LAS float*)(lds + L_CSP + (((b0 + b) * 32 + lane) * 4 + mi) * 4) = cs[b][0]; *(LAS float*)(lds + L_CSP + (((b0 + b) * 32 + 16 + lane) * 4 + mi) * 4) = cs[b][1]; }
  };
  auto cs_get = [&](int nb, int b0) {
#pragma unroll
    for (int b = 0; b < 3; ++b) if (b < nb) { const f32x4 c4 = lds_f4(L_CSP + ((b0 + b) * 32 + r) * 16);
      *(LAS float*)(lds + L_CSW + w * 384 + b * 128 + r * 4) = (c4[0] + c4[1]) + (c4[2] + c4[3]); }
  };
  wait_vm<(L::NPRO - 1) * NA + L::NTW>();
  bar();
  e0 = (u64)word_get(2) | ((u64)word_get(3) << 32);
  epoch = e0 + 1ull;
  tg.a = (unsigned)epoch; tg.b = ((unsigned)(e0 >> 32) ^ 0x5bd1e995u) + tg.a;
  STAMP(2); CSTAMP(23);
  ldx(0); ldx(1);
#pragma unroll
  for (int b = 0; b < NB1; ++b) { ld1a(0, b); ld1a(1, b); }
  u32x4 sv;
  float rmean = 0.f, rrstd = 0.f;
  const unsigned sv_off = WS_RS + (unsigned)(128 * rb + 32 * mi) * 16u;
  static_for<0, 32>([&](auto tc) {
    constexpr int t = decltype(tc)::value, s = t >> 2, p = t & 3;
    if constexpr (p == 2) {
      if (s == 7) cs_put(NB1, NJ ? 2 : 0);
      wait_vm<SC.wait_stage(t, s + 1)>();
      if (s == 7) bar(); else bar_raw();
      STAMP2(24 + s);
      if (s == 7) cs_get(NB1, NJ ? 2 : 0);
      if (s == 1) CSTAMP(60); if (s == 4) CSTAMP(61); if (s == 7) CSTAMP(62);
    }
    if (NJ) op_at(tc, IC<0>{});
    static_for<0, NB1>([&](auto bc) {
      constexpr int b = decltype(bc)::value;
      acc[b] = mfma(wf[t % 3][b], xf[t], acc[b]);
      if (t < 30) { if (b == 0) ldx(t + 2); ld1a(t + 2, b); }
      if (t == 31) { ld1b(0, b); if (b == NB1 - 1) { for (int bb = NB1; bb < 3; ++bb) ld1b(0, bb); } }
      if (p == (NJ ? 1 : b)) cs[b] = mfma16(sel, wf[t % 3][b], cs[b]);
      if (b == 0) {
        if (t == 2 + NJ) {
          tie(xrow);
          float e[8];
#pragma unroll
          for (int i = 0; i < 4; ++i) { e[2 * i] = bf_lo(xrow[i]); e[2 * i + 1] = bf_hi(xrow[i]); }
          rmean = wave_sum(((e[0] + e[1]) + (e[2] + e[3])) + ((e[4] + e[5]) + (e[6] + e[7]))) * (1.f / 512.f);
          asm volatile("" : "+v"(rmean));
        }
        if (t == 4) {
          float q = 0.f;
#pragma unroll
          for (int i = 0; i < 4; ++i) { const float d0 = bf_lo(xrow[i]) - rmean, d1 = bf_hi(xrow[i]) - rmean; q = fmaf(d0, d0, q); q = fmaf(d1, d1, q); }
          rrstd = __builtin_amdgcn_rsqf(wave_sum(q) * (1.f / 512.f) + 1e-5f);
          asm volatile("" : "+v"(rrstd));
        }
      }
      SB0();
      if (b == 0) op_at(tc, IC<NJ ? 1 : 0>{});
    });
    if (!NJ) op_at(tc, IC<1>{});
    if (t == 22) sv = ld16c_asm(rs_ws, (unsigned)r * 16u, sv_off);
    if (t == 5 && lane == 0) st16c(u32x4{__float_as_uint(rmean), tg.a, __float_as_uint(rrstd), tg.b}, rs_ws, 0u, WS_RS + myrow * 16u);
    SB0();
  });
  STAMP(3); CSTAMP(22);
  { constexpr int after = SC.before[32] - L::NST - SC.before[23]; wait_vm_tie<after>(sv); }
  NOTE(53, __all(sv[1] == tg.a && sv[3] == tg.b) ? 1 : 2);
  for (unsigned spins = 0; !__all(sv[1] == tg.a && sv[3] == tg.b) && spins < (1u << 20); ++spins) {
    __builtin_amdgcn_s_sleep(1);
    sv = ld16c_asm(rs_ws, (unsigned)r * 16u, sv_off); wait_vm_tie<0>(sv);
  }
  const float mean = __uint_as_float(sv[0]), rstd = __uint_as_float(sv[2]);
  const int sb = rb * 4 + mi;
#pragma unroll
  for (int b = 0; b < NB1; ++b) {
    const int blk = 3 * g + (NJ ? 2 : b), which = blk >> 4, hp = blk & 15;
    const float sc = which == 0 ? rstd * (0.25f * 1.4426950408889634f) : rstd;
    float v[16];
#pragma unroll
    for (int i = 0; i < 4; ++i) { const f32x4 c4 = lds_f4(L_CSW + w * 384 + b * 128 + (8 * i + 4 * h) * 4);
#pragma unroll
      for (int e = 0; e < 4; ++e) v[4 * i + e] = (acc[b][4 * i + e] - mean * c4[e]) * sc; }
    const unsigned vo = which == 2 ? (unsigned)(h * 512 + ((r ^ (4 * h)) * 16)) : (unsigned)(lane * 16);
#pragma unroll
    for (int hd = 0; hd < 2; ++hd) {
      const u32x4 o = {pk(v[8 * hd], v[8 * hd + 1]), pk(v[8 * hd + 2], v[8 * hd + 3]), pk(v[8 * hd + 4], v[8 * hd + 5]), pk(v[8 * hd + 6], v[8 * hd + 7])};
      st16c(o, rs_ws, vo, WS_QKV + (unsigned)((which * 32 + 2 * hp + hd) * 64 + sb) * 1024u);
    }
  }
  STAMP(4); CSTAMP(18);
#pragma unroll
  for (int b = 0; b < 3; ++b) { cs[b] = f32x4{0.f, 0.f, 0.f, 0.f};
#pragma unroll
    for (int i = 0; i < 16; ++i) acc[b][i] = 0.f; }
  bool act;
  const unsigned qv = qkv_flag_voff(rb2, hp2, lane, act);
  u32x4 fq;
  SB0();
  static_for<32, 64>([&](auto tc) {
    constexpr int Tg = decltype(tc)::value, t = Tg - 32, s = Tg >> 2, p = t & 3;
    if constexpr (p == 3) {
      if (s == 15) cs_put(3, NJ * 3);
      if constexpr (s == 9) wait_vm<(SC.before[Tg] - SC.before[32] < SC.wait_stage(Tg, 10) ? SC.before[Tg] - SC.before[32] : SC.wait_stage(Tg, 10))>();
      else if constexpr (s == 14) { wait_vm<0>(); if (w == 0) { tie(fq); word_set(0, flags_ok(fq, act, tg) ? 1u : 0u); } }
      else if constexpr (s <= 13) wait_vm<SC.wait_stage(Tg, s + 1)>();
      if (s >= 14) bar(); else bar_raw();
      STAMP2(24 + s);
      if (s == 8) CSTAMP(56); if (s == 11) CSTAMP(57); if (s == 13) CSTAMP(58); if (s == 15) CSTAMP(59);
      if (s == 14) early = word_get(0) != 0u;
      if (s == 15) cs_get(3, NJ * 3);
    }
    if (NJ) op_at(tc, IC<0>{});
    static_for<0, 3>([&](auto bc) {
      constexpr int b = decltype(bc)::value;
      acc[b] = mfma(wg[t & 1][b], xf[t], acc[b]);
      if (t < 31) ld1b(t + 1, b);
      if (p == ((b + 2 * NJ) & 3) || (s == 15 && p == 2 && ((b + 2 * NJ) & 3) == 3)) { if (!(s == 15 && p == 3)) cs[b] = mfma16(sel, wg[t & 1][b], cs[b]); }
      SB0();
    });
    if (!NJ) op_at(tc, IC<0>{});
    op_at(tc, IC<1>{});
    if (Tg == 39) flag_store(rs_ws, WS_FQKV + (unsigned)((rb * 16 + g) * 64 + w * 8), tg, lane);
    if (Tg == 55 && w == 0) fq = ld16c_asm(rs_ws, qv, 0u);
    SB0();
  });
  STAMP(5); CSTAMP(19);
  const float r2 = rstd * rstd;
#pragma unroll
  for (int b = 0; b < 3; ++b) {
    float v[16];
#pragma unroll
    for (int i = 0; i < 4; ++i) { const f32x4 c4 = lds_f4(L_CSW + w * 384 + b * 128 + (8 * i + 4 * h) * 4);
#pragma unroll
      for (int e = 0; e < 4; ++e) v[4 * i + e] = acc[b][4 * i + e] - mean * c4[e]; }
    float hv[8];
#pragma unroll
    for (int i = 0; i < 8; ++i) hv[i] = fmaxf(v[i], 0.f) * v[8 + i] * r2;
    const u32x4 o = {pk(hv[0], hv[1]), pk(hv[2], hv[3]), pk(hv[4], hv[5]), pk(hv[6], hv[7])};
    if (NJ * 3 + b < nu) st16c(o, rs_ws, (unsigned)lane * 16u, WS_H + (unsigned)(sb * NU + u0 + NJ * 3 + b) * 1024u);
  }
  STAMP(6);
  if (NJ == 0 || nu == 6) wait_vm<3>(); else wait_vm<2>();
  return early;
}

template <bool FULL, class F1, class FM, class F2>
DI u32x4 attn_task(int j0, int qb, int hd, int lane, F1 side1, FM mid, F2 side2) {
  const int q = lane & 31, hh = lane >> 5, tq = q - 4 * hh;
  const bf16x8 qf = lds_b128(LA_Q + (unsigned)(hd * 4 + qb) * 1024u + lane * 16u);
  const unsigned kbase = LA_K + (unsigned)(hd * 12 + qb) * 1024u + lane * 16u;
  const float NEG = -__builtin_inff();
  bf16x8 kf[9];
#pragma unroll
  for (int j = 0; j < 9; ++j) if (FULL || j >= j0) kf[j] = lds_b128(kbase + j * 1024u);
  f32x16 c;
#pragma unroll
  for (int i = 0; i < 16; ++i) c[i] = 0.f;
  auto on = [&](int j) { return FULL || j >= j0; };
  float m0 = NEG, m1 = NEG;
  f32x16 s[2];
  {
    const f32x16 s8 = mfma(kf[8], qf, c);
    f32x16 s0;
    if (FULL) s0 = mfma(kf[0], qf, c);
    if (on(1)) s[1] = mfma(kf[1], qf, c);
    SB0();
#pragma unroll
    for (int i = 0; i < 16; i += 2) {
      const int c0 = (i & 3) + 8 * (i >> 2);
      const float a = c0 <= tq ? s8[i] : (FULL ? s0[i] : NEG), b = c0 + 1 <= tq ? s8[i + 1] : (FULL ? s0[i + 1] : NEG);
      if (i & 2) m1 = fmaxf(fmaxf(m1, a), b); else m0 = fmaxf(fmaxf(m0, a), b);
    }
    side1(0);
    SB0();
  }
#pragma unroll
  for (int j = 1; j < 8; ++j) {
    if (j < 7 && on(j + 1)) s[(j + 1) & 1] = mfma(kf[j + 1], qf, c);
    SB0();
    if (on(j)) {
#pragma unroll
      for (int i = 0; i < 8; i += 2) { m0 = fmaxf(fmaxf(m0, s[j & 1][i]), s[j & 1][i + 1]); m1 = fmaxf(fmaxf(m1, s[j & 1][8 + i]), s[j & 1][9 + i]); }
    }
    side1(j);
    SB0();
  }
  const float m = xhalf_max(fmaxf(m0, m1));
  mid();
#pragma unroll
  for (int i = 0; i < 16; ++i) c[i] = -m;
  const int grp = (lane >> 4) & 1, q4 = (lane & 15) >> 2, p4 = lane & 3;
  const unsigned vb = grp ? L_ONES + 128u : LA_V + (unsigned)(hd * 12 + qb) * 1024u + (unsigned)((p4 & 1) * 512 + (((4 * hh + q4) ^ (4 * (p4 & 1))) * 16) + (p4 >> 1) * 8);
  bf16x8 vf[2][2];
  auto ldv = [&](int j, int slot) {
#pragma unroll
    for (int u = 0; u < 2; ++u) vf[slot][u] = cat(lds_tr(vb + j * 1024u + u * 256u), lds_tr(vb + j * 1024u + u * 256u + 128u));
  };
  f32x16 acc;
#pragma unroll
  for (int i = 0; i < 16; ++i) acc[i] = 0.f;
  auto pv = [&](int slot, const unsigned (&pp)[8]) {
#pragma unroll
    for (int u = 0; u < 2; ++u) acc = mfma(vf[slot][u], __builtin_bit_cast(bf16x8, u32x4{pp[4 * u], pp[4 * u + 1], pp[4 * u + 2], pp[4 * u + 3]}), acc);
  };
  f32x16 s8, s0;
  if (on(1)) { s[1] = mfma(kf[1], qf, c); ldv(1, 1); }
#pragma unroll
  for (int j = 1; j < 8; ++j) {
    if (j < 7) { if (on(j + 1)) { s[(j + 1) & 1] = mfma(kf[j + 1], qf, c); ldv(j + 1, (j + 1) & 1); } }
    else { s8 = mfma(kf[8], qf, c); if (FULL) s0 = mfma(kf[0], qf, c); ldv(8, 0); }
    SB0();
    if (on(j)) {
      unsigned pp[8];
#pragma unroll
      for (int i = 0; i < 8; ++i) pp[i] = pk(__builtin_amdgcn_exp2f(s[j & 1][2 * i]), __builtin_amdgcn_exp2f(s[j & 1][2 * i + 1]));
      pv(j & 1, pp);
    }
    side2(j);
    SB0();
  }
  {
    if (FULL) ldv(0, 1);
    unsigned p8[8], p0[8];
#pragma unroll
    for (int i = 0; i < 8; ++i) {
      const int c0 = ((2 * i) & 3) + 8 * (i >> 1);
      const bool va = c0 <= tq, vb_ = c0 + 1 <= tq;
      const float a = va ? s8[2 * i] : (FULL ? s0[2 * i] : NEG), b = vb_ ? s8[2 * i + 1] : (FULL ? s0[2 * i + 1] : NEG);
      const unsigned pp = pk(__builtin_amdgcn_exp2f(a), __builtin_amdgcn_exp2f(b));
      const unsigned mk = (va ? 0xffffu : 0u) | (vb_ ? 0xffff0000u : 0u);
      p8[i] = pp & mk; p0[i] = pp & ~mk;
    }
    pv(0, p8);
    if (FULL) pv(1, p0);
  }
  const float inv = __builtin_amdgcn_rcpf(acc[8]);
  return u32x4{pk(acc[0] * inv, acc[1] * inv), pk(acc[2] * inv, acc[3] * inv), pk(acc[4] * inv, acc[5] * inv), pk(acc[6] * inv, acc[7] * inv)};
}

struct Sched3 {
  static constexpr int a_stage(int st) { return st <= 6 ? st + 4 : (st >= 8 && st <= 11 ? st + 3 : -1); }
  static constexpr int w_stage(int st) { return st <= 10 ? st + 4 : -1; }
  int before[16] = {}, lastA[15] = {}, lastW[15] = {};
  constexpr Sched3() {
    int c = 9;
    for (int i = 0; i < 4; ++i) { lastA[i] = 2 * i + 2; lastW[i] = 0; }
    for (int st = 0; st < 15; ++st) {
      before[st] = c;
      if (st == 3) c += 1;
      if (a_stage(st) >= 0) { c += 2; lastA[a_stage(st)] = c; }
      if (w_stage(st) >= 0) { c += 2; lastW[w_stage(st)] = c; }
      if (st == 8) c += 2;
    }
    before[15] = c;
  }
  constexpr int wait(int st) const { return before[st] - (lastA[st + 1] > lastW[st + 1] ? lastA[st + 1] : lastW[st + 1]); }
};

DI void phase23(const Ptrs& P, bool early, int rb1, int g1, u64 epoch, int rb, int hp, int rb64, int cb, int rot, int w, int lane, Tag tg, Stamps& T) {
  const rsrc_t rs_ws = mk_rsrc(P.ws, WS_BYTES), rs_d = mk_rsrc(P.wd, FF * DM * 2), rs_o = mk_rsrc(P.wo, DM * DM * 2);
  for (unsigned i = threadIdx.x; i < ONES_BYTES / 16; i += 512) *(LAS u32x4*)(lds + L_ONES + i * 16) = u32x4{ONES2, ONES2, ONES2, ONES2};
  NOTE(63, early ? 1 : 2);
  if (!early) {
    bool act; const unsigned qv = qkv_flag_voff(rb, hp, lane, act);
    if (w == 0) flags_spin(rs_ws, qv, act, tg);
    bar();
    const AttnSrc asrc = attn_src(rb, hp, w);
#pragma unroll
    for (int i = 0; i < 7; ++i) attn_issue(rs_ws, asrc, w, lane, i, true);
    wait_vm<0>();
  }
  bar();
  STAMP(9);
  unsigned vw[2];
#pragma unroll
  for (int j = 0; j < 2; ++j) { const int G = (2 * w + j) * 64 + lane, row = G >> 3;
    vw[j] = (unsigned)row * 1024u + (unsigned)(((G & 7) ^ (((row >> 1) & 1) << 2)) * 16); }
  const int sbl = w >> 2, sbg = 2 * rb64 + sbl;
  auto chunk = [&](int st) { return st < 10 ? (st + rot >= 10 ? st + rot - 10 : st + rot) : (st == 10 ? 10 : ((st - 11 + rot) & 3)); };
  auto pieceA = [&](int st, int j, bool go) {
    const int c = chunk(st), ks = 2 * (w & 3) + j;
    const unsigned src = st < 11 ? WS_H + (unsigned)(sbg * NU + c * 8 + ks) * 1024u : WS_O + (unsigned)(sbg * 32 + c * 8 + ks) * 1024u;
    dma16c(l3a(st & 3) + (unsigned)(sbl * 8 + ks) * 1024u, (unsigned)lane * 16u + (go ? 0u : 0x40000000u), rs_ws, src);
  };
  auto pieceW = [&](int st, int j) {
    dma16(l3w(st & 3) + (unsigned)(2 * w + j) * 1024u, vw[j], st < 11 ? rs_d : rs_o, (unsigned)cb * 128u + (unsigned)chunk(st) * 131072u);
  };
  auto side = [&](int j) {
    if (j & 1) pieceW(j >> 2, (j >> 1) & 1);
    if (j == 2) { wait_vm<1>(); flag_store(rs_ws, WS_FH + (unsigned)((rb1 * 16 + g1) * 64 + w * 8), tg, lane); }
  };
  const int qb = w & 3, hd = w >> 2, sb = 4 * rb + qb;
  const unsigned hv = WS_FH + (unsigned)((rb64 >> 1) * 1024 + lane * 16), ov = WS_FO + (unsigned)((rb64 >> 1) * 1024 + lane * 16);
  u32x4 o, fh;
  auto side2 = [&](int j) {
    if (j & 1) pieceW(2 + (j >> 2), (j >> 1) & 1);
    if (j == 7) fh = ld16c_asm(rs_ws, hv, 0u);
  };
  auto mid = [&]() { STAMP(10); };
  if (sb >= 8) o = attn_task<true>(0, qb, hd, lane, side, mid, side2); else o = attn_task<false>(8 - sb, qb, hd, lane, side, mid, side2);
  STAMP(11);
  wait_vm_tie<0>(fh);
  NOTE(55, flags_ok(fh, true, tg) ? 1 : 2);
  if (!flags_ok(fh, true, tg)) flags_spin(rs_ws, hv, true, tg);
  pieceA(0, 0, true); pieceA(0, 1, true);
  bar();
  STAMP(13);
#pragma unroll
  for (int s = 1; s < 4; ++s) { pieceA(s, 0, true); pieceA(s, 1, true); }
  st16c(o, rs_ws, (unsigned)lane * 16u, WS_O + (unsigned)(sb * 32 + 2 * hp + hd) * 1024u);
  const int mb = w & 1, nb2 = (w >> 1) & 1, kh = w >> 2;
  const int h = lane >> 5, nsub = (lane >> 4) & 1, q4 = (lane & 15) >> 2, p4 = lane & 3;
  const unsigned aa = (unsigned)(mb * 8 + kh * 4) * 1024u + lane * 16u;
  const unsigned wa = (unsigned)(16 * kh + h) * 512u + q4 * 128u + (unsigned)((nb2 ^ (q4 >> 1)) * 64) + nsub * 32u + p4 * 8u;
  const int row = 64 * rb64 + 32 * mb + (lane & 31);
  f32x16 acc;
#pragma unroll
  for (int i = 0; i < 16; ++i) acc[i] = 0.f;
  bf16x8 af[2][4], wfr[2][4];
  auto ld3 = [&](int st, int ks) {
    af[st & 1][ks] = lds_b128(l3a(st & 3) + aa + ks * 1024u); const unsigned a = l3w(st & 3) + wa + ks * 2048u; wfr[st & 1][ks] = cat(lds_tr(a), lds_tr(a + 1024u));
  };
  const int col0 = 64 * cb + 32 * nb2 + 16 * kh + 4 * h;
  wait_vm<7>();
  bar();
  STAMP(14);
#pragma unroll
  for (int ks = 0; ks < 4; ++ks) ld3(0, ks);
  u32x4 fo, fe[4];
  const unsigned short* xr = (const unsigned short*)P.x + (size_t)row * DM + col0;
  u32x2 x0, x1;
  const bool keeper = blockIdx.x == 0 && w == 0;
  static constexpr Sched3 S3{};
  static_for<0, 15>([&](auto sc) {
    constexpr int st = decltype(sc)::value;
    if constexpr (st < 14) {
      wait_vm<S3.wait(st)>();
      if (st == 8 && w == 0) { wait_vm_tie<2>(fo); NOTE(54, flags_ok(fo, true, tg) ? 1 : 2); if (!flags_ok(fo, true, tg)) flags_spin(rs_ws, ov, true, tg); }
      bar();
      STAMP2(40 + st);
    }
    if (st == 3) flag_store(rs_ws, WS_FO + (unsigned)((rb * 16 + hp) * 64 + w * 8), tg, lane);
    SB0();
#pragma unroll
    for (int ks = 0; ks < 4; ++ks) {
      if (ks < 2 || st != 10 || kh == 0) acc = mfma(wfr[st & 1][ks], af[st & 1][ks], acc);
      if (st < 14) ld3(st + 1, ks);
      SB0();
      if (ks < 2) { if (Sched3::a_stage(st) >= 0) pieceA(Sched3::a_stage(st), ks, true); }
      else if (Sched3::w_stage(st) >= 0) pieceW(Sched3::w_stage(st), ks - 2);
      SB0();
    }
    if (st == 6 && w == 0) fo = ld16c_asm(rs_ws, ov, 0u);
    if (st == 8) { x0 = *(const GAS u32x2*)xr; x1 = *(const GAS u32x2*)(xr + 8); }
    if (st == 9 && keeper) {
#pragma unroll
      for (int i = 0; i < 4; ++i) fe[i] = ld16c_asm(rs_ws, WS_FQKV + (unsigned)(i * 64 + lane) * 64u, 0u);
    }
    SB0();
  });
  STAMP(15);
  bar();
  {
    const unsigned base = L3_RED + (unsigned)((w & 3) * 2 + kh) * 2048u + lane * 16u;
    if (kh) { *(LAS f32x4*)(lds + base) = f32x4{acc[0], acc[1], acc[2], acc[3]}; *(LAS f32x4*)(lds + base + 1024u) = f32x4{acc[4], acc[5], acc[6], acc[7]}; }
    else { *(LAS f32x4*)(lds + base) = f32x4{acc[8], acc[9], acc[10], acc[11]}; *(LAS f32x4*)(lds + base + 1024u) = f32x4{acc[12], acc[13], acc[14], acc[15]}; }
  }
  bar();
  {
    const unsigned base = L3_RED + (unsigned)((w & 3) * 2 + (kh ^ 1)) * 2048u + lane * 16u;
    const f32x4 a0 = lds_f4(base), a1 = lds_f4(base + 1024u);
    unsigned short* orow = (unsigned short*)P.out + (size_t)row * DM + col0;
    float v[8];
#pragma unroll
    for (int e = 0; e < 4; ++e) { v[e] = (kh ? acc[8 + e] : acc[e]) + a0[e]; v[4 + e] = (kh ? acc[12 + e] : acc[4 + e]) + a1[e]; }
    v[0] += bf_lo(x0[0]); v[1] += bf_hi(x0[0]); v[2] += bf_lo(x0[1]); v[3] += bf_hi(x0[1]);
    v[4] += bf_lo(x1[0]); v[5] += bf_hi(x1[0]); v[6] += bf_lo(x1[1]); v[7] += bf_hi(x1[1]);
    *(GAS u32x2*)orow = u32x2{pk(v[0], v[1]), pk(v[2], v[3])};
    *(GAS u32x2*)(orow + 8) = u32x2{pk(v[4], v[5]), pk(v[6], v[7])};
  }
  if (keeper) {
#pragma unroll
    for (int i = 0; i < 4; ++i) { tie(fe[i]); if (!flags_ok(fe[i], true, tg)) flags_spin(rs_ws, WS_FQKV + (unsigned)(i * 64 + lane) * 64u, true, tg); }
    if (lane == 0) __hip_atomic_store((GAS u64*)(P.ws + WS_EPOCH), epoch, __ATOMIC_RELAXED, __HIP_MEMORY_SCOPE_AGENT);
  }
}

__global__ __launch_bounds__(512, 2) void mk_kernel(const void* x, const void* wqkv, const void* wo, const void* wg, const void* wu, const void* wd, void* out, char* ws) {
  u64 pc0 = 0;
  asm volatile("mk_code_begin:\n\ts_getpc_b64 %0" : "=s"(pc0));
  {
    unsigned len;
    asm volatile("s_mov_b32 %0, mk_code_end-mk_code_begin" : "=s"(len));
    const unsigned wv = __builtin_amdgcn_readfirstlane(threadIdx.x >> 6);
    const unsigned per = ((len >> 5) + 1023u) & ~1023u;
    const unsigned o = (blockIdx.x >> 3) * per + wv * 1024u;
    dma16(L_DUMMY + wv * 1024u, (threadIdx.x & 63u) * 16u, mk_rsrc((const void*)(pc0 & ~255ull), len & ~15u), wv * 1024u < per ? o : 0x40000000u);
  }
  const Ptrs P{x, wqkv, wo, wg, wu, wd, out, ws, pc0};
  Stamps T;
  STAMP(0); CSTAMP(20);
  __builtin_amdgcn_s_setprio(1);
  const int b = blockIdx.x, xcd = b & 7, idx = b >> 3;
  const int w = __builtin_amdgcn_readfirstlane(threadIdx.x >> 6), lane = threadIdx.x & 63;
  Tag tg; u64 epoch;
  const int rb = 2 * (idx >> 2) + (xcd >> 2), g = (xcd & 3) * 4 + (idx & 3), rot1 = ((idx >> 2) + 2 * (idx & 3)) & 7;
  bool early;
  if (w < 4) early = phase1<0>(P, rb, g, rot1, w, lane, rb, g, tg, epoch, T); else early = phase1<1>(P, rb, g, rot1, w - 4, lane, rb, g, tg, epoch, T);
  const int rot3 = ((idx & 7) + 3 * (idx >> 3)) % 10;
  phase23(P, early, rb, g, epoch, rb, g, xcd * 4 + (idx >> 3), idx & 7, rot3, w, lane, tg, T);
  asm volatile("mk_code_end:");
}

}

extern "C" void kernel_launch(void* const* d_in, const int* in_sizes, int n_in, void* d_out, int out_size, void* d_ws, size_t ws_size, hipStream_t stream) {
  const void *x = d_in[0], *wqkv = d_in[1], *wo = d_in[2], *wg = d_in[4], *wu = d_in[5], *wd = d_in[6];
  mk_kernel<<<256, 512, 0, stream>>>(x, wqkv, wo, wg, wu, wd, d_out, (char*)d_ws);
}
```

```cpp
#include <hip/hip_runtime.h>
#include <hip/hip_bf16.h>
#include <stdint.h>

namespace {

typedef short bf16x8 __attribute__((ext_vector_type(8)));
typedef short s16x4 __attribute__((ext_vector_type(4)));
typedef float f32x16 __attribute__((ext_vector_type(16)));
typedef float f32x4 __attribute__((ext_vector_type(4)));
typedef float f32x2 __attribute__((ext_vector_type(2)));
typedef unsigned u32x4 __attribute__((ext_vector_type(4)));
typedef unsigned u32x2 __attribute__((ext_vector_type(2)));
typedef __bf16 bf16x2_t __attribute__((ext_vector_type(2)));
typedef short v4i16_t __attribute__((ext_vector_type(4)));
typedef __amdgpu_buffer_rsrc_t rsrc_t;
#define LAS __attribute__((address_space(3)))
#define GAS __attribute__((address_space(1)))
#define DI __device__ __forceinline__
#define SB0() __builtin_amdgcn_sched_barrier(0)
typedef unsigned long long u64;

constexpr int SEQ = 2048, DM = 512, FF = 1376, NU = 86;
constexpr unsigned WS_EPOCH = 0;
constexpr unsigned WS_FQKV = 16384, WS_FH = 32768, WS_FO = 49152;
constexpr unsigned WS_RS = 65536;
constexpr unsigned WS_QKV = 1u << 20;
constexpr unsigned WS_H = 7u << 20;
constexpr unsigned WS_O = 13u << 20;
constexpr unsigned WS_BYTES = 16u << 20;
constexpr unsigned SLOT1 = 28672;
constexpr unsigned L_CSP = 143360;
constexpr unsigned L_CSW = 146432;
constexpr unsigned L_ONES = 152576;
constexpr unsigned ONES_BYTES = 9984;
constexpr unsigned L_WORD = 163584;
constexpr unsigned LDS_BYTES = 163840;
constexpr unsigned LA_Q = 28672, LA_K = LA_Q + 8192, LA_V = LA_K + 24576;

__shared__ __attribute__((aligned(1024))) char lds[LDS_BYTES];

DI unsigned pk(float a, float b) { f32x2 v = {a, b}; bf16x2_t r = __builtin_convertvector(v, bf16x2_t); return __builtin_bit_cast(unsigned, r); }
DI float dot2(unsigned a, unsigned b, float c) { return __builtin_amdgcn_fdot2_f32_bf16(__builtin_bit_cast(bf16x2_t, a), __builtin_bit_cast(bf16x2_t, b), c, false); }
DI rsrc_t mk_rsrc(const void* p, unsigned bytes) { return __builtin_amdgcn_make_buffer_rsrc((void*)p, 0, bytes, 0x00020000); }
DI void dma16(unsigned ldsaddr, unsigned voff, rsrc_t rs, unsigned soff) {
  asm volatile("s_mov_b32 m0, %0\n\ts_nop 0\n\tbuffer_load_dwordx4 %1, %2, %3 offen lds" ::"s"(ldsaddr), "v"(voff), "s"(rs), "s"(soff) : "memory");
}
DI void dma16c(unsigned ldsaddr, unsigned voff, rsrc_t rs, unsigned soff) {
  asm volatile("s_mov_b32 m0, %0\n\ts_nop 0\n\tbuffer_load_dwordx4 %1, %2, %3 offen sc1 lds" ::"s"(ldsaddr), "v"(voff), "s"(rs), "s"(soff) : "memory");
}
DI void pf_lines(unsigned ldsaddr, unsigned voff, rsrc_t rs) {
  asm volatile("s_mov_b32 m0, %0\n\ts_nop 0\n\tbuffer_load_dword %1, %2, 0 offen lds" ::"s"(ldsaddr), "v"(voff), "s"(rs) : "memory");
}
template <int N> DI void wait_vm() { asm volatile("s_waitcnt vmcnt(%0)" ::"n"(N) : "memory"); }
template <int N> DI void wait_vm_tie(u32x4& v) { asm volatile("s_waitcnt vmcnt(%1)" : "+v"(v) : "n"(N) : "memory"); }
template <int V> struct IC { static constexpr int value = V; };
template <int I, int N, class F> DI void static_for(F&& f) { if constexpr (I < N) { f(IC<I>{}); static_for<I + 1, N>(f); } }
DI void bar_raw() { asm volatile("s_barrier" ::: "memory"); }
DI void tie(u32x4& v) { asm volatile("" : "+v"(v) :: "memory"); }
DI void bar() { asm volatile("s_waitcnt lgkmcnt(0)\n\ts_barrier" ::: "memory"); }
DI f32x16 mfma(bf16x8 a, bf16x8 b, f32x16 c) { return __builtin_amdgcn_mfma_f32_32x32x16_bf16(a, b, c, 0, 0, 0); }
DI bf16x8 lds_b128(unsigned a) { return *(const LAS bf16x8*)(lds + a); }
#define MK_REAL 1
DI s16x4 lds_tr(unsigned a) { return __builtin_amdgcn_ds_read_tr16_b64_v4i16((LAS v4i16_t*)(lds + a)); }
DI f32x4 mfma16(bf16x8 a, bf16x8 b, f32x4 c) { return __builtin_amdgcn_mfma_f32_16x16x32_bf16(a, b, c, 0, 0, 0); }
DI f32x4 lds_f4(unsigned a) { return *(const LAS f32x4*)(lds + a); }
DI bf16x8 cat(s16x4 lo, s16x4 hi) { return __builtin_shufflevector(lo, hi, 0, 1, 2, 3, 4, 5, 6, 7); }
DI float xhalf_sum(float v) { auto r = __builtin_amdgcn_permlane32_swap(__float_as_uint(v), __float_as_uint(v), false, false); return __uint_as_float(r[0]) + __uint_as_float(r[1]); }
DI float xhalf_max(float v) { auto r = __builtin_amdgcn_permlane32_swap(__float_as_uint(v), __float_as_uint(v), false, false); return fmaxf(__uint_as_float(r[0]), __uint_as_float(r[1])); }
DI void st16c(u32x4 v, rsrc_t rs, unsigned voff, unsigned soff) { __builtin_amdgcn_raw_buffer_store_b128(v, rs, voff + soff, 0, 16); }
DI void st8c(u32x2 v, rsrc_t rs, unsigned voff, unsigned soff) { __builtin_amdgcn_raw_buffer_store_b64(v, rs, voff, soff, 16); }
constexpr unsigned ONES2 = 0x3F803F80u;
template <int CTRL> DI float dpp_add(float v) { return v + __int_as_float(__builtin_amdgcn_update_dpp(0, __float_as_int(v), CTRL, 0xf, 0xf, true)); }
DI float wave_sum(float v) {
  v = dpp_add<0xB1>(v); v = dpp_add<0x4E>(v); v = dpp_add<0x141>(v); v = dpp_add<0x140>(v);
  auto a = __builtin_amdgcn_permlane16_swap(__float_as_uint(v), __float_as_uint(v), false, false);
  v = __uint_as_float(a[0]) + __uint_as_float(a[1]);
  return xhalf_sum(v);
}
DI float bf_lo(unsigned u) { return __uint_as_float(u << 16); }
DI float bf_hi(unsigned u) { return __uint_as_float(u & 0xffff0000u); }
DI u32x4 ld16_asm(rsrc_t rs, unsigned voff, unsigned soff) { u32x4 v; asm volatile("buffer_load_dwordx4 %0, %1, %2, %3 offen" : "=v"(v) : "v"(voff), "s"(rs), "s"(soff) : "memory"); return v; }
DI u32x4 ld16c_asm(rsrc_t rs, unsigned voff, unsigned soff) { u32x4 v; asm volatile("buffer_load_dwordx4 %0, %1, %2, %3 offen sc1" : "=v"(v) : "v"(voff), "s"(rs), "s"(soff) : "memory"); return v; }
#define STAMP2(i) do {} while (0)
struct Stamps {};
#define STAMP(i) do {} while (0)
#define CSTAMP(i) do {} while (0)
#define NOTE(i, v) do {} while (0)

struct Ptrs { const void *x, *wqkv, *wo, *wg, *wu, *wd; void* out; char* ws; u64 pc; };
struct Tag { unsigned a, b; };

DI void flag_store(rsrc_t rs_ws, unsigned off, Tag tg, int lane) { if (lane == 0) st8c(u32x2{tg.a, tg.b}, rs_ws, 0u, off); }
DI bool flags_ok(u32x4 v, bool act, Tag tg) { return __all(!act || (v[0] == tg.a && v[1] == tg.b && v[2] == tg.a && v[3] == tg.b)); }
DI void flags_spin(rsrc_t rs_ws, unsigned voff, bool act, Tag tg) {
  for (unsigned spins = 0; spins < (1u << 20); ++spins) {
    u32x4 v = ld16c_asm(rs_ws, voff, 0u); wait_vm_tie<0>(v);
    if (flags_ok(v, act, tg)) break;
    __builtin_amdgcn_s_sleep(1);
  }
}
DI void word_set(int i, unsigned v) { *(volatile LAS unsigned*)(lds + L_WORD + i * 4) = v; }
DI unsigned word_get(int i) { return __builtin_amdgcn_readfirstlane(*(volatile LAS unsigned*)(lds + L_WORD + i * 4)); }

struct AttnSrc { unsigned q, kv; int sb0; };
DI AttnSrc attn_src(int rb, int hp, int w) {
  AttnSrc a;
  a.q = __builtin_amdgcn_readfirstlane(WS_QKV + (unsigned)((2 * hp + (w >> 2)) * 64 + 4 * rb + (w & 3)) * 1024u);
  a.kv = __builtin_amdgcn_readfirstlane(WS_QKV + (unsigned)(((1 + (w >> 2)) * 32 + 2 * hp + ((w >> 1) & 1)) * 64) * 1024u);
  a.sb0 = __builtin_amdgcn_readfirstlane(4 * rb - 8 + 6 * (w & 1));
  return a;
}
DI void attn_issue(rsrc_t rs_ws, const AttnSrc& a, int w, int lane, int piece, bool go) {
  const unsigned vo = (unsigned)lane * 16u + (go ? 0u : 0x40000000u);
  if (piece == 0) { dma16c(LA_Q + (unsigned)w * 1024u, vo, rs_ws, a.q); return; }
  const int sbk = a.sb0 + piece - 1;
  dma16c(LA_K + (unsigned)((w >> 1) * 12 + 6 * (w & 1) + piece - 1) * 1024u, vo, rs_ws, a.kv + (unsigned)(sbk < 0 ? 0 : sbk) * 1024u);
}
DI unsigned qkv_flag_voff(int rb, int hp, int lane, bool& act) {
  const int c = lane >> 2, kind = c == 0 ? 0 : (c <= 3 ? 1 : 2), d = c == 0 ? 0 : (c - 1) % 3, rr = rb - d;
  act = lane < 28 && rr >= 0;
  return act ? WS_FQKV + (unsigned)((rr * 16 + (kind * 16 + hp) / 3) * 64 + (lane & 3) * 16) : 0u;
}

constexpr unsigned L_DUMMY = 152576;
#define TLBW_N 0
#define PFX_N 0
#define P1_HOLD 12
constexpr int OP_NONE = -1, OP_PFW = 200, OP_ATT = 400;
template <int NJ> struct Sched1 {
  static constexpr int NA = NJ ? 4 : 3, NST = NJ ? 2 : 4;
  static constexpr int n(int sg) { return sg < 8 ? NA : 3; }
  static constexpr int TB(int s) { return s < 8 ? 4 * s + 2 : 4 * s + 3; }
  static constexpr int extras(int T) { return (T == 5 ? 1 : 0)   + (T == 22 ? 1 : 0)   + (T == 39 ? 1 : 0)  ; }
  static constexpr int NPRO = 4;
  static constexpr int NTW = TLBW_N;
  static constexpr int NPX = PFX_N;
  static constexpr int PRO = 1 + NPX + NPRO * NA + 1 + NTW;
  static constexpr int HOLD = P1_HOLD;
  int op[64][2] = {}, before[65] = {}, last[16] = {};
  constexpr Sched1() {
    int sg = NPRO, j = 0, pf = 0, att = 0, c = PRO;
    last[0] = 1 + NPX + NA;
    for (int i = 1; i < NPRO; ++i) last[i] = 1 + NPX + NA + 1 + NTW + i * NA;
    for (int T = 0; T < 64; ++T) {
      before[T] = c;
      for (int k = 0; k < 2; ++k) {
        int o = OP_NONE;
        if (k == 0 || T < 14) {
          if (sg <= 15 && (sg < 5 || T >= TB(sg - 4)) && (sg < 8 || T >= 4 * sg - HOLD)) {   o = sg * 8 + j; if (++j == n(sg)) { last[sg] = c + 1; ++sg; j = 0; } }
          else if (pf == 0 && T >= 4) o = OP_PFW + pf++;
        }
        if (T >= 59 && att < 7 && (k == 0 || T >= 61)) o = OP_ATT + att++;
        op[T][k] = o;
        if (o != OP_NONE) ++c;
      }
      c += extras(T);
      if (T == 31) c += NST;
    }
    before[64] = c;
  }
  constexpr int wait_stage(int T, int sg) const { return before[T] - last[sg]; }
};

template <int NJ>
DI bool phase1(const Ptrs& P, int rb, int g, int rot, int mi, int lane, int rb2, int hp2, Tag& tg, u64& epoch, Stamps& T_) {
  Stamps& T = T_;
  typedef Sched1<NJ> L;
  static constexpr Sched1<NJ> SC{};
  const int w = NJ * 4 + mi;
  u64 e0 = 0;
  if (w == 0) asm volatile("s_load_dwordx2 %0, %1, 0x0" : "=s"(e0) : "s"(P.ws) : "memory");
  const rsrc_t rs_x = mk_rsrc(P.x, SEQ * DM * 2), rs_q = mk_rsrc(P.wqkv, DM * 1536 * 2);
  const rsrc_t rs_f = mk_rsrc(NJ ? P.wu : P.wg, DM * FF * 2), rs_ws = mk_rsrc(P.ws, WS_BYTES);
  const unsigned sx0 = (unsigned)rb * 131072u, sq0 = (unsigned)g * 192u;
  constexpr int NQ = NJ ? 2 : 1;
  constexpr int NA = 2 + NQ;
  unsigned vx[2], vq[2], vf[3];
#pragma unroll
  for (int j = 0; j < 2; ++j) vx[j] = (unsigned)(16 * w + 8 * j + (lane >> 3)) * 1024u + (unsigned)(((lane & 7) ^ (4 * j + (lane >> 4))) * 16);
#pragma unroll
  for (int j = 0; j < NQ; ++j) { const int ins = NJ ? 2 * mi + j : 8 + mi; const int G = ins * 64 + lane;
    vq[j] = (unsigned)(G / 12) * 3072u + (unsigned)(G % 12) * 16u; }
  const int u0 = (g * NU) >> 4, nu = (((g + 1) * NU) >> 4) - u0;
  const unsigned sf0 = (unsigned)u0 * 32u;
  auto piece = [&](int gs, int j) {
    const unsigned slot = (unsigned)(gs % 5) * SLOT1;
    const unsigned c = (unsigned)(gs + rot) & 7u;
    if (gs < 8) {
      if (j < 2) dma16(slot + (unsigned)(2 * w + j) * 1024u, vx[j], rs_x, sx0 + c * 128u);
      else dma16(slot + 16384u + (unsigned)(NJ ? 2 * mi + j - 2 : 8 + mi) * 1024u, vq[j - 2], rs_q, sq0 + c * (64u * 3072u));
    } else dma16(slot + NJ * 12288u + (unsigned)(3 * mi + j) * 1024u, vf[j], rs_f, sf0 + c * (64u * 2752u));
  };
#pragma unroll
  for (int j = 0; j < NA; ++j) piece(0, j);
  const unsigned myrow = (unsigned)(128 * rb + 8 * g + w);
  u32x4 xrow = ld16_asm(rs_x, (unsigned)lane * 16u, myrow * 1024u);
#pragma unroll
  for (int sg = 1; sg < L::NPRO; ++sg)
#pragma unroll
    for (int j = 0; j < NA; ++j) piece(sg, j);
  SB0();
  STAMP(1);
#pragma unroll
  for (int j = 0; j < 3; ++j) { const int G = (3 * mi + j) * 64 + lane, kg = G / 48, rr = G % 48, unit = (rr >> 3) ^ NJ;
    vf[j] = unit < nu ? (unsigned)(4 * kg + ((rr & 7) >> 1)) * 2752u + (unsigned)(unit * 32 + (rr & 1) * 16) : 0x40000000u; }
  const int r = lane & 31, h = lane >> 5, nsub = (lane >> 4) & 1, q4 = (lane & 15) >> 2, p4 = lane & 3;
  unsigned xa[4], ro[4];
#pragma unroll
  for (int p = 0; p < 4; ++p) { const int ks = (p + mi) & 3;
    xa[p] = (unsigned)(32 * mi + r) * 128u + (unsigned)(((2 * ks + h) ^ ((r >> 1) & 7)) * 16);
    ro[p] = (unsigned)ks * 3072u; }
  const unsigned wl = (unsigned)(2 * h) * 768u + q4 * 32u + p4 * 8u;
  const unsigned wa = 16384u + (unsigned)(2 * h) * 768u + q4 * 192u + nsub * 32u + p4 * 8u;
  const unsigned wbE = nsub * 12288u + wl + nsub * 128u;
  const unsigned wbO = nsub * 12288u + wl - nsub * 128u;
  const AttnSrc asrc = attn_src(rb2, hp2, w);
  bool early = false;
  auto warm = [&](int i) {
    const unsigned xl = blockIdx.x >> 3, dst = L_DUMMY + (unsigned)w * 256u;
    if (i == 0) {
      const unsigned q = (unsigned)(w & 1) * 64u + (unsigned)lane, rr = q / 7u, ln = q - rr * 7u;
      const unsigned ub = (unsigned)((((blockIdx.x & 3) * 4 * NU) >> 4) * 32) & ~127u;
      pf_lines(dst, (w < 4 && q < 112u) ? (16u * xl + rr) * 2752u + ub + ln * 128u : 0x40000000u, mk_rsrc((w & 2) ? P.wu : P.wg, DM * FF * 2));
    } else if (i == 1) {
      pf_lines(dst, lane < 43 ? ((xl * 8u + (unsigned)w) * 43u + (unsigned)lane) * 128u : 0x40000000u, mk_rsrc(P.wd, FF * DM * 2));
    } else {
      pf_lines(dst, lane < 16 ? ((xl * 8u + (unsigned)w) * 16u + (unsigned)lane) * 128u : 0x40000000u, mk_rsrc(P.wo, DM * DM * 2));
    }
  };
  auto op_at = [&](auto tc, auto kc) {
    constexpr int o = SC.op[decltype(tc)::value][decltype(kc)::value];
    if constexpr (o >= OP_ATT) attn_issue(rs_ws, asrc, w, lane, o - OP_ATT, early);
    else if constexpr (o >= OP_PFW) warm(o - OP_PFW);
    else if constexpr (o >= 0) piece(o >> 3, o & 7);
  };
  bf16x8 sel;
  { const unsigned v = (lane & 15) == ((lane >> 4) & 1) ? ONES2 : 0u; sel = __builtin_bit_cast(bf16x8, u32x4{v, v, v, v}); }
  if (w == 0) {
    asm volatile("s_waitcnt lgkmcnt(0)" : "+s"(e0) :: "memory");
    word_set(2, (unsigned)e0); word_set(3, (unsigned)(e0 >> 32));
  }

  bf16x8 xf[32];
  constexpr int NB1 = NJ ? 1 : 2;
  f32x16 acc[3];
  f32x4 cs[3];
#pragma unroll
  for (int b = 0; b < 3; ++b) { cs[b] = f32x4{0.f, 0.f, 0.f, 0.f};
#pragma unroll
    for (int i = 0; i < 16; ++i) acc[b][i] = 0.f; }
  bf16x8 wf[3][NB1], wg[2][3];
  auto ldx = [&](int t) { xf[t] = lds_b128((unsigned)((t >> 2) % 5) * SLOT1 + xa[t & 3]); };
  auto ld1a = [&](int t, int b) {
    const unsigned a = (unsigned)((t >> 2) % 5) * SLOT1 + wa + ro[t & 3] + (NJ ? 2 : b) * 64u; wf[t % 3][b] = cat(lds_tr(a), lds_tr(a + 768u));
  };
  auto ld1b = [&](int t, int b) {
    const int u = NJ * 3 + b;
    const unsigned a = (unsigned)((8 + (t >> 2)) % 5) * SLOT1 + ((u & 1) ? wbO : wbE) + ro[t & 3] + u * 128u; wg[t & 1][b] = cat(lds_tr(a), lds_tr(a + 768u));
  };
  auto cs_put = [&](int nb, int b0) {
#pragma unroll
    for (int b = 0; b < 3; ++b) if (b < nb && lane < 16) { *(LAS float*)(lds + L_CSP + (((b0 + b) * 32 + lane) * 4 + mi) * 4) = cs[b][0]; *(LAS float*)(lds + L_CSP + (((b0 + b) * 32 + 16 + lane) * 4 + mi) * 4) = cs[b][1]; }
  };
  auto cs_get = [&](int nb, int b0) {
#pragma unroll
    for (int b = 0; b < 3; ++b) if (b < nb) { const f32x4 c4 = lds_f4(L_CSP + ((b0 + b) * 32 + r) * 16);
      *(LAS float*)(lds + L_CSW + w * 384 + b * 128 + r * 4) = (c4[0] + c4[1]) + (c4[2] + c4[3]); }
  };
  wait_vm<(L::NPRO - 1) * NA + L::NTW>();
  bar();
  e0 = (u64)word_get(2) | ((u64)word_get(3) << 32);
  epoch = e0 + 1ull;
  tg.a = (unsigned)epoch; tg.b = ((unsigned)(e0 >> 32) ^ 0x5bd1e995u) + tg.a;
  STAMP(2); CSTAMP(23);
  ldx(0); ldx(1);
#pragma unroll
  for (int b = 0; b < NB1; ++b) { ld1a(0, b); ld1a(1, b); }
  u32x4 sv;
  float rmean = 0.f, rrstd = 0.f;
  const unsigned sv_off = WS_RS + (unsigned)(128 * rb + 32 * mi) * 16u;
  static_for<0, 32>([&](auto tc) {
    constexpr int t = decltype(tc)::value, s = t >> 2, p = t & 3;
    if constexpr (p == 2) {
      if (s == 7) cs_put(NB1, NJ ? 2 : 0);
      wait_vm<SC.wait_stage(t, s + 1)>();
      if (s == 7) bar(); else bar_raw();
      STAMP2(24 + s);
      if (s == 7) cs_get(NB1, NJ ? 2 : 0);
      if (s == 1) CSTAMP(60); if (s == 4) CSTAMP(61); if (s == 7) CSTAMP(62);
    }
    if (NJ) op_at(tc, IC<0>{});
    static_for<0, NB1>([&](auto bc) {
      constexpr int b = decltype(bc)::value;
      acc[b] = mfma(wf[t % 3][b], xf[t], acc[b]);
      if (t < 30) { if (b == 0) ldx(t + 2); ld1a(t + 2, b); }
      if (t == 31) { ld1b(0, b); if (b == NB1 - 1) { for (int bb = NB1; bb < 3; ++bb) ld1b(0, bb); } }
      if (p == (NJ ? 1 : b)) cs[b] = mfma16(sel, wf[t % 3][b], cs[b]);
      if (b == 0) {
        if (t == 2 + NJ) {
          tie(xrow);
          float e[8];
#pragma unroll
          for (int i = 0; i < 4; ++i) { e[2 * i] = bf_lo(xrow[i]); e[2 * i + 1] = bf_hi(xrow[i]); }
          rmean = wave_sum(((e[0] + e[1]) + (e[2] + e[3])) + ((e[4] + e[5]) + (e[6] + e[7]))) * (1.f / 512.f);
          asm volatile("" : "+v"(rmean));
        }
        if (t == 4) {
          float q = 0.f;
#pragma unroll
          for (int i = 0; i < 4; ++i) { const float d0 = bf_lo(xrow[i]) - rmean, d1 = bf_hi(xrow[i]) - rmean; q = fmaf(d0, d0, q); q = fmaf(d1, d1, q); }
          rrstd = __builtin_amdgcn_rsqf(wave_sum(q) * (1.f / 512.f) + 1e-5f);
          asm volatile("" : "+v"(rrstd));
        }
      }
      SB0();
      if (b == 0) op_at(tc, IC<NJ ? 1 : 0>{});
    });
    if (!NJ) op_at(tc, IC<1>{});
    if (t == 22) sv = ld16c_asm(rs_ws, (unsigned)r * 16u, sv_off);
    if (t == 5 && lane == 0) st16c(u32x4{__float_as_uint(rmean), tg.a, __float_as_uint(rrstd), tg.b}, rs_ws, 0u, WS_RS + myrow * 16u);
    SB0();
  });
  STAMP(3); CSTAMP(22);
  { constexpr int after = SC.before[32] - L::NST - SC.before[23]; wait_vm_tie<after>(sv); }
  NOTE(53, __all(sv[1] == tg.a && sv[3] == tg.b) ? 1 : 2);
  for (unsigned spins = 0; !__all(sv[1] == tg.a && sv[3] == tg.b) && spins < (1u << 20); ++spins) {
    __builtin_amdgcn_s_sleep(1);
    sv = ld16c_asm(rs_ws, (unsigned)r * 16u, sv_off); wait_vm_tie<0>(sv);
  }
  const float mean = __uint_as_float(sv[0]), rstd = __uint_as_float(sv[2]);
  const int sb = rb * 4 + mi;
#pragma unroll
  for (int b = 0; b < NB1; ++b) {
    const int blk = 3 * g + (NJ ? 2 : b), which = blk >> 4, hp = blk & 15;
    const float sc = which == 0 ? rstd * (0.25f * 1.4426950408889634f) : rstd;
    float v[16];
#pragma unroll
    for (int i = 0; i < 4; ++i) { const f32x4 c4 = lds_f4(L_CSW + w * 384 + b * 128 + (8 * i + 4 * h) * 4);
#pragma unroll
      for (int e = 0; e < 4; ++e) v[4 * i + e] = (acc[b][4 * i + e] - mean * c4[e]) * sc; }
    const unsigned vo = which == 2 ? (unsigned)(h * 512 + ((r ^ (4 * h)) * 16)) : (unsigned)(lane * 16);
#pragma unroll
    for (int hd = 0; hd < 2; ++hd) {
      const u32x4 o = {pk(v[8 * hd], v[8 * hd + 1]), pk(v[8 * hd + 2], v[8 * hd + 3]), pk(v[8 * hd + 4], v[8 * hd + 5]), pk(v[8 * hd + 6], v[8 * hd + 7])};
      st16c(o, rs_ws, vo, WS_QKV + (unsigned)((which * 32 + 2 * hp + hd) * 64 + sb) * 1024u);
    }
  }
  STAMP(4); CSTAMP(18);
#pragma unroll
  for (int b = 0; b < 3; ++b) { cs[b] = f32x4{0.f, 0.f, 0.f, 0.f};
#pragma unroll
    for (int i = 0; i < 16; ++i) acc[b][i] = 0.f; }
  bool act;
  const unsigned qv = qkv_flag_voff(rb2, hp2, lane, act);
  u32x4 fq;
  SB0();
  static_for<32, 64>([&](auto tc) {
    constexpr int Tg = decltype(tc)::value, t = Tg - 32, s = Tg >> 2, p = t & 3;
    if constexpr (p == 3) {
      if (s == 15) cs_put(3, NJ * 3);
      if constexpr (s == 9) wait_vm<(SC.before[Tg] - SC.before[32] < SC.wait_stage(Tg, 10) ? SC.before[Tg] - SC.before[32] : SC.wait_stage(Tg, 10))>();
      else if constexpr (s == 14) { wait_vm<0>(); if (w == 0) { tie(fq); word_set(0, flags_ok(fq, act, tg) ? 1u : 0u); } }
      else if constexpr (s <= 13) wait_vm<SC.wait_stage(Tg, s + 1)>();
      if (s >= 14) bar(); else bar_raw();
      STAMP2(24 + s);
      if (s == 8) CSTAMP(56); if (s == 11) CSTAMP(57); if (s == 13) CSTAMP(58); if (s == 15) CSTAMP(59);
      if (s == 14) early = word_get(0) != 0u;
      if (s == 15) cs_get(3, NJ * 3);
    }
    if (NJ) op_at(tc, IC<0>{});
    static_for<0, 3>([&](auto bc) {
      constexpr int b = decltype(bc)::value;
      acc[b] = mfma(wg[t & 1][b], xf[t], acc[b]);
      if (t < 31) ld1b(t + 1, b);
      if (p == ((b + 2 * NJ) & 3) || (s == 15 && p == 2 && ((b + 2 * NJ) & 3) == 3)) { if (!(s == 15 && p == 3)) cs[b] = mfma16(sel, wg[t & 1][b], cs[b]); }
      SB0();
    });
    if (!NJ) op_at(tc, IC<0>{});
    op_at(tc, IC<1>{});
    if (Tg == 39) flag_store(rs_ws, WS_FQKV + (unsigned)((rb * 16 + g) * 64 + w * 8), tg, lane);
    if (Tg == 55 && w == 0) fq = ld16c_asm(rs_ws, qv, 0u);
    SB0();
  });
  STAMP(5); CSTAMP(19);
  const float r2 = rstd * rstd;
#pragma unroll
  for (int b = 0; b < 3; ++b) {
    float v[16];
#pragma unroll
    for (int i = 0; i < 4; ++i) { const f32x4 c4 = lds_f4(L_CSW + w * 384 + b * 128 + (8 * i + 4 * h) * 4);
#pragma unroll
      for (int e = 0; e < 4; ++e) v[4 * i + e] = acc[b][4 * i + e] - mean * c4[e]; }
    float hv[8];
#pragma unroll
    for (int i = 0; i < 8; ++i) hv[i] = fmaxf(v[i], 0.f) * v[8 + i] * r2;
    const u32x4 o = {pk(hv[0], hv[1]), pk(hv[2], hv[3]), pk(hv[4], hv[5]), pk(hv[6], hv[7])};
    if (NJ * 3 + b < nu) st16c(o, rs_ws, (unsigned)lane * 16u, WS_H + (unsigned)(sb * NU + u0 + NJ * 3 + b) * 1024u);
  }
  STAMP(6);
  if (NJ == 0 || nu == 6) wait_vm<3>(); else wait_vm<2>();
  return early;
}

template <bool FULL, class F1, class FM, class F2>
DI u32x4 attn_task(int j0, int qb, int hd, int lane, F1 side1, FM mid, F2 side2) {
  const int q = lane & 31, hh = lane >> 5, tq = q - 4 * hh;
  const bf16x8 qf = lds_b128(LA_Q + (unsigned)(hd * 4 + qb) * 1024u + lane * 16u);
  const unsigned kbase = LA_K + (unsigned)(hd * 12 + qb) * 1024u + lane * 16u;
  const float NEG = -__builtin_inff();
  bf16x8 kf[9];
#pragma unroll
  for (int j = 0; j < 9; ++j) if (FULL || j >= j0) kf[j] = lds_b128(kbase + j * 1024u);
  f32x16 c;
#pragma unroll
  for (int i = 0; i < 16; ++i) c[i] = 0.f;
  auto on = [&](int j) { return FULL || j >= j0; };
  float m0 = NEG, m1 = NEG;
  f32x16 s[2];
  {
    const f32x16 s8 = mfma(kf[8], qf, c);
    f32x16 s0;
    if (FULL) s0 = mfma(kf[0], qf, c);
    if (on(1)) s[1] = mfma(kf[1], qf, c);
    SB0();
#pragma unroll
    for (int i = 0; i < 16; i += 2) {
      const int c0 = (i & 3) + 8 * (i >> 2);
      const float a = c0 <= tq ? s8[i] : (FULL ? s0[i] : NEG), b = c0 + 1 <= tq ? s8[i + 1] : (FULL ? s0[i + 1] : NEG);
      if (i & 2) m1 = fmaxf(fmaxf(m1, a), b); else m0 = fmaxf(fmaxf(m0, a), b);
    }
    side1(0);
    SB0();
  }
#pragma unroll
  for (int j = 1; j < 8; ++j) {
    if (j < 7 && on(j + 1)) s[(j + 1) & 1] = mfma(kf[j + 1], qf, c);
    SB0();
    if (on(j)) {
#pragma unroll
      for (int i = 0; i < 8; i += 2) { m0 = fmaxf(fmaxf(m0, s[j & 1][i]), s[j & 1][i + 1]); m1 = fmaxf(fmaxf(m1, s[j & 1][8 + i]), s[j & 1][9 + i]); }
    }
    side1(j);
    SB0();
  }
  const float m = xhalf_max(fmaxf(m0, m1));
  mid();
#pragma unroll
  for (int i = 0; i < 16; ++i) c[i] = -m;
  const int grp = (lane >> 4) & 1, q4 = (lane & 15) >> 2, p4 = lane & 3;
  const unsigned vb = grp ? L_ONES + 128u : LA_V + (unsigned)(hd * 12 + qb) * 1024u + (unsigned)((p4 & 1) * 512 + (((4 * hh + q4) ^ (4 * (p4 & 1))) * 16) + (p4 >> 1) * 8);
  bf16x8 vf[2][2];
  auto ldv = [&](int j, int slot) {
#pragma unroll
    for (int u = 0; u < 2; ++u) vf[slot][u] = cat(lds_tr(vb + j * 1024u + u * 256u), lds_tr(vb + j * 1024u + u * 256u + 128u));
  };
  f32x16 acc;
#pragma unroll
  for (int i = 0; i < 16; ++i) acc[i] = 0.f;
  auto pv = [&](int slot, const unsigned (&pp)[8]) {
#pragma unroll
    for (int u = 0; u < 2; ++u) acc = mfma(vf[slot][u], __builtin_bit_cast(bf16x8, u32x4{pp[4 * u], pp[4 * u + 1], pp[4 * u + 2], pp[4 * u + 3]}), acc);
  };
  f32x16 s8, s0;
  if (on(1)) { s[1] = mfma(kf[1], qf, c); ldv(1, 1); }
#pragma unroll
  for (int j = 1; j < 8; ++j) {
    if (j < 7) { if (on(j + 1)) { s[(j + 1) & 1] = mfma(kf[j + 1], qf, c); ldv(j + 1, (j + 1) & 1); } }
    else { s8 = mfma(kf[8], qf, c); if (FULL) s0 = mfma(kf[0], qf, c); ldv(8, 0); }
    SB0();
    if (on(j)) {
      unsigned pp[8];
#pragma unroll
      for (int i = 0; i < 8; ++i) pp[i] = pk(__builtin_amdgcn_exp2f(s[j & 1][2 * i]), __builtin_amdgcn_exp2f(s[j & 1][2 * i + 1]));
      pv(j & 1, pp);
    }
    side2(j);
    SB0();
  }
  {
    if (FULL) ldv(0, 1);
    unsigned p8[8], p0[8];
#pragma unroll
    for (int i = 0; i < 8; ++i) {
      const int c0 = ((2 * i) & 3) + 8 * (i >> 1);
      const bool va = c0 <= tq, vb_ = c0 + 1 <= tq;
      const float a = va ? s8[2 * i] : (FULL ? s0[2 * i] : NEG), b = vb_ ? s8[2 * i + 1] : (FULL ? s0[2 * i + 1] : NEG);
      const unsigned pp = pk(__builtin_amdgcn_exp2f(a), __builtin_amdgcn_exp2f(b));
      const unsigned mk = (va ? 0xffffu : 0u) | (vb_ ? 0xffff0000u : 0u);
      p8[i] = pp & mk; p0[i] = pp & ~mk;
    }
    pv(0, p8);
    if (FULL) pv(1, p0);
  }
  const float inv = __builtin_amdgcn_rcpf(acc[8]);
  return u32x4{pk(acc[0] * inv, acc[1] * inv), pk(acc[2] * inv, acc[3] * inv), pk(acc[4] * inv, acc[5] * inv), pk(acc[6] * inv, acc[7] * inv)};
}

#define H_FLAG_AT 2
constexpr int DW = 10, DA = 8, NK3 = 30, NDN = 22, OCHK = NDN - (DA - 1), OPOLL = OCHK - 5, XLD = 20;
struct Sched3 {
  int before[NK3 + 1] = {}, lastW[NK3] = {}, lastA[NK3] = {};
  constexpr Sched3() {
    int c = 0;
    for (int i = 0; i < DW - 1; ++i) { c += 2; lastW[i] = c; }
    for (int i = 0; i < DA - 1; ++i) { c += 2; lastA[i] = c; }
    for (int i = 0; i < NK3; ++i) {
      before[i] = c;
      if (i + DW - 1 < NK3) { c += 2; lastW[i + DW - 1] = c; }
      if (i + DA - 1 < NK3) { c += 2; lastA[i + DA - 1] = c; }
      if (i == XLD) c += 4;
    }
    before[NK3] = c;
  }
  constexpr int wait(int i) const { const int w = i + 1 < NK3 ? lastW[i + 1] : 0; return before[i] - (lastA[i] > w ? lastA[i] : w); }
};
DI unsigned l3w(int kq) { return kq < 3 ? 86016u + (unsigned)kq * (DW * 2048u) : 0u; }
constexpr unsigned L3_RED = 28672;

DI void phase23(const Ptrs& P, bool early, int rb1, int g1, u64 epoch, int rb, int hp, int rb64, int cb, int rot, int w, int lane, Tag tg, Stamps& T) {
  const rsrc_t rs_ws = mk_rsrc(P.ws, WS_BYTES);
  for (unsigned i = threadIdx.x; i < ONES_BYTES / 16; i += 512) *(LAS u32x4*)(lds + L_ONES + i * 16) = u32x4{ONES2, ONES2, ONES2, ONES2};
  NOTE(63, early ? 1 : 2);
  if (!early) {
    bool act; const unsigned qv = qkv_flag_voff(rb, hp, lane, act);
    if (w == 0) flags_spin(rs_ws, qv, act, tg);
    bar();
    const AttnSrc asrc = attn_src(rb, hp, w);
#pragma unroll
    for (int i = 0; i < 7; ++i) attn_issue(rs_ws, asrc, w, lane, i, true);
    wait_vm<0>();
  }
  bar();
  STAMP(9);
  const unsigned fh_off = WS_FH + (unsigned)((rb1 * 16 + g1) * 64 + w * 8);
  const int h = lane >> 5, nsub = (lane >> 4) & 1, q4 = (lane & 15) >> 2, p4 = lane & 3;
  f32x16 acc[4];
  u32x2 xr[4];
  const int kq = w - 4;
  const bool keeper = blockIdx.x == 0 && w == 4;
  u32x4 fe[4];
  const rsrc_t rs_d = mk_rsrc(P.wd, FF * DM * 2), rs_o = mk_rsrc(P.wo, DM * DM * 2);
  const int ulo = kq < 3 ? 22 * kq : 65, nun = kq < 2 ? 22 : 21;
  unsigned vw[2];
#pragma unroll
  for (int j = 0; j < 2; ++j) vw[j] = (unsigned)(8 * j + (lane >> 3)) * 1024u + (unsigned)(((lane & 7) ^ (((lane >> 4) & 1) << 2)) * 16);
  const unsigned ring = __builtin_amdgcn_readfirstlane(l3w(kq)), vo = (unsigned)lane * 16u;
  auto unit = [&](int i) { const int r = i + rot; return i < NDN ? (i < nun ? ulo + (r >= nun ? r - nun : r) : -1) : 8 * kq + ((i - NDN + rot) & 7); };
  auto reqW = [&](int i) {
    const int u = unit(i);
#pragma unroll
    for (int j = 0; j < 2; ++j) dma16(ring + (unsigned)(i % DW) * 2048u + j * 1024u, u < 0 ? 0x40000000u : vw[j], i < NDN ? rs_d : rs_o, (unsigned)cb * 128u + (unsigned)(u < 0 ? 0 : u) * 16384u);
  };
  const unsigned hv = WS_FH + (unsigned)((rb64 >> 1) * 1024 + lane * 16), ov = WS_FO + (unsigned)((rb64 >> 1) * 1024 + lane * 16);
  const int pg = lane >> 2;
  const bool pair_ok = ((lane & 3) & 1) == (rb64 & 1);
  const bool h_act = pair_ok && (((pg + 1) * NU) >> 4) > ulo && ((pg * NU) >> 4) < ulo + nun, o_act = pair_ok && (pg >> 2) == kq;
  u32x4 fh;
  if (w >= 4) __builtin_amdgcn_s_setprio(3);
  {
    const int qb = w & 3, hd = w >> 2, sb = 4 * rb + qb;
    auto side1 = [&](int j) {
      if (w >= 4 && j >= 1) reqW(j - 1);
    };
    auto mid = [&]() { STAMP(10); };
    auto side2 = [&](int j) {
      if (w >= 4 && j <= 2) reqW(6 + j);
      if (j == H_FLAG_AT) { if (w >= 4) wait_vm<18>(); else wait_vm<0>(); flag_store(rs_ws, fh_off, tg, lane); }
      if (w >= 4 && j == 7) fh = ld16c_asm(rs_ws, hv, 0u);
    };
    u32x4 o;
    if (sb >= 8) o = attn_task<true>(0, qb, hd, lane, side1, mid, side2); else o = attn_task<false>(8 - sb, qb, hd, lane, side1, mid, side2);
    st16c(o, rs_ws, (unsigned)lane * 16u, WS_O + (unsigned)(sb * 32 + 2 * hp + hd) * 1024u);
    STAMP(11);
  }
  const unsigned fo_off = WS_FO + (unsigned)((rb * 16 + hp) * 64 + w * 8);
  if (w < 4) {
    wait_vm<0>();
    flag_store(rs_ws, fo_off, tg, lane);
    STAMP(12);
  } else {
    wait_vm_tie<1>(fh);
    NOTE(55, flags_ok(fh, h_act, tg) ? 1 : 2);
    if (!flags_ok(fh, h_act, tg)) flags_spin(rs_ws, hv, h_act, tg);
    STAMP(13);
    u32x4 A[DA][2];
    auto reqA = [&](int i) {
      const int u = unit(i);
#pragma unroll
      for (int mb = 0; mb < 2; ++mb)
        A[i % DA][mb] = ld16c_asm(rs_ws, u < 0 ? 0x40000000u : vo, i < NDN ? WS_H + (unsigned)((2 * rb64 + mb) * NU + (u < 0 ? 0 : u)) * 1024u : WS_O + (unsigned)((2 * rb64 + mb) * 32 + u) * 1024u);
    };
#pragma unroll
    for (int i = 0; i < DA - 1; ++i) reqA(i);
#pragma unroll
    for (int b = 0; b < 4; ++b)
#pragma unroll
      for (int i = 0; i < 16; ++i) acc[b][i] = 0.f;
    unsigned wa[2];
#pragma unroll
    for (int nb = 0; nb < 2; ++nb) wa[nb] = ring + (unsigned)h * 512u + q4 * 128u + (unsigned)((nb ^ (q4 >> 1)) * 64) + nsub * 32u + p4 * 8u;
    const int row = 64 * rb64 + 32 * (kq >> 1) + (lane & 31), col0 = 64 * cb + 32 * (kq & 1) + 4 * h;
    const unsigned short* xp = (const unsigned short*)P.x + (size_t)row * DM + col0;
    static constexpr Sched3 S3{};
    u32x4 fo;
    bf16x8 wf[2][2];
    auto ldw = [&](int i) {
#pragma unroll
      for (int nb = 0; nb < 2; ++nb) { const unsigned a = wa[nb] + (unsigned)(i % DW) * 2048u; wf[i & 1][nb] = cat(lds_tr(a), lds_tr(a + 1024u)); }
    };
    ldw(0);
    static_for<0, NK3>([&](auto ic) {
      constexpr int i = decltype(ic)::value;
      wait_vm<S3.wait(i)>();
      tie(A[i % DA][0]); tie(A[i % DA][1]);
      if (i == 0) STAMP(14);
      if constexpr (i == OCHK) {
        wait_vm_tie<S3.before[i] - S3.before[OPOLL + 1]>(fo);
        NOTE(54, flags_ok(fo, o_act, tg) ? 1 : 2);
        if (!flags_ok(fo, o_act, tg)) flags_spin(rs_ws, ov, o_act, tg);
      }
      SB0();
      acc[0] = mfma(wf[i & 1][0], __builtin_bit_cast(bf16x8, A[i % DA][0]), acc[0]);
      if constexpr (i + 1 < NK3) ldw(i + 1);
      SB0();
      acc[1] = mfma(wf[i & 1][1], __builtin_bit_cast(bf16x8, A[i % DA][0]), acc[1]);
      SB0();
      if constexpr (i + DW - 1 < NK3) reqW(i + DW - 1);
      acc[2] = mfma(wf[i & 1][0], __builtin_bit_cast(bf16x8, A[i % DA][1]), acc[2]);
      SB0();
      acc[3] = mfma(wf[i & 1][1], __builtin_bit_cast(bf16x8, A[i % DA][1]), acc[3]);
      SB0();
      if constexpr (i + DA - 1 < NK3) reqA(i + DA - 1);
      if (i == 1) flag_store(rs_ws, fo_off, tg, lane);
      if (i == OPOLL) fo = ld16c_asm(rs_ws, ov, 0u);
      if (i == XLD) {
#pragma unroll
        for (int q = 0; q < 4; ++q) xr[q] = *(const GAS u32x2*)(xp + 8 * q);
      }
      if (i == 22 && keeper) {
#pragma unroll
        for (int q = 0; q < 4; ++q) fe[q] = ld16c_asm(rs_ws, WS_FQKV + (unsigned)(q * 64 + lane) * 64u, 0u);
      }
      SB0();
    });
    STAMP(15);
  }
  bar();
  if (w >= 4) {
#pragma unroll
    for (int b = 0; b < 4; ++b) {
      if (b != kq) {
        const unsigned base = L3_RED + (unsigned)(b * 3 + (kq - b - 1 + 4) % 4) * 4096u + lane * 16u;
#pragma unroll
        for (int q = 0; q < 4; ++q) *(LAS f32x4*)(lds + base + q * 1024u) = f32x4{acc[b][4 * q], acc[b][4 * q + 1], acc[b][4 * q + 2], acc[b][4 * q + 3]};
      }
    }
  }
  bar();
  if (w >= 4) {
    f32x16 r = kq == 0 ? acc[0] : (kq == 1 ? acc[1] : (kq == 2 ? acc[2] : acc[3]));
#pragma unroll
    for (int s = 0; s < 3; ++s)
#pragma unroll
      for (int q = 0; q < 4; ++q) { const f32x4 v = lds_f4(L3_RED + (unsigned)(kq * 3 + s) * 4096u + lane * 16u + q * 1024u);
#pragma unroll
        for (int e = 0; e < 4; ++e) r[4 * q + e] += v[e]; }
    const int row = 64 * rb64 + 32 * (kq >> 1) + (lane & 31), col0 = 64 * cb + 32 * (kq & 1) + 4 * h;
    unsigned short* op = (unsigned short*)P.out + (size_t)row * DM + col0;
#pragma unroll
    for (int q = 0; q < 4; ++q)
      *(GAS u32x2*)(op + 8 * q) = u32x2{pk(r[4 * q] + bf_lo(xr[q][0]), r[4 * q + 1] + bf_hi(xr[q][0])), pk(r[4 * q + 2] + bf_lo(xr[q][1]), r[4 * q + 3] + bf_hi(xr[q][1]))};
    if (keeper) {
#pragma unroll
      for (int q = 0; q < 4; ++q) { tie(fe[q]); if (!flags_ok(fe[q], true, tg)) flags_spin(rs_ws, WS_FQKV + (unsigned)(q * 64 + lane) * 64u, true, tg); }
      if (lane == 0) __hip_atomic_store((GAS u64*)(P.ws + WS_EPOCH), epoch, __ATOMIC_RELAXED, __HIP_MEMORY_SCOPE_AGENT);
    }
  }
}

__global__ __launch_bounds__(512, 2) void mk_kernel(const void* x, const void* wqkv, const void* wo, const void* wg, const void* wu, const void* wd, void* out, char* ws) {
  u64 pc0 = 0;
  asm volatile("mk_code_begin:\n\ts_getpc_b64 %0" : "=s"(pc0));
  {
    unsigned len;
    asm volatile("s_mov_b32 %0, mk_code_end-mk_code_begin" : "=s"(len));
    const unsigned wv = __builtin_amdgcn_readfirstlane(threadIdx.x >> 6);
    const unsigned per = ((len >> 5) + 1023u) & ~1023u;
    const unsigned o = (blockIdx.x >> 3) * per + wv * 1024u;
    dma16(L_DUMMY + wv * 1024u, (threadIdx.x & 63u) * 16u, mk_rsrc((const void*)(pc0 & ~255ull), len & ~15u), wv * 1024u < per ? o : 0x40000000u);
  }
  const Ptrs P{x, wqkv, wo, wg, wu, wd, out, ws, pc0};
  Stamps T;
  STAMP(0); CSTAMP(20);
  __builtin_amdgcn_s_setprio(1);
  const int b = blockIdx.x, xcd = b & 7, idx = b >> 3;
  const int w = __builtin_amdgcn_readfirstlane(threadIdx.x >> 6), lane = threadIdx.x & 63;
  Tag tg; u64 epoch;
  const int rb = 2 * (idx >> 2) + (xcd >> 2), g = (xcd & 3) * 4 + (idx & 3), rot1 = ((idx >> 2) + 2 * (idx & 3)) & 7;
  bool early;
  if (w < 4) early = phase1<0>(P, rb, g, rot1, w, lane, rb, g, tg, epoch, T); else early = phase1<1>(P, rb, g, rot1, w - 4, lane, rb, g, tg, epoch, T);
  const int rot3 = ((idx & 7) * 2 + 5 * (idx >> 3)) % 21;
  phase23(P, early, rb, g, epoch, rb, g, xcd * 4 + (idx >> 3), idx & 7, rot3, w, lane, tg, T);
  asm volatile("mk_code_end:");
}

}

extern "C" void kernel_launch(void* const* d_in, const int* in_sizes, int n_in, void* d_out, int out_size, void* d_ws, size_t ws_size, hipStream_t stream) {
  const void *x = d_in[0], *wqkv = d_in[1], *wo = d_in[2], *wg = d_in[4], *wu = d_in[5], *wd = d_in[6];
  mk_kernel<<<256, 512, 0, stream>>>(x, wqkv, wo, wg, wu, wd, d_out, (char*)d_ws);
}
```

```cpp
#include <hip/hip_runtime.h>
#include <hip/hip_bf16.h>
#include <stdint.h>

namespace {

typedef short bf16x8 __attribute__((ext_vector_type(8)));
typedef short s16x4 __attribute__((ext_vector_type(4)));
typedef float f32x16 __attribute__((ext_vector_type(16)));
typedef float f32x4 __attribute__((ext_vector_type(4)));
typedef float f32x2 __attribute__((ext_vector_type(2)));
typedef unsigned u32x4 __attribute__((ext_vector_type(4)));
typedef unsigned u32x2 __attribute__((ext_vector_type(2)));
typedef __bf16 bf16x2_t __attribute__((ext_vector_type(2)));
typedef short v4i16_t __attribute__((ext_vector_type(4)));
typedef __amdgpu_buffer_rsrc_t rsrc_t;
#define LAS __attribute__((address_space(3)))
#define GAS __attribute__((address_space(1)))
#define DI __device__ __forceinline__
#define SB0() __builtin_amdgcn_sched_barrier(0)
typedef unsigned long long u64;

constexpr int SEQ = 2048, DM = 512, FF = 1376, NU = 86;
constexpr unsigned WS_EPOCH = 0;
constexpr unsigned WS_FQKV = 16384, WS_FH = 32768, WS_FO = 49152;
constexpr unsigned WS_RS = 65536;
constexpr unsigned WS_QKV = 1u << 20;
constexpr unsigned WS_H = 7u << 20;
constexpr unsigned WS_O = 13u << 20;
constexpr unsigned WS_BYTES = 16u << 20;
constexpr unsigned SLOT1 = 28672;
constexpr unsigned L_CSP = 143360;
constexpr unsigned L_CSW = 146432;
constexpr unsigned L_ONES = 152576;
constexpr unsigned ONES_BYTES = 9984;
constexpr unsigned L_WORD = 163584;
constexpr unsigned LDS_BYTES = 163840;
constexpr unsigned LA_Q = 28672, LA_K = LA_Q + 8192, LA_V = LA_K + 24576;

__shared__ __attribute__((aligned(1024))) char lds[LDS_BYTES];

DI unsigned pk(float a, float b) { f32x2 v = {a, b}; bf16x2_t r = __builtin_convertvector(v, bf16x2_t); return __builtin_bit_cast(unsigned, r); }
DI float dot2(unsigned a, unsigned b, float c) { return __builtin_amdgcn_fdot2_f32_bf16(__builtin_bit_cast(bf16x2_t, a), __builtin_bit_cast(bf16x2_t, b), c, false); }
DI rsrc_t mk_rsrc(const void* p, unsigned bytes) { return __builtin_amdgcn_make_buffer_rsrc((void*)p, 0, bytes, 0x00020000); }
DI void dma16(unsigned ldsaddr, unsigned voff, rsrc_t rs, unsigned soff) {
  asm volatile("s_mov_b32 m0, %0\n\ts_nop 0\n\tbuffer_load_dwordx4 %1, %2, %3 offen lds" ::"s"(ldsaddr), "v"(voff), "s"(rs), "s"(soff) : "memory");
}
DI void dma16c(unsigned ldsaddr, unsigned voff, rsrc_t rs, unsigned soff) {
  asm volatile("s_mov_b32 m0, %0\n\ts_nop 0\n\tbuffer_load_dwordx4 %1, %2, %3 offen sc1 lds" ::"s"(ldsaddr), "v"(voff), "s"(rs), "s"(soff) : "memory");
}
DI void pf_lines(unsigned ldsaddr, unsigned voff, rsrc_t rs) {
  asm volatile("s_mov_b32 m0, %0\n\ts_nop 0\n\tbuffer_load_dword %1, %2, 0 offen lds" ::"s"(ldsaddr), "v"(voff), "s"(rs) : "memory");
}
template <int N> DI void wait_vm() { asm volatile("s_waitcnt vmcnt(%0)" ::"n"(N) : "memory"); }
template <int N> DI void wait_vm_tie(u32x4& v) { asm volatile("s_waitcnt vmcnt(%1)" : "+v"(v) : "n"(N) : "memory"); }
template <int V> struct IC { static constexpr int value = V; };
template <int I, int N, class F> DI void static_for(F&& f) { if constexpr (I < N) { f(IC<I>{}); static_for<I + 1, N>(f); } }
DI void bar_raw() { asm volatile("s_barrier" ::: "memory"); }
DI void tie(u32x4& v) { asm volatile("" : "+v"(v) :: "memory"); }
DI void bar() { asm volatile("s_waitcnt lgkmcnt(0)\n\ts_barrier" ::: "memory"); }
DI f32x16 mfma(bf16x8 a, bf16x8 b, f32x16 c) { return __builtin_amdgcn_mfma_f32_32x32x16_bf16(a, b, c, 0, 0, 0); }
DI bf16x8 lds_b128(unsigned a) { return *(const LAS bf16x8*)(lds + a); }
#define MK_REAL 1
DI s16x4 lds_tr(unsigned a) { return __builtin_amdgcn_ds_read_tr16_b64_v4i16((LAS v4i16_t*)(lds + a)); }
DI f32x4 mfma16(bf16x8 a, bf16x8 b, f32x4 c) { return __builtin_amdgcn_mfma_f32_16x16x32_bf16(a, b, c, 0, 0, 0); }
DI f32x4 lds_f4(unsigned a) { return *(const LAS f32x4*)(lds + a); }
DI bf16x8 cat(s16x4 lo, s16x4 hi) { return __builtin_shufflevector(lo, hi, 0, 1, 2, 3, 4, 5, 6, 7); }
DI float xhalf_sum(float v) { auto r = __builtin_amdgcn_permlane32_swap(__float_as_uint(v), __float_as_uint(v), false, false); return __uint_as_float(r[0]) + __uint_as_float(r[1]); }
DI float xhalf_max(float v) { auto r = __builtin_amdgcn_permlane32_swap(__float_as_uint(v), __float_as_uint(v), false, false); return fmaxf(__uint_as_float(r[0]), __uint_as_float(r[1])); }
DI void st16c(u32x4 v, rsrc_t rs, unsigned voff, unsigned soff) { __builtin_amdgcn_raw_buffer_store_b128(v, rs, voff + soff, 0, 16); }
DI void st8c(u32x2 v, rsrc_t rs, unsigned voff, unsigned soff) { __builtin_amdgcn_raw_buffer_store_b64(v, rs, voff, soff, 16); }
constexpr unsigned ONES2 = 0x3F803F80u;
template <int CTRL> DI float dpp_add(float v) { return v + __int_as_float(__builtin_amdgcn_update_dpp(0, __float_as_int(v), CTRL, 0xf, 0xf, true)); }
DI float wave_sum(float v) {
  v = dpp_add<0xB1>(v); v = dpp_add<0x4E>(v); v = dpp_add<0x141>(v); v = dpp_add<0x140>(v);
  auto a = __builtin_amdgcn_permlane16_swap(__float_as_uint(v), __float_as_uint(v), false, false);
  v = __uint_as_float(a[0]) + __uint_as_float(a[1]);
  return xhalf_sum(v);
}
DI float bf_lo(unsigned u) { return __uint_as_float(u << 16); }
DI float bf_hi(unsigned u) { return __uint_as_float(u & 0xffff0000u); }
DI u32x4 ld16_asm(rsrc_t rs, unsigned voff, unsigned soff) { u32x4 v; asm volatile("buffer_load_dwordx4 %0, %1, %2, %3 offen" : "=v"(v) : "v"(voff), "s"(rs), "s"(soff) : "memory"); return v; }
DI u32x4 ld16c_asm(rsrc_t rs, unsigned voff, unsigned soff) { u32x4 v; asm volatile("buffer_load_dwordx4 %0, %1, %2, %3 offen sc1" : "=v"(v) : "v"(voff), "s"(rs), "s"(soff) : "memory"); return v; }
#define STAMP2(i) do {} while (0)
struct Stamps {};
#define STAMP(i) do {} while (0)
#define CSTAMP(i) do {} while (0)
#define NOTE(i, v) do {} while (0)

struct Ptrs { const void *x, *wqkv, *wo, *wg, *wu, *wd; void* out; char* ws; u64 pc; };
struct Tag { unsigned a, b; };

DI void flag_store(rsrc_t rs_ws, unsigned off, Tag tg, int lane) { if (lane == 0) st8c(u32x2{tg.a, tg.b}, rs_ws, 0u, off); }
DI bool flags_ok(u32x4 v, bool act, Tag tg) { return __all(!act || (v[0] == tg.a && v[1] == tg.b && v[2] == tg.a && v[3] == tg.b)); }
DI void flags_spin(rsrc_t rs_ws, unsigned voff, bool act, Tag tg) {
  for (unsigned spins = 0; spins < (1u << 20); ++spins) {
    u32x4 v = ld16c_asm(rs_ws, voff, 0u); wait_vm_tie<0>(v);
    if (flags_ok(v, act, tg)) break;
    __builtin_amdgcn_s_sleep(1);
  }
}
DI void word_set(int i, unsigned v) { *(volatile LAS unsigned*)(lds + L_WORD + i * 4) = v; }
DI unsigned word_get(int i) { return __builtin_amdgcn_readfirstlane(*(volatile LAS unsigned*)(lds + L_WORD + i * 4)); }

struct AttnSrc { unsigned q, kv; int sb0; };
DI AttnSrc attn_src(int rb, int hp, int w) {
  AttnSrc a;
  a.q = __builtin_amdgcn_readfirstlane(WS_QKV + (unsigned)((2 * hp + (w >> 2)) * 64 + 4 * rb + (w & 3)) * 1024u);
  a.kv = __builtin_amdgcn_readfirstlane(WS_QKV + (unsigned)(((1 + (w >> 2)) * 32 + 2 * hp + ((w >> 1) & 1)) * 64) * 1024u);
  a.sb0 = __builtin_amdgcn_readfirstlane(4 * rb - 8 + 6 * (w & 1));
  return a;
}
DI void attn_issue(rsrc_t rs_ws, const AttnSrc& a, int w, int lane, int piece, bool go) {
  const unsigned vo = (unsigned)lane * 16u + (go ? 0u : 0x40000000u);
  if (piece == 0) { dma16c(LA_Q + (unsigned)w * 1024u, vo, rs_ws, a.q); return; }
  const int sbk = a.sb0 + piece - 1;
  dma16c(LA_K + (unsigned)((w >> 1) * 12 + 6 * (w & 1) + piece - 1) * 1024u, vo, rs_ws, a.kv + (unsigned)(sbk < 0 ? 0 : sbk) * 1024u);
}
DI unsigned qkv_flag_voff(int rb, int hp, int lane, bool& act) {
  const int c = lane >> 2, kind = c == 0 ? 0 : (c <= 3 ? 1 : 2), d = c == 0 ? 0 : (c - 1) % 3, rr = rb - d;
  act = lane < 28 && rr >= 0;
  return act ? WS_FQKV + (unsigned)((rr * 16 + (kind * 16 + hp) / 3) * 64 + (lane & 3) * 16) : 0u;
}

constexpr unsigned L_DUMMY = 152576;
#define TLBW_N 0
#define PFX_N 0
#define PF3_N 8
#define PF3_T 33
#define P1_HOLD 12
constexpr int OP_NONE = -1, OP_PFW = 200, OP_ATT = 400;
template <int NJ> struct Sched1 {
  static constexpr int NA = NJ ? 4 : 3, NST = NJ ? 2 : 4;
  static constexpr int n(int sg) { return sg < 8 ? NA : 3; }
  static constexpr int TB(int s) { return s < 8 ? 4 * s + 2 : 4 * s + 3; }
  static constexpr int extras(int T) { return (T == 5 ? 1 : 0)   + (T == 22 ? 1 : 0)   + (T == 39 ? 1 : 0)  ; }
  static constexpr int NPRO = 4;
  static constexpr int NTW = TLBW_N;
  static constexpr int NPX = PFX_N;
  static constexpr int PRO = 1 + NPX + NPRO * NA + 1 + NTW;
  static constexpr int HOLD = P1_HOLD;
  int op[64][2] = {}, before[65] = {}, last[16] = {};
  constexpr Sched1() {
    int sg = NPRO, j = 0, pf = 0, att = 0, c = PRO;
    last[0] = 1 + NPX + NA;
    for (int i = 1; i < NPRO; ++i) last[i] = 1 + NPX + NA + 1 + NTW + i * NA;
    for (int T = 0; T < 64; ++T) {
      before[T] = c;
      for (int k = 0; k < 2; ++k) {
        int o = OP_NONE;
        if (k == 0 || T < 14) {
          if (sg <= 15 && (sg < 5 || T >= TB(sg - 4)) && (sg < 8 || T >= 4 * sg - HOLD)) {   o = sg * 8 + j; if (++j == n(sg)) { last[sg] = c + 1; ++sg; j = 0; } }
          else if (pf == 0 && T >= 4) o = OP_PFW + pf++;
          else if (pf >= 1 && pf < 1 + PF3_N && T >= PF3_T) o = OP_PFW + pf++;
        }
        if (T >= 59 && att < 7 && (k == 0 || T >= 61)) o = OP_ATT + att++;
        op[T][k] = o;
        if (o != OP_NONE) ++c;
      }
      c += extras(T);
      if (T == 31) c += NST;
    }
    before[64] = c;
  }
  constexpr int wait_stage(int T, int sg) const { return before[T] - last[sg]; }
};

template <int NJ>
DI bool phase1(const Ptrs& P, int rb, int g, int rot, int mi, int lane, int rb2, int hp2, Tag& tg, u64& epoch, Stamps& T_) {
  Stamps& T = T_;
  typedef Sched1<NJ> L;
  static constexpr Sched1<NJ> SC{};
  const int w = NJ * 4 + mi;
  u64 e0 = 0;
  if (w == 0) asm volatile("s_load_dwordx2 %0, %1, 0x0" : "=s"(e0) : "s"(P.ws) : "memory");
  const rsrc_t rs_x = mk_rsrc(P.x, SEQ * DM * 2), rs_q = mk_rsrc(P.wqkv, DM * 1536 * 2);
  const rsrc_t rs_f = mk_rsrc(NJ ? P.wu : P.wg, DM * FF * 2), rs_ws = mk_rsrc(P.ws, WS_BYTES);
  const unsigned sx0 = (unsigned)rb * 131072u, sq0 = (unsigned)g * 192u;
  constexpr int NQ = NJ ? 2 : 1;
  constexpr int NA = 2 + NQ;
  unsigned vx[2], vq[2], vf[3];
#pragma unroll
  for (int j = 0; j < 2; ++j) vx[j] = (unsigned)(16 * w + 8 * j + (lane >> 3)) * 1024u + (unsigned)(((lane & 7) ^ (4 * j + (lane >> 4))) * 16);
#pragma unroll
  for (int j = 0; j < NQ; ++j) { const int ins = NJ ? 2 * mi + j : 8 + mi; const int G = ins * 64 + lane;
    vq[j] = (unsigned)(G / 12) * 3072u + (unsigned)(G % 12) * 16u; }
  const int u0 = (g * NU) >> 4, nu = (((g + 1) * NU) >> 4) - u0;
  const unsigned sf0 = (unsigned)u0 * 32u;
  auto piece = [&](int gs, int j) {
    const unsigned slot = (unsigned)(gs % 5) * SLOT1;
    const unsigned c = (unsigned)(gs + rot) & 7u;
    if (gs < 8) {
      if (j < 2) dma16(slot + (unsigned)(2 * w + j) * 1024u, vx[j], rs_x, sx0 + c * 128u);
      else dma16(slot + 16384u + (unsigned)(NJ ? 2 * mi + j - 2 : 8 + mi) * 1024u, vq[j - 2], rs_q, sq0 + c * (64u * 3072u));
    } else dma16(slot + NJ * 12288u + (unsigned)(3 * mi + j) * 1024u, vf[j], rs_f, sf0 + c * (64u * 2752u));
  };
#pragma unroll
  for (int j = 0; j < NA; ++j) piece(0, j);
  const unsigned myrow = (unsigned)(128 * rb + 8 * g + w);
  u32x4 xrow = ld16_asm(rs_x, (unsigned)lane * 16u, myrow * 1024u);
#pragma unroll
  for (int sg = 1; sg < L::NPRO; ++sg)
#pragma unroll
    for (int j = 0; j < NA; ++j) piece(sg, j);
  SB0();
  STAMP(1);
#pragma unroll
  for (int j = 0; j < 3; ++j) { const int G = (3 * mi + j) * 64 + lane, kg = G / 48, rr = G % 48, unit = (rr >> 3) ^ NJ;
    vf[j] = unit < nu ? (unsigned)(4 * kg + ((rr & 7) >> 1)) * 2752u + (unsigned)(unit * 32 + (rr & 1) * 16) : 0x40000000u; }
  const int r = lane & 31, h = lane >> 5, nsub = (lane >> 4) & 1, q4 = (lane & 15) >> 2, p4 = lane & 3;
  unsigned xa[4], ro[4];
#pragma unroll
  for (int p = 0; p < 4; ++p) { const int ks = (p + mi) & 3;
    xa[p] = (unsigned)(32 * mi + r) * 128u + (unsigned)(((2 * ks + h) ^ ((r >> 1) & 7)) * 16);
    ro[p] = (unsigned)ks * 3072u; }
  const unsigned wl = (unsigned)(2 * h) * 768u + q4 * 32u + p4 * 8u;
  const unsigned wa = 16384u + (unsigned)(2 * h) * 768u + q4 * 192u + nsub * 32u + p4 * 8u;
  const unsigned wbE = nsub * 12288u + wl + nsub * 128u;
  const unsigned wbO = nsub * 12288u + wl - nsub * 128u;
  const AttnSrc asrc = attn_src(rb2, hp2, w);
  bool early = false;
  auto warm = [&](int i) {
    const unsigned xl = blockIdx.x >> 3, dst = L_DUMMY + (unsigned)w * 256u;
    if (i == 0) {
      const unsigned q = (unsigned)(w & 1) * 64u + (unsigned)lane, rr = q / 7u, ln = q - rr * 7u;
      const unsigned ub = (unsigned)((((blockIdx.x & 3) * 4 * NU) >> 4) * 32) & ~127u;
      pf_lines(dst, (w < 4 && q < 112u) ? (16u * xl + rr) * 2752u + ub + ln * 128u : 0x40000000u, mk_rsrc((w & 2) ? P.wu : P.wg, DM * FF * 2));
    } else {
      const unsigned q = xl * 8u + (unsigned)w + 256u * (unsigned)(i - 1);
      dma16(L_DUMMY + 2048u + (unsigned)w * 1024u, (unsigned)lane * 16u + (q < 1888u ? 0u : 0x40000000u), mk_rsrc(q < 1376u ? P.wd : P.wo, q < 1376u ? FF * DM * 2 : DM * DM * 2), (q < 1376u ? q : q - 1376u) * 1024u);
    }
  };
  auto op_at = [&](auto tc, auto kc) {
    constexpr int o = SC.op[decltype(tc)::value][decltype(kc)::value];
    if constexpr (o >= OP_ATT) attn_issue(rs_ws, asrc, w, lane, o - OP_ATT, early);
    else if constexpr (o >= OP_PFW) warm(o - OP_PFW);
    else if constexpr (o >= 0) piece(o >> 3, o & 7);
  };
  bf16x8 sel;
  { const unsigned v = (lane & 15) == ((lane >> 4) & 1) ? ONES2 : 0u; sel = __builtin_bit_cast(bf16x8, u32x4{v, v, v, v}); }
  if (w == 0) {
    asm volatile("s_waitcnt lgkmcnt(0)" : "+s"(e0) :: "memory");
    word_set(2, (unsigned)e0); word_set(3, (unsigned)(e0 >> 32));
  }

  bf16x8 xf[32];
  constexpr int NB1 = NJ ? 1 : 2;
  f32x16 acc[3];
  f32x4 cs[3];
#pragma unroll
  for (int b = 0; b < 3; ++b) { cs[b] = f32x4{0.f, 0.f, 0.f, 0.f};
#pragma unroll
    for (int i = 0; i < 16; ++i) acc[b][i] = 0.f; }
  bf16x8 wf[3][NB1], wg[2][3];
  auto ldx = [&](int t) { xf[t] = lds_b128((unsigned)((t >> 2) % 5) * SLOT1 + xa[t & 3]); };
  auto ld1a = [&](int t, int b) {
    const unsigned a = (unsigned)((t >> 2) % 5) * SLOT1 + wa + ro[t & 3] + (NJ ? 2 : b) * 64u; wf[t % 3][b] = cat(lds_tr(a), lds_tr(a + 768u));
  };
  auto ld1b = [&](int t, int b) {
    const int u = NJ * 3 + b;
    const unsigned a = (unsigned)((8 + (t >> 2)) % 5) * SLOT1 + ((u & 1) ? wbO : wbE) + ro[t & 3] + u * 128u; wg[t & 1][b] = cat(lds_tr(a), lds_tr(a + 768u));
  };
  auto cs_put = [&](int nb, int b0) {
#pragma unroll
    for (int b = 0; b < 3; ++b) if (b < nb && lane < 16) { *(LAS float*)(lds + L_CSP + (((b0 + b) * 32 + lane) * 4 + mi) * 4) = cs[b][0]; *(LAS float*)(lds + L_CSP + (((b0 + b) * 32 + 16 + lane) * 4 + mi) * 4) = cs[b][1]; }
  };
  auto cs_get = [&](int nb, int b0) {
#pragma unroll
    for (int b = 0; b < 3; ++b) if (b < nb) { const f32x4 c4 = lds_f4(L_CSP + ((b0 + b) * 32 + r) * 16);
      *(LAS float*)(lds + L_CSW + w * 384 + b * 128 + r * 4) = (c4[0] + c4[1]) + (c4[2] + c4[3]); }
  };
  wait_vm<(L::NPRO - 1) * NA + L::NTW>();
  bar();
  e0 = (u64)word_get(2) | ((u64)word_get(3) << 32);
  epoch = e0 + 1ull;
  tg.a = (unsigned)epoch; tg.b = ((unsigned)(e0 >> 32) ^ 0x5bd1e995u) + tg.a;
  STAMP(2); CSTAMP(23);
  ldx(0); ldx(1);
#pragma unroll
  for (int b = 0; b < NB1; ++b) { ld1a(0, b); ld1a(1, b); }
  u32x4 sv;
  float rmean = 0.f, rrstd = 0.f;
  const unsigned sv_off = WS_RS + (unsigned)(128 * rb + 32 * mi) * 16u;
  static_for<0, 32>([&](auto tc) {
    constexpr int t = decltype(tc)::value, s = t >> 2, p = t & 3;
    if constexpr (p == 2) {
      if (s == 7) cs_put(NB1, NJ ? 2 : 0);
      wait_vm<SC.wait_stage(t, s + 1)>();
      if (s == 7) bar(); else bar_raw();
      STAMP2(24 + s);
      if (s == 7) cs_get(NB1, NJ ? 2 : 0);
      if (s == 1) CSTAMP(60); if (s == 4) CSTAMP(61); if (s == 7) CSTAMP(62);
    }
    if (NJ) op_at(tc, IC<0>{});
    static_for<0, NB1>([&](auto bc) {
      constexpr int b = decltype(bc)::value;
      acc[b] = mfma(wf[t % 3][b], xf[t], acc[b]);
      if (t < 30) { if (b == 0) ldx(t + 2); ld1a(t + 2, b); }
      if (t == 31) { ld1b(0, b); if (b == NB1 - 1) { for (int bb = NB1; bb < 3; ++bb) ld1b(0, bb); } }
      if (p == (NJ ? 1 : b)) cs[b] = mfma16(sel, wf[t % 3][b], cs[b]);
      if (b == 0) {
        if (t == 2 + NJ) {
          tie(xrow);
          float e[8];
#pragma unroll
          for (int i = 0; i < 4; ++i) { e[2 * i] = bf_lo(xrow[i]); e[2 * i + 1] = bf_hi(xrow[i]); }
          rmean = wave_sum(((e[0] + e[1]) + (e[2] + e[3])) + ((e[4] + e[5]) + (e[6] + e[7]))) * (1.f / 512.f);
          asm volatile("" : "+v"(rmean));
        }
        if (t == 4) {
          float q = 0.f;
#pragma unroll
          for (int i = 0; i < 4; ++i) { const float d0 = bf_lo(xrow[i]) - rmean, d1 = bf_hi(xrow[i]) - rmean; q = fmaf(d0, d0, q); q = fmaf(d1, d1, q); }
          rrstd = __builtin_amdgcn_rsqf(wave_sum(q) * (1.f / 512.f) + 1e-5f);
          asm volatile("" : "+v"(rrstd));
        }
      }
      SB0();
      if (b == 0) op_at(tc, IC<NJ ? 1 : 0>{});
    });
    if (!NJ) op_at(tc, IC<1>{});
    if (t == 22) sv = ld16c_asm(rs_ws, (unsigned)r * 16u, sv_off);
    if (t == 5 && lane == 0) st16c(u32x4{__float_as_uint(rmean), tg.a, __float_as_uint(rrstd), tg.b}, rs_ws, 0u, WS_RS + myrow * 16u);
    SB0();
  });
  STAMP(3); CSTAMP(22);
  { constexpr int after = SC.before[32] - L::NST - SC.before[23]; wait_vm_tie<after>(sv); }
  NOTE(53, __all(sv[1] == tg.a && sv[3] == tg.b) ? 1 : 2);
  for (unsigned spins = 0; !__all(sv[1] == tg.a && sv[3] == tg.b) && spins < (1u << 20); ++spins) {
    __builtin_amdgcn_s_sleep(1);
    sv = ld16c_asm(rs_ws, (unsigned)r * 16u, sv_off); wait_vm_tie<0>(sv);
  }
  const float mean = __uint_as_float(sv[0]), rstd = __uint_as_float(sv[2]);
  const int sb = rb * 4 + mi;
#pragma unroll
  for (int b = 0; b < NB1; ++b) {
    const int blk = 3 * g + (NJ ? 2 : b), which = blk >> 4, hp = blk & 15;
    const float sc = which == 0 ? rstd * (0.25f * 1.4426950408889634f) : rstd;
    float v[16];
#pragma unroll
    for (int i = 0; i < 4; ++i) { const f32x4 c4 = lds_f4(L_CSW + w * 384 + b * 128 + (8 * i + 4 * h) * 4);
#pragma unroll
      for (int e = 0; e < 4; ++e) v[4 * i + e] = (acc[b][4 * i + e] - mean * c4[e]) * sc; }
    const unsigned vo = which == 2 ? (unsigned)(h * 512 + ((r ^ (4 * h)) * 16)) : (unsigned)(lane * 16);
#pragma unroll
    for (int hd = 0; hd < 2; ++hd) {
      const u32x4 o = {pk(v[8 * hd], v[8 * hd + 1]), pk(v[8 * hd + 2], v[8 * hd + 3]), pk(v[8 * hd + 4], v[8 * hd + 5]), pk(v[8 * hd + 6], v[8 * hd + 7])};
      st16c(o, rs_ws, vo, WS_QKV + (unsigned)((which * 32 + 2 * hp + hd) * 64 + sb) * 1024u);
    }
  }
  STAMP(4); CSTAMP(18);
#pragma unroll
  for (int b = 0; b < 3; ++b) { cs[b] = f32x4{0.f, 0.f, 0.f, 0.f};
#pragma unroll
    for (int i = 0; i < 16; ++i) acc[b][i] = 0.f; }
  bool act;
  const unsigned qv = qkv_flag_voff(rb2, hp2, lane, act);
  u32x4 fq;
  SB0();
  static_for<32, 64>([&](auto tc) {
    constexpr int Tg = decltype(tc)::value, t = Tg - 32, s = Tg >> 2, p = t & 3;
    if constexpr (p == 3) {
      if (s == 15) cs_put(3, NJ * 3);
      if constexpr (s == 9) wait_vm<(SC.before[Tg] - SC.before[32] < SC.wait_stage(Tg, 10) ? SC.before[Tg] - SC.before[32] : SC.wait_stage(Tg, 10))>();
      else if constexpr (s == 14) { wait_vm<0>(); if (w == 0) { tie(fq); word_set(0, flags_ok(fq, act, tg) ? 1u : 0u); } }
      else if constexpr (s <= 13) wait_vm<SC.wait_stage(Tg, s + 1)>();
      if (s >= 14) bar(); else bar_raw();
      STAMP2(24 + s);
      if (s == 8) CSTAMP(56); if (s == 11) CSTAMP(57); if (s == 13) CSTAMP(58); if (s == 15) CSTAMP(59);
      if (s == 14) early = word_get(0) != 0u;
      if (s == 15) cs_get(3, NJ * 3);
    }
    if (NJ) op_at(tc, IC<0>{});
    static_for<0, 3>([&](auto bc) {
      constexpr int b = decltype(bc)::value;
      acc[b] = mfma(wg[t & 1][b], xf[t], acc[b]);
      if (t < 31) ld1b(t + 1, b);
      if (p == ((b + 2 * NJ) & 3) || (s == 15 && p == 2 && ((b + 2 * NJ) & 3) == 3)) { if (!(s == 15 && p == 3)) cs[b] = mfma16(sel, wg[t & 1][b], cs[b]); }
      SB0();
    });
    if (!NJ) op_at(tc, IC<0>{});
    op_at(tc, IC<1>{});
    if (Tg == 39) flag_store(rs_ws, WS_FQKV + (unsigned)((rb * 16 + g) * 64 + w * 8), tg, lane);
    if (Tg == 55 && w == 0) fq = ld16c_asm(rs_ws, qv, 0u);
    SB0();
  });
  STAMP(5); CSTAMP(19);
  const float r2 = rstd * rstd;
#pragma unroll
  for (int b = 0; b < 3; ++b) {
    float v[16];
#pragma unroll
    for (int i = 0; i < 4; ++i) { const f32x4 c4 = lds_f4(L_CSW + w * 384 + b * 128 + (8 * i + 4 * h) * 4);
#pragma unroll
      for (int e = 0; e < 4; ++e) v[4 * i + e] = acc[b][4 * i + e] - mean * c4[e]; }
    float hv[8];
#pragma unroll
    for (int i = 0; i < 8; ++i) hv[i] = fmaxf(v[i], 0.f) * v[8 + i] * r2;
    const u32x4 o = {pk(hv[0], hv[1]), pk(hv[2], hv[3]), pk(hv[4], hv[5]), pk(hv[6], hv[7])};
    if (NJ * 3 + b < nu) st16c(o, rs_ws, (unsigned)lane * 16u, WS_H + (unsigned)(sb * NU + u0 + NJ * 3 + b) * 1024u);
  }
  STAMP(6);
  if (NJ == 0 || nu == 6) wait_vm<3>(); else wait_vm<2>();
  return early;
}

template <bool FULL, class F1, class FM, class F2>
DI u32x4 attn_task(int j0, int qb, int hd, int lane, F1 side1, FM mid, F2 side2) {
  const int q = lane & 31, hh = lane >> 5, tq = q - 4 * hh;
  const bf16x8 qf = lds_b128(LA_Q + (unsigned)(hd * 4 + qb) * 1024u + lane * 16u);
  const unsigned kbase = LA_K + (unsigned)(hd * 12 + qb) * 1024u + lane * 16u;
  const float NEG = -__builtin_inff();
  bf16x8 kf[9];
#pragma unroll
  for (int j = 0; j < 9; ++j) if (FULL || j >= j0) kf[j] = lds_b128(kbase + j * 1024u);
  f32x16 c;
#pragma unroll
  for (int i = 0; i < 16; ++i) c[i] = 0.f;
  auto on = [&](int j) { return FULL || j >= j0; };
  float m0 = NEG, m1 = NEG;
  f32x16 s[2];
  {
    const f32x16 s8 = mfma(kf[8], qf, c);
    f32x16 s0;
    if (FULL) s0 = mfma(kf[0], qf, c);
    if (on(1)) s[1] = mfma(kf[1], qf, c);
    SB0();
#pragma unroll
    for (int i = 0; i < 16; i += 2) {
      const int c0 = (i & 3) + 8 * (i >> 2);
      const float a = c0 <= tq ? s8[i] : (FULL ? s0[i] : NEG), b = c0 + 1 <= tq ? s8[i + 1] : (FULL ? s0[i + 1] : NEG);
      if (i & 2) m1 = fmaxf(fmaxf(m1, a), b); else m0 = fmaxf(fmaxf(m0, a), b);
    }
    side1(0);
    SB0();
  }
#pragma unroll
  for (int j = 1; j < 8; ++j) {
    if (j < 7 && on(j + 1)) s[(j + 1) & 1] = mfma(kf[j + 1], qf, c);
    SB0();
    if (on(j)) {
#pragma unroll
      for (int i = 0; i < 8; i += 2) { m0 = fmaxf(fmaxf(m0, s[j & 1][i]), s[j & 1][i + 1]); m1 = fmaxf(fmaxf(m1, s[j & 1][8 + i]), s[j & 1][9 + i]); }
    }
    side1(j);
    SB0();
  }
  const float m = xhalf_max(fmaxf(m0, m1));
  mid();
#pragma unroll
  for (int i = 0; i < 16; ++i) c[i] = -m;
  const int grp = (lane >> 4) & 1, q4 = (lane & 15) >> 2, p4 = lane & 3;
  const unsigned vb = grp ? L_ONES + 128u : LA_V + (unsigned)(hd * 12 + qb) * 1024u + (unsigned)((p4 & 1) * 512 + (((4 * hh + q4) ^ (4 * (p4 & 1))) * 16) + (p4 >> 1) * 8);
  bf16x8 vf[2][2];
  auto ldv = [&](int j, int slot) {
#pragma unroll
    for (int u = 0; u < 2; ++u) vf[slot][u] = cat(lds_tr(vb + j * 1024u + u * 256u), lds_tr(vb + j * 1024u + u * 256u + 128u));
  };
  f32x16 acc;
#pragma unroll
  for (int i = 0; i < 16; ++i) acc[i] = 0.f;
  auto pv = [&](int slot, const unsigned (&pp)[8]) {
#pragma unroll
    for (int u = 0; u < 2; ++u) acc = mfma(vf[slot][u], __builtin_bit_cast(bf16x8, u32x4{pp[4 * u], pp[4 * u + 1], pp[4 * u + 2], pp[4 * u + 3]}), acc);
  };
  f32x16 s8, s0;
  if (on(1)) { s[1] = mfma(kf[1], qf, c); ldv(1, 1); }
#pragma unroll
  for (int j = 1; j < 8; ++j) {
    if (j < 7) { if (on(j + 1)) { s[(j + 1) & 1] = mfma(kf[j + 1], qf, c); ldv(j + 1, (j + 1) & 1); } }
    else { s8 = mfma(kf[8], qf, c); if (FULL) s0 = mfma(kf[0], qf, c); ldv(8, 0); }
    SB0();
    if (on(j)) {
      unsigned pp[8];
#pragma unroll
      for (int i = 0; i < 8; ++i) pp[i] = pk(__builtin_amdgcn_exp2f(s[j & 1][2 * i]), __builtin_amdgcn_exp2f(s[j & 1][2 * i + 1]));
      pv(j & 1, pp);
    }
    side2(j);
    SB0();
  }
  {
    if (FULL) ldv(0, 1);
    unsigned p8[8], p0[8];
#pragma unroll
    for (int i = 0; i < 8; ++i) {
      const int c0 = ((2 * i) & 3) + 8 * (i >> 1);
      const bool va = c0 <= tq, vb_ = c0 + 1 <= tq;
      const float a = va ? s8[2 * i] : (FULL ? s0[2 * i] : NEG), b = vb_ ? s8[2 * i + 1] : (FULL ? s0[2 * i + 1] : NEG);
      const unsigned pp = pk(__builtin_amdgcn_exp2f(a), __builtin_amdgcn_exp2f(b));
      const unsigned mk = (va ? 0xffffu : 0u) | (vb_ ? 0xffff0000u : 0u);
      p8[i] = pp & mk; p0[i] = pp & ~mk;
    }
    pv(0, p8);
    if (FULL) pv(1, p0);
  }
  const float inv = __builtin_amdgcn_rcpf(acc[8]);
  return u32x4{pk(acc[0] * inv, acc[1] * inv), pk(acc[2] * inv, acc[3] * inv), pk(acc[4] * inv, acc[5] * inv), pk(acc[6] * inv, acc[7] * inv)};
}

#define H_FLAG_AT 2
constexpr int DW = 10, DA = 8, NK3 = 30, NDN = 22, OCHK = NDN - (DA - 1), OPOLL = OCHK - 5, XLD = 20;
struct Sched3 {
  int before[NK3 + 1] = {}, lastW[NK3] = {}, lastA[NK3] = {};
  constexpr Sched3() {
    int c = 0;
    for (int i = 0; i < DW - 1; ++i) { c += 2; lastW[i] = c; }
    for (int i = 0; i < DA - 1; ++i) { c += 2; lastA[i] = c; }
    for (int i = 0; i < NK3; ++i) {
      before[i] = c;
      if (i + DW - 1 < NK3) { c += 2; lastW[i + DW - 1] = c; }
      if (i + DA - 1 < NK3) { c += 2; lastA[i + DA - 1] = c; }
      if (i == XLD) c += 4;
    }
    before[NK3] = c;
  }
  constexpr int wait(int i) const { const int w = i + 1 < NK3 ? lastW[i + 1] : 0; return before[i] - (lastA[i] > w ? lastA[i] : w); }
};
DI unsigned l3w(int kq) { return kq < 3 ? 86016u + (unsigned)kq * (DW * 2048u) : 0u; }
constexpr unsigned L3_RED = 28672;

DI void phase23(const Ptrs& P, bool early, int rb1, int g1, u64 epoch, int rb, int hp, int rb64, int cb, int rot, int w, int lane, Tag tg, Stamps& T) {
  const rsrc_t rs_ws = mk_rsrc(P.ws, WS_BYTES);
  for (unsigned i = threadIdx.x; i < ONES_BYTES / 16; i += 512) *(LAS u32x4*)(lds + L_ONES + i * 16) = u32x4{ONES2, ONES2, ONES2, ONES2};
  NOTE(63, early ? 1 : 2);
  if (!early) {
    bool act; const unsigned qv = qkv_flag_voff(rb, hp, lane, act);
    if (w == 0) flags_spin(rs_ws, qv, act, tg);
    bar();
    const AttnSrc asrc = attn_src(rb, hp, w);
#pragma unroll
    for (int i = 0; i < 7; ++i) attn_issue(rs_ws, asrc, w, lane, i, true);
    wait_vm<0>();
  }
  bar();
  STAMP(9);
  const unsigned fh_off = WS_FH + (unsigned)((rb1 * 16 + g1) * 64 + w * 8);
  const int h = lane >> 5, nsub = (lane >> 4) & 1, q4 = (lane & 15) >> 2, p4 = lane & 3;
  f32x16 acc[4];
  u32x2 xr[4];
  const int kq = w - 4;
  const bool keeper = blockIdx.x == 0 && w == 4;
  u32x4 fe[4];
  const rsrc_t rs_d = mk_rsrc(P.wd, FF * DM * 2), rs_o = mk_rsrc(P.wo, DM * DM * 2);
  const int ulo = kq < 3 ? 22 * kq : 65, nun = kq < 2 ? 22 : 21;
  unsigned vw[2];
#pragma unroll
  for (int j = 0; j < 2; ++j) vw[j] = (unsigned)(8 * j + (lane >> 3)) * 1024u + (unsigned)(((lane & 7) ^ (((lane >> 4) & 1) << 2)) * 16);
  const unsigned ring = __builtin_amdgcn_readfirstlane(l3w(kq)), vo = (unsigned)lane * 16u;
  auto unit = [&](int i) { const int r = i + rot; return i < NDN ? (i < nun ? ulo + (r >= nun ? r - nun : r) : -1) : 8 * kq + ((i - NDN + rot) & 7); };
  auto reqW = [&](int i) {
    const int u = unit(i);
#pragma unroll
    for (int j = 0; j < 2; ++j) dma16(ring + (unsigned)(i % DW) * 2048u + j * 1024u, u < 0 ? 0x40000000u : vw[j], i < NDN ? rs_d : rs_o, (unsigned)cb * 128u + (unsigned)(u < 0 ? 0 : u) * 16384u);
  };
  const unsigned hv = WS_FH + (unsigned)((rb64 >> 1) * 1024 + lane * 16), ov = WS_FO + (unsigned)((rb64 >> 1) * 1024 + lane * 16);
  const int pg = lane >> 2;
  const bool pair_ok = ((lane & 3) & 1) == (rb64 & 1);
  const bool h_act = pair_ok && (((pg + 1) * NU) >> 4) > ulo && ((pg * NU) >> 4) < ulo + nun, o_act = pair_ok && (pg >> 2) == kq;
  u32x4 fh;
  if (w >= 4) __builtin_amdgcn_s_setprio(3);
  {
    const int qb = w & 3, hd = w >> 2, sb = 4 * rb + qb;
    auto side1 = [&](int j) {
      if (w >= 4 && j >= 1) reqW(j - 1);
    };
    auto mid = [&]() { STAMP(10); };
    auto side2 = [&](int j) {
      if (w >= 4 && j <= 2) reqW(6 + j);
      if (j == H_FLAG_AT) { if (w >= 4) wait_vm<18>(); else wait_vm<0>(); flag_store(rs_ws, fh_off, tg, lane); }
      if (w >= 4 && j == 7) fh = ld16c_asm(rs_ws, hv, 0u);
    };
    u32x4 o;
    if (sb >= 8) o = attn_task<true>(0, qb, hd, lane, side1, mid, side2); else o = attn_task<false>(8 - sb, qb, hd, lane, side1, mid, side2);
    st16c(o, rs_ws, (unsigned)lane * 16u, WS_O + (unsigned)(sb * 32 + 2 * hp + hd) * 1024u);
    STAMP(11);
  }
  const unsigned fo_off = WS_FO + (unsigned)((rb * 16 + hp) * 64 + w * 8);
  if (w < 4) {
    wait_vm<0>();
    flag_store(rs_ws, fo_off, tg, lane);
    STAMP(12);
  } else {
    wait_vm_tie<1>(fh);
    NOTE(55, flags_ok(fh, h_act, tg) ? 1 : 2);
    if (!flags_ok(fh, h_act, tg)) flags_spin(rs_ws, hv, h_act, tg);
    STAMP(13);
    u32x4 A[DA][2];
    auto reqA = [&](int i) {
      const int u = unit(i);
#pragma unroll
      for (int mb = 0; mb < 2; ++mb)
        A[i % DA][mb] = ld16c_asm(rs_ws, u < 0 ? 0x40000000u : vo, i < NDN ? WS_H + (unsigned)((2 * rb64 + mb) * NU + (u < 0 ? 0 : u)) * 1024u : WS_O + (unsigned)((2 * rb64 + mb) * 32 + u) * 1024u);
    };
#pragma unroll
    for (int i = 0; i < DA - 1; ++i) reqA(i);
#pragma unroll
    for (int b = 0; b < 4; ++b)
#pragma unroll
      for (int i = 0; i < 16; ++i) acc[b][i] = 0.f;
    unsigned wa[2];
#pragma unroll
    for (int nb = 0; nb < 2; ++nb) wa[nb] = ring + (unsigned)h * 512u + q4 * 128u + (unsigned)((nb ^ (q4 >> 1)) * 64) + nsub * 32u + p4 * 8u;
    const int row = 64 * rb64 + 32 * (kq >> 1) + (lane & 31), col0 = 64 * cb + 32 * (kq & 1) + 4 * h;
    const unsigned short* xp = (const unsigned short*)P.x + (size_t)row * DM + col0;
    static constexpr Sched3 S3{};
    u32x4 fo;
    bf16x8 wf[2][2];
    auto ldw = [&](int i) {
#pragma unroll
      for (int nb = 0; nb < 2; ++nb) { const unsigned a = wa[nb] + (unsigned)(i % DW) * 2048u; wf[i & 1][nb] = cat(lds_tr(a), lds_tr(a + 1024u)); }
    };
    ldw(0);
    static_for<0, NK3>([&](auto ic) {
      constexpr int i = decltype(ic)::value;
      wait_vm<S3.wait(i)>();
      tie(A[i % DA][0]); tie(A[i % DA][1]);
      if (i == 0) STAMP(14);
      if constexpr (i == OCHK) {
        wait_vm_tie<S3.before[i] - S3.before[OPOLL + 1]>(fo);
        NOTE(54, flags_ok(fo, o_act, tg) ? 1 : 2);
        if (!flags_ok(fo, o_act, tg)) flags_spin(rs_ws, ov, o_act, tg);
      }
      SB0();
      acc[0] = mfma(wf[i & 1][0], __builtin_bit_cast(bf16x8, A[i % DA][0]), acc[0]);
      if constexpr (i + 1 < NK3) ldw(i + 1);
      SB0();
      acc[1] = mfma(wf[i & 1][1], __builtin_bit_cast(bf16x8, A[i % DA][0]), acc[1]);
      SB0();
      if constexpr (i + DW - 1 < NK3) reqW(i + DW - 1);
      acc[2] = mfma(wf[i & 1][0], __builtin_bit_cast(bf16x8, A[i % DA][1]), acc[2]);
      SB0();
      acc[3] = mfma(wf[i & 1][1], __builtin_bit_cast(bf16x8, A[i % DA][1]), acc[3]);
      SB0();
      if constexpr (i + DA - 1 < NK3) reqA(i + DA - 1);
      if (i == 1) flag_store(rs_ws, fo_off, tg, lane);
      if (i == OPOLL) fo = ld16c_asm(rs_ws, ov, 0u);
      if (i == XLD) {
#pragma unroll
        for (int q = 0; q < 4; ++q) xr[q] = *(const GAS u32x2*)(xp + 8 * q);
      }
      if (i == 22 && keeper) {
#pragma unroll
        for (int q = 0; q < 4; ++q) fe[q] = ld16c_asm(rs_ws, WS_FQKV + (unsigned)(q * 64 + lane) * 64u, 0u);
      }
      SB0();
    });
    STAMP(15);
  }
  bar();
  if (w >= 4) {
#pragma unroll
    for (int b = 0; b < 4; ++b) {
      if (b != kq) {
        const unsigned base = L3_RED + (unsigned)(b * 3 + (kq - b - 1 + 4) % 4) * 4096u + lane * 16u;
#pragma unroll
        for (int q = 0; q < 4; ++q) *(LAS f32x4*)(lds + base + q * 1024u) = f32x4{acc[b][4 * q], acc[b][4 * q + 1], acc[b][4 * q + 2], acc[b][4 * q + 3]};
      }
    }
  }
  bar();
  if (w >= 4) {
    f32x16 r = kq == 0 ? acc[0] : (kq == 1 ? acc[1] : (kq == 2 ? acc[2] : acc[3]));
#pragma unroll
    for (int s = 0; s < 3; ++s)
#pragma unroll
      for (int q = 0; q < 4; ++q) { const f32x4 v = lds_f4(L3_RED + (unsigned)(kq * 3 + s) * 4096u + lane * 16u + q * 1024u);
#pragma unroll
        for (int e = 0; e < 4; ++e) r[4 * q + e] += v[e]; }
    const int row = 64 * rb64 + 32 * (kq >> 1) + (lane & 31), col0 = 64 * cb + 32 * (kq & 1) + 4 * h;
    unsigned short* op = (unsigned short*)P.out + (size_t)row * DM + col0;
#pragma unroll
    for (int q = 0; q < 4; ++q)
      *(GAS u32x2*)(op + 8 * q) = u32x2{pk(r[4 * q] + bf_lo(xr[q][0]), r[4 * q + 1] + bf_hi(xr[q][0])), pk(r[4 * q + 2] + bf_lo(xr[q][1]), r[4 * q + 3] + bf_hi(xr[q][1]))};
    if (keeper) {
#pragma unroll
      for (int q = 0; q < 4; ++q) { tie(fe[q]); if (!flags_ok(fe[q], true, tg)) flags_spin(rs_ws, WS_FQKV + (unsigned)(q * 64 + lane) * 64u, true, tg); }
      if (lane == 0) __hip_atomic_store((GAS u64*)(P.ws + WS_EPOCH), epoch, __ATOMIC_RELAXED, __HIP_MEMORY_SCOPE_AGENT);
    }
  }
}

__global__ __launch_bounds__(512, 2) void mk_kernel(const void* x, const void* wqkv, const void* wo, const void* wg, const void* wu, const void* wd, void* out, char* ws) {
  u64 pc0 = 0;
  asm volatile("mk_code_begin:\n\ts_getpc_b64 %0" : "=s"(pc0));
  {
    unsigned len;
    asm volatile("s_mov_b32 %0, mk_code_end-mk_code_begin" : "=s"(len));
    const unsigned wv = __builtin_amdgcn_readfirstlane(threadIdx.x >> 6);
    const unsigned per = ((len >> 5) + 1023u) & ~1023u;
    const unsigned o = (blockIdx.x >> 3) * per + wv * 1024u;
    dma16(L_DUMMY + wv * 1024u, (threadIdx.x & 63u) * 16u, mk_rsrc((const void*)(pc0 & ~255ull), len & ~15u), wv * 1024u < per ? o : 0x40000000u);
  }
  const Ptrs P{x, wqkv, wo, wg, wu, wd, out, ws, pc0};
  Stamps T;
  STAMP(0); CSTAMP(20);
  __builtin_amdgcn_s_setprio(1);
  const int b = blockIdx.x, xcd = b & 7, idx = b >> 3;
  const int w = __builtin_amdgcn_readfirstlane(threadIdx.x >> 6), lane = threadIdx.x & 63;
  Tag tg; u64 epoch;
  const int rb = 2 * (idx >> 2) + (xcd >> 2), g = (xcd & 3) * 4 + (idx & 3), rot1 = ((idx >> 2) + 2 * (idx & 3)) & 7;
  bool early;
  if (w < 4) early = phase1<0>(P, rb, g, rot1, w, lane, rb, g, tg, epoch, T); else early = phase1<1>(P, rb, g, rot1, w - 4, lane, rb, g, tg, epoch, T);
  const int rot3 = ((idx & 7) * 2 + 5 * (idx >> 3)) % 21;
  phase23(P, early, rb, g, epoch, rb, g, xcd * 4 + (idx >> 3), idx & 7, rot3, w, lane, tg, T);
  asm volatile("mk_code_end:");
}

}

extern "C" void kernel_launch(void* const* d_in, const int* in_sizes, int n_in, void* d_out, int out_size, void* d_ws, size_t ws_size, hipStream_t stream) {
  const void *x = d_in[0], *wqkv = d_in[1], *wo = d_in[2], *wg = d_in[4], *wu = d_in[5], *wd = d_in[6];
  mk_kernel<<<256, 512, 0, stream>>>(x, wqkv, wo, wg, wu, wd, d_out, (char*)d_ws);
}
```

```cpp
#include <hip/hip_runtime.h>
#include <hip/hip_bf16.h>
#include <stdint.h>

namespace {

typedef short bf16x8 __attribute__((ext_vector_type(8)));
typedef short s16x4 __attribute__((ext_vector_type(4)));
typedef float f32x16 __attribute__((ext_vector_type(16)));
typedef float f32x4 __attribute__((ext_vector_type(4)));
typedef float f32x2 __attribute__((ext_vector_type(2)));
typedef unsigned u32x4 __attribute__((ext_vector_type(4)));
typedef unsigned u32x2 __attribute__((ext_vector_type(2)));
typedef __bf16 bf16x2_t __attribute__((ext_vector_type(2)));
typedef short v4i16_t __attribute__((ext_vector_type(4)));
typedef __amdgpu_buffer_rsrc_t rsrc_t;
#define LAS __attribute__((address_space(3)))
#define GAS __attribute__((address_space(1)))
#define DI __device__ __forceinline__
#define SB0() __builtin_amdgcn_sched_barrier(0)
typedef unsigned long long u64;

constexpr int SEQ = 2048, DM = 512, FF = 1376, NU = 86;
constexpr unsigned WS_EPOCH = 0;
constexpr unsigned WS_FQKV = 16384, WS_FH = 32768, WS_FO = 49152;
constexpr unsigned WS_RS = 65536;
constexpr unsigned WS_QKV = 1u << 20;
constexpr unsigned WS_H = 7u << 20;
constexpr unsigned WS_O = 13u << 20;
constexpr unsigned WS_BYTES = 16u << 20;
constexpr unsigned SLOT1 = 28672;
constexpr unsigned L_CSP = 143360;
constexpr unsigned L_CSW = 146432;
constexpr unsigned L_ONES = 152576;
constexpr unsigned ONES_BYTES = 9984;
constexpr unsigned L_WORD = 163584;
constexpr unsigned LDS_BYTES = 163840;
constexpr unsigned LA_Q = 28672, LA_K = LA_Q + 8192, LA_V = LA_K + 24576;

__shared__ __attribute__((aligned(1024))) char lds[LDS_BYTES];

DI unsigned pk(float a, float b) { f32x2 v = {a, b}; bf16x2_t r = __builtin_convertvector(v, bf16x2_t); return __builtin_bit_cast(unsigned, r); }
DI float dot2(unsigned a, unsigned b, float c) { return __builtin_amdgcn_fdot2_f32_bf16(__builtin_bit_cast(bf16x2_t, a), __builtin_bit_cast(bf16x2_t, b), c, false); }
DI rsrc_t mk_rsrc(const void* p, unsigned bytes) { return __builtin_amdgcn_make_buffer_rsrc((void*)p, 0, bytes, 0x00020000); }
#define LD_POL ""
#define ST_AUX 16
DI void dma16(unsigned ldsaddr, unsigned voff, rsrc_t rs, unsigned soff) {
  asm volatile("s_mov_b32 m0, %0\n\ts_nop 0\n\tbuffer_load_dwordx4 %1, %2, %3 offen" LD_POL " lds" ::"s"(ldsaddr), "v"(voff), "s"(rs), "s"(soff) : "memory");
}
DI void dma16c(unsigned ldsaddr, unsigned voff, rsrc_t rs, unsigned soff) {
  asm volatile("s_mov_b32 m0, %0\n\ts_nop 0\n\tbuffer_load_dwordx4 %1, %2, %3 offen sc1 lds" ::"s"(ldsaddr), "v"(voff), "s"(rs), "s"(soff) : "memory");
}
DI void pf_lines(unsigned ldsaddr, unsigned voff, rsrc_t rs) {
  asm volatile("s_mov_b32 m0, %0\n\ts_nop 0\n\tbuffer_load_dword %1, %2, 0 offen lds" ::"s"(ldsaddr), "v"(voff), "s"(rs) : "memory");
}
template <int N> DI void wait_vm() { asm volatile("s_waitcnt vmcnt(%0)" ::"n"(N) : "memory"); }
template <int N> DI void wait_vm_tie(u32x4& v) { asm volatile("s_waitcnt vmcnt(%1)" : "+v"(v) : "n"(N) : "memory"); }
template <int V> struct IC { static constexpr int value = V; };
template <int I, int N, class F> DI void static_for(F&& f) { if constexpr (I < N) { f(IC<I>{}); static_for<I + 1, N>(f); } }
DI void bar_raw() { asm volatile("s_barrier" ::: "memory"); }
DI void tie(u32x4& v) { asm volatile("" : "+v"(v) :: "memory"); }
DI void bar() { asm volatile("s_waitcnt lgkmcnt(0)\n\ts_barrier" ::: "memory"); }
DI f32x16 mfma(bf16x8 a, bf16x8 b, f32x16 c) { return __builtin_amdgcn_mfma_f32_32x32x16_bf16(a, b, c, 0, 0, 0); }
DI bf16x8 lds_b128(unsigned a) { return *(const LAS bf16x8*)(lds + a); }
#define MK_REAL 1
DI s16x4 lds_tr(unsigned a) { return __builtin_amdgcn_ds_read_tr16_b64_v4i16((LAS v4i16_t*)(lds + a)); }
DI f32x4 mfma16(bf16x8 a, bf16x8 b, f32x4 c) { return __builtin_amdgcn_mfma_f32_16x16x32_bf16(a, b, c, 0, 0, 0); }
DI f32x4 lds_f4(unsigned a) { return *(const LAS f32x4*)(lds + a); }
DI bf16x8 cat(s16x4 lo, s16x4 hi) { return __builtin_shufflevector(lo, hi, 0, 1, 2, 3, 4, 5, 6, 7); }
DI float xhalf_sum(float v) { auto r = __builtin_amdgcn_permlane32_swap(__float_as_uint(v), __float_as_uint(v), false, false); return __uint_as_float(r[0]) + __uint_as_float(r[1]); }
DI float xhalf_max(float v) { auto r = __builtin_amdgcn_permlane32_swap(__float_as_uint(v), __float_as_uint(v), false, false); return fmaxf(__uint_as_float(r[0]), __uint_as_float(r[1])); }
DI void st16c(u32x4 v, rsrc_t rs, unsigned voff, unsigned soff) { __builtin_amdgcn_raw_buffer_store_b128(v, rs, voff + soff, 0, ST_AUX); }
DI void st8c(u32x2 v, rsrc_t rs, unsigned voff, unsigned soff) { __builtin_amdgcn_raw_buffer_store_b64(v, rs, voff, soff, 16); }
constexpr unsigned ONES2 = 0x3F803F80u;
constexpr unsigned SPIN_CAP = 1u << 19;
template <int CTRL> DI float dpp_add(float v) { return v + __int_as_float(__builtin_amdgcn_update_dpp(0, __float_as_int(v), CTRL, 0xf, 0xf, true)); }
DI float wave_sum(float v) {
  v = dpp_add<0xB1>(v); v = dpp_add<0x4E>(v); v = dpp_add<0x141>(v); v = dpp_add<0x140>(v);
  auto a = __builtin_amdgcn_permlane16_swap(__float_as_uint(v), __float_as_uint(v), false, false);
  v = __uint_as_float(a[0]) + __uint_as_float(a[1]);
  return xhalf_sum(v);
}
DI float bf_lo(unsigned u) { return __uint_as_float(u << 16); }
DI float bf_hi(unsigned u) { return __uint_as_float(u & 0xffff0000u); }
DI u32x4 ld16_asm(rsrc_t rs, unsigned voff, unsigned soff) { u32x4 v; asm volatile("buffer_load_dwordx4 %0, %1, %2, %3 offen" : "=v"(v) : "v"(voff), "s"(rs), "s"(soff) : "memory"); return v; }
DI u32x4 ld16c_asm(rsrc_t rs, unsigned voff, unsigned soff) { u32x4 v; asm volatile("buffer_load_dwordx4 %0, %1, %2, %3 offen sc1" : "=v"(v) : "v"(voff), "s"(rs), "s"(soff) : "memory"); return v; }
#define STAMP2(i) do {} while (0)
struct Stamps {};
#define STAMP(i) do {} while (0)
#define CSTAMP(i) do {} while (0)
#define NOTE(i, v) do {} while (0)

struct Ptrs { const void *x, *wqkv, *wo, *wg, *wu, *wd; void* out; char* ws; u64 pc; };
struct Tag { unsigned a, b; };

DI void flag_store(rsrc_t rs_ws, unsigned off, Tag tg, int lane) { if (lane == 0) st8c(u32x2{tg.a, tg.b}, rs_ws, 0u, off); }
DI bool flags_ok(u32x4 v, bool act, Tag tg) { return __all(!act || (v[0] == tg.a && v[1] == tg.b && v[2] == tg.a && v[3] == tg.b)); }
DI void flags_spin(rsrc_t rs_ws, unsigned voff, bool act, Tag tg) {
  for (unsigned spins = 0; spins < SPIN_CAP; ++spins) {
    u32x4 v = ld16c_asm(rs_ws, voff, 0u); wait_vm_tie<0>(v);
    if (flags_ok(v, act, tg)) break;
    __builtin_amdgcn_s_sleep(1);
  }
}
DI void word_set(int i, unsigned v) { *(volatile LAS unsigned*)(lds + L_WORD + i * 4) = v; }
DI unsigned word_get(int i) { return __builtin_amdgcn_readfirstlane(*(volatile LAS unsigned*)(lds + L_WORD + i * 4)); }

struct AttnSrc { unsigned q, kv; int sb0; };
DI AttnSrc attn_src(int rb, int hp, int w) {
  AttnSrc a;
  a.q = __builtin_amdgcn_readfirstlane(WS_QKV + (unsigned)((2 * hp + (w >> 2)) * 64 + 4 * rb + (w & 3)) * 1024u);
  a.kv = __builtin_amdgcn_readfirstlane(WS_QKV + (unsigned)(((1 + (w >> 2)) * 32 + 2 * hp + ((w >> 1) & 1)) * 64) * 1024u);
  a.sb0 = __builtin_amdgcn_readfirstlane(4 * rb - 8 + 6 * (w & 1));
  return a;
}
DI void attn_issue(rsrc_t rs_ws, const AttnSrc& a, int w, int lane, int piece, bool go) {
  const unsigned vo = (unsigned)lane * 16u + (go ? 0u : 0x40000000u);
  if (piece == 0) { dma16c(LA_Q + (unsigned)w * 1024u, vo, rs_ws, a.q); return; }
  const int sbk = a.sb0 + piece - 1;
  dma16c(LA_K + (unsigned)((w >> 1) * 12 + 6 * (w & 1) + piece - 1) * 1024u, vo, rs_ws, a.kv + (unsigned)(sbk < 0 ? 0 : sbk) * 1024u);
}
DI unsigned qkv_flag_voff(int rb, int hp, int lane, bool& act) {
  const int c = lane >> 2, kind = c == 0 ? 0 : (c <= 3 ? 1 : 2), d = c == 0 ? 0 : (c - 1) % 3, rr = rb - d;
  act = lane < 28 && rr >= 0;
  return act ? WS_FQKV + (unsigned)((rr * 16 + (kind * 16 + hp) / 3) * 64 + (lane & 3) * 16) : 0u;
}

constexpr unsigned L_DUMMY = 152576;
#define TLBW_N 0
#define PFX_N 0
#define PF3_N 0
#define PF3_T 33
#define P0_T 4
#define P1_NPRO 3
#define ATT_B 14
#define P1_HOLD 12
constexpr int OP_NONE = -1, OP_PFW = 200, OP_ATT = 400;
template <int NJ> struct Sched1 {
  static constexpr int NA = NJ ? 4 : 3, NST = NJ ? 2 : 4;
  static constexpr int n(int sg) { return sg < 8 ? NA : 3; }
  static constexpr int TB(int s) { return s < 8 ? 4 * s + 2 : 4 * s + 3; }
  static constexpr int extras(int T) { return (T == 5 ? 1 : 0)   + (T == 22 ? 1 : 0)   + (T == 39 ? 1 : 0)  ; }
  static constexpr int NPRO = P1_NPRO;
  static constexpr int NTW = TLBW_N;
  static constexpr int NPX = PFX_N;
  static constexpr int PRO = 1 + NPX + NPRO * NA + 1 + NTW;
  static constexpr int HOLD = P1_HOLD;
  int op[64][2] = {}, before[65] = {}, last[16] = {};
  constexpr Sched1() {
    int sg = NPRO, j = 0, pf = 0, att = 0, c = PRO;
    last[0] = 1 + NPX + NA;
    for (int i = 1; i < NPRO; ++i) last[i] = 1 + NPX + NA + 1 + NTW + i * NA;
    for (int T = 0; T < 64; ++T) {
      before[T] = c;
      for (int k = 0; k < 2; ++k) {
        int o = OP_NONE;
        if (k == 0 || T < 14) {
          if (sg <= 15 && (sg < 5 || T >= TB(sg - 4)) && (sg < 8 || T >= 4 * sg - HOLD)) {   o = sg * 8 + j; if (++j == n(sg)) { last[sg] = c + 1; ++sg; j = 0; } }
          else if (pf == 0 && T >= P0_T) o = OP_PFW + pf++;
          else if (pf >= 1 && pf < 1 + PF3_N && T >= PF3_T) o = OP_PFW + pf++;
        }
        if (T >= 4 * ATT_B + 3 && att < 7 && (k == 0 || T >= 61)) o = OP_ATT + att++;
        op[T][k] = o;
        if (o != OP_NONE) ++c;
      }
      c += extras(T);
      if (T == 31) c += NST;
    }
    before[64] = c;
  }
  constexpr int wait_stage(int T, int sg) const { return before[T] - last[sg]; }
};

template <int NJ>
DI bool phase1(const Ptrs& P, int rb, int g, int rot, int mi, int lane, int rb2, int hp2, Tag& tg, u64& epoch, Stamps& T_) {
  Stamps& T = T_;
  typedef Sched1<NJ> L;
  static constexpr Sched1<NJ> SC{};
  const int w = NJ * 4 + mi;
  const rsrc_t rs_x = mk_rsrc(P.x, SEQ * DM * 2), rs_q = mk_rsrc(P.wqkv, DM * 1536 * 2);
  const rsrc_t rs_f = mk_rsrc(NJ ? P.wu : P.wg, DM * FF * 2), rs_ws = mk_rsrc(P.ws, WS_BYTES);
  u32x4 ev;
  if (w == 0) ev = ld16c_asm(rs_ws, 0u, WS_EPOCH);
  const unsigned sx0 = (unsigned)rb * 131072u, sq0 = (unsigned)g * 192u;
  constexpr int NQ = NJ ? 2 : 1;
  constexpr int NA = 2 + NQ;
  unsigned vx[2], vq[2], vf[3];
#pragma unroll
  for (int j = 0; j < 2; ++j) vx[j] = (unsigned)(16 * w + 8 * j + (lane >> 3)) * 1024u + (unsigned)(((lane & 7) ^ (4 * j + (lane >> 4))) * 16);
#pragma unroll
  for (int j = 0; j < NQ; ++j) { const int ins = NJ ? 2 * mi + j : 8 + mi; const int G = ins * 64 + lane;
    vq[j] = (unsigned)(G / 12) * 3072u + (unsigned)(G % 12) * 16u; }
  const int u0 = (g * NU) >> 4, nu = (((g + 1) * NU) >> 4) - u0;
  const unsigned sf0 = (unsigned)u0 * 32u;
  auto piece = [&](int gs, int j) {
    const unsigned slot = (unsigned)(gs % 5) * SLOT1;
    const unsigned c = (unsigned)(gs + rot) & 7u;
    if (gs < 8) {
      if (j < 2) dma16(slot + (unsigned)(2 * w + j) * 1024u, vx[j], rs_x, sx0 + c * 128u);
      else dma16(slot + 16384u + (unsigned)(NJ ? 2 * mi + j - 2 : 8 + mi) * 1024u, vq[j - 2], rs_q, sq0 + c * (64u * 3072u));
    } else dma16(slot + NJ * 12288u + (unsigned)(3 * mi + j) * 1024u, vf[j], rs_f, sf0 + c * (64u * 2752u));
  };
#pragma unroll
  for (int j = 0; j < NA; ++j) piece(0, j);
  const unsigned myrow = (unsigned)(128 * rb + 8 * g + w);
  u32x4 xrow = ld16_asm(rs_x, (unsigned)lane * 16u, myrow * 1024u);
#pragma unroll
  for (int sg = 1; sg < L::NPRO; ++sg)
#pragma unroll
    for (int j = 0; j < NA; ++j) piece(sg, j);
  SB0();
  STAMP(1);
#pragma unroll
  for (int j = 0; j < 3; ++j) { const int G = (3 * mi + j) * 64 + lane, kg = G / 48, rr = G % 48, unit = (rr >> 3) ^ NJ;
    vf[j] = unit < nu ? (unsigned)(4 * kg + ((rr & 7) >> 1)) * 2752u + (unsigned)(unit * 32 + (rr & 1) * 16) : 0x40000000u; }
  const int r = lane & 31, h = lane >> 5, nsub = (lane >> 4) & 1, q4 = (lane & 15) >> 2, p4 = lane & 3;
  unsigned xa[4], ro[4];
#pragma unroll
  for (int p = 0; p < 4; ++p) { const int ks = (p + mi) & 3;
    xa[p] = (unsigned)(32 * mi + r) * 128u + (unsigned)(((2 * ks + h) ^ ((r >> 1) & 7)) * 16);
    ro[p] = (unsigned)ks * 3072u; }
  const unsigned wl = (unsigned)(2 * h) * 768u + q4 * 32u + p4 * 8u;
  const unsigned wa = 16384u + (unsigned)(2 * h) * 768u + q4 * 192u + nsub * 32u + p4 * 8u;
  const unsigned wbE = nsub * 12288u + wl + nsub * 128u;
  const unsigned wbO = nsub * 12288u + wl - nsub * 128u;
  const AttnSrc asrc = attn_src(rb2, hp2, w);
  bool early = false;
  auto warm = [&](int i) {
    const unsigned xl = blockIdx.x >> 3, dst = L_DUMMY + (unsigned)w * 256u;
    if (i == 0) {
      const unsigned q = (unsigned)(w & 1) * 64u + (unsigned)lane, rr = q / 7u, ln = q - rr * 7u;
      const unsigned ub = (unsigned)((((blockIdx.x & 3) * 4 * NU) >> 4) * 32) & ~127u;
      pf_lines(dst, (w < 4 && q < 112u) ? (16u * xl + rr) * 2752u + ub + ln * 128u : 0x40000000u, mk_rsrc((w & 2) ? P.wu : P.wg, DM * FF * 2));
    } else {
      const unsigned q = xl * 8u + (unsigned)w + 256u * (unsigned)(i - 1);
      dma16(L_DUMMY + 2048u + (unsigned)w * 1024u, (unsigned)lane * 16u + (q < 1888u ? 0u : 0x40000000u), mk_rsrc(q < 1376u ? P.wd : P.wo, q < 1376u ? FF * DM * 2 : DM * DM * 2), (q < 1376u ? q : q - 1376u) * 1024u);
    }
  };
  auto op_at = [&](auto tc, auto kc) {
    constexpr int o = SC.op[decltype(tc)::value][decltype(kc)::value];
    if constexpr (o >= OP_ATT) attn_issue(rs_ws, asrc, w, lane, o - OP_ATT, early);
    else if constexpr (o >= OP_PFW) warm(o - OP_PFW);
    else if constexpr (o >= 0) piece(o >> 3, o & 7);
  };
  bf16x8 sel;
  { const unsigned v = (lane & 15) == ((lane >> 4) & 1) ? ONES2 : 0u; sel = __builtin_bit_cast(bf16x8, u32x4{v, v, v, v}); }

  bf16x8 xf[32];
  constexpr int NB1 = NJ ? 1 : 2;
  f32x16 acc[3];
  f32x4 cs[3];
#pragma unroll
  for (int b = 0; b < 3; ++b) { cs[b] = f32x4{0.f, 0.f, 0.f, 0.f};
#pragma unroll
    for (int i = 0; i < 16; ++i) acc[b][i] = 0.f; }
  bf16x8 wf[3][NB1], wg[2][3];
  auto ldx = [&](int t) { xf[t] = lds_b128((unsigned)((t >> 2) % 5) * SLOT1 + xa[t & 3]); };
  auto ld1a = [&](int t, int b) {
    const unsigned a = (unsigned)((t >> 2) % 5) * SLOT1 + wa + ro[t & 3] + (NJ ? 2 : b) * 64u; wf[t % 3][b] = cat(lds_tr(a), lds_tr(a + 768u));
  };
  auto ld1b = [&](int t, int b) {
    const int u = NJ * 3 + b;
    const unsigned a = (unsigned)((8 + (t >> 2)) % 5) * SLOT1 + ((u & 1) ? wbO : wbE) + ro[t & 3] + u * 128u; wg[t & 1][b] = cat(lds_tr(a), lds_tr(a + 768u));
  };
  auto cs_put = [&](int nb, int b0) {
#pragma unroll
    for (int b = 0; b < 3; ++b) if (b < nb && lane < 16) { *(LAS float*)(lds + L_CSP + (((b0 + b) * 32 + lane) * 4 + mi) * 4) = cs[b][0]; *(LAS float*)(lds + L_CSP + (((b0 + b) * 32 + 16 + lane) * 4 + mi) * 4) = cs[b][1]; }
  };
  auto cs_get = [&](int nb, int b0) {
#pragma unroll
    for (int b = 0; b < 3; ++b) if (b < nb) { const f32x4 c4 = lds_f4(L_CSP + ((b0 + b) * 32 + r) * 16);
      *(LAS float*)(lds + L_CSW + w * 384 + b * 128 + r * 4) = (c4[0] + c4[1]) + (c4[2] + c4[3]); }
  };
  wait_vm<(L::NPRO - 1) * NA + L::NTW>();
  if (w == 0) { tie(ev); word_set(2, ev[0]); word_set(3, ev[1]); }
  bar();
  const u64 e0 = (u64)word_get(2) | ((u64)word_get(3) << 32);
  epoch = e0 + 1ull;
  tg.a = (unsigned)epoch; tg.b = ((unsigned)(e0 >> 32) ^ 0x5bd1e995u) + tg.a;
  STAMP(2); CSTAMP(23);
  ldx(0); ldx(1);
#pragma unroll
  for (int b = 0; b < NB1; ++b) { ld1a(0, b); ld1a(1, b); }
  u32x4 sv;
  float rmean = 0.f, rrstd = 0.f;
  const unsigned sv_off = WS_RS + (unsigned)(128 * rb + 32 * mi) * 16u;
  static_for<0, 32>([&](auto tc) {
    constexpr int t = decltype(tc)::value, s = t >> 2, p = t & 3;
    if constexpr (p == 2) {
      if (s == 7) cs_put(NB1, NJ ? 2 : 0);
      wait_vm<SC.wait_stage(t, s + 1)>();
      if (s == 7) bar(); else bar_raw();
      STAMP2(24 + s);
      if (s == 7) cs_get(NB1, NJ ? 2 : 0);
      if (s == 1) CSTAMP(60); if (s == 4) CSTAMP(61); if (s == 7) CSTAMP(62);
    }
    if (NJ) op_at(tc, IC<0>{});
    static_for<0, NB1>([&](auto bc) {
      constexpr int b = decltype(bc)::value;
      acc[b] = mfma(wf[t % 3][b], xf[t], acc[b]);
      if (t < 30) { if (b == 0) ldx(t + 2); ld1a(t + 2, b); }
      if (t == 31) { ld1b(0, b); if (b == NB1 - 1) { for (int bb = NB1; bb < 3; ++bb) ld1b(0, bb); } }
      if (p == (NJ ? 1 : b)) cs[b] = mfma16(sel, wf[t % 3][b], cs[b]);
      if (b == 0) {
        if (t == 2 + NJ) {
          tie(xrow);
          float e[8];
#pragma unroll
          for (int i = 0; i < 4; ++i) { e[2 * i] = bf_lo(xrow[i]); e[2 * i + 1] = bf_hi(xrow[i]); }
          rmean = wave_sum(((e[0] + e[1]) + (e[2] + e[3])) + ((e[4] + e[5]) + (e[6] + e[7]))) * (1.f / 512.f);
          asm volatile("" : "+v"(rmean));
        }
        if (t == 4) {
          float q = 0.f;
#pragma unroll
          for (int i = 0; i < 4; ++i) { const float d0 = bf_lo(xrow[i]) - rmean, d1 = bf_hi(xrow[i]) - rmean; q = fmaf(d0, d0, q); q = fmaf(d1, d1, q); }
          rrstd = __builtin_amdgcn_rsqf(wave_sum(q) * (1.f / 512.f) + 1e-5f);
          asm volatile("" : "+v"(rrstd));
        }
      }
      SB0();
      if (b == 0) op_at(tc, IC<NJ ? 1 : 0>{});
    });
    if (!NJ) op_at(tc, IC<1>{});
    if (t == 22) sv = ld16c_asm(rs_ws, (unsigned)r * 16u, sv_off);
    if (t == 5 && lane == 0) st16c(u32x4{__float_as_uint(rmean), tg.a, __float_as_uint(rrstd), tg.b}, rs_ws, 0u, WS_RS + myrow * 16u);
    SB0();
  });
  STAMP(3); CSTAMP(22);
  { constexpr int after = SC.before[32] - L::NST - SC.before[23]; wait_vm_tie<after>(sv); }
  NOTE(53, __all(sv[1] == tg.a && sv[3] == tg.b) ? 1 : 2);
  for (unsigned spins = 0; !__all(sv[1] == tg.a && sv[3] == tg.b) && spins < SPIN_CAP; ++spins) {
    __builtin_amdgcn_s_sleep(1);
    sv = ld16c_asm(rs_ws, (unsigned)r * 16u, sv_off); wait_vm_tie<0>(sv);
  }
  const float mean = __uint_as_float(sv[0]), rstd = __uint_as_float(sv[2]);
  const int sb = rb * 4 + mi;
#pragma unroll
  for (int b = 0; b < NB1; ++b) {
    const int blk = 3 * g + (NJ ? 2 : b), which = blk >> 4, hp = blk & 15;
    const float sc = which == 0 ? rstd * (0.25f * 1.4426950408889634f) : rstd;
    float v[16];
#pragma unroll
    for (int i = 0; i < 4; ++i) { const f32x4 c4 = lds_f4(L_CSW + w * 384 + b * 128 + (8 * i + 4 * h) * 4);
#pragma unroll
      for (int e = 0; e < 4; ++e) v[4 * i + e] = (acc[b][4 * i + e] - mean * c4[e]) * sc; }
    const unsigned vo = which == 2 ? (unsigned)(h * 512 + ((r ^ (4 * h)) * 16)) : (unsigned)(lane * 16);
#pragma unroll
    for (int hd = 0; hd < 2; ++hd) {
      const u32x4 o = {pk(v[8 * hd], v[8 * hd + 1]), pk(v[8 * hd + 2], v[8 * hd + 3]), pk(v[8 * hd + 4], v[8 * hd + 5]), pk(v[8 * hd + 6], v[8 * hd + 7])};
      st16c(o, rs_ws, vo, WS_QKV + (unsigned)((which * 32 + 2 * hp + hd) * 64 + sb) * 1024u);
    }
  }
  STAMP(4); CSTAMP(18);
#pragma unroll
  for (int b = 0; b < 3; ++b) { cs[b] = f32x4{0.f, 0.f, 0.f, 0.f};
#pragma unroll
    for (int i = 0; i < 16; ++i) acc[b][i] = 0.f; }
  bool act;
  const unsigned qv = qkv_flag_voff(rb2, hp2, lane, act);
  u32x4 fq;
  SB0();
  static_for<32, 64>([&](auto tc) {
    constexpr int Tg = decltype(tc)::value, t = Tg - 32, s = Tg >> 2, p = t & 3;
    if constexpr (p == 3) {
      if (s == 15) cs_put(3, NJ * 3);
      if constexpr (s == 9) wait_vm<(SC.before[Tg] - SC.before[32] < SC.wait_stage(Tg, 10) ? SC.before[Tg] - SC.before[32] : SC.wait_stage(Tg, 10))>();
      else if constexpr (s == ATT_B) { wait_vm<0>(); if (w == 0) { tie(fq); word_set(0, flags_ok(fq, act, tg) ? 1u : 0u); } }
      else if constexpr (s <= 14) wait_vm<SC.wait_stage(Tg, s + 1)>();
      if (s == ATT_B || s == 15) bar(); else bar_raw();
      STAMP2(24 + s);
      if (s == 8) CSTAMP(56); if (s == 11) CSTAMP(57); if (s == 13) CSTAMP(58); if (s == 15) CSTAMP(59);
      if (s == ATT_B) early = word_get(0) != 0u;
      if (s == 15) cs_get(3, NJ * 3);
    }
    if (NJ) op_at(tc, IC<0>{});
    static_for<0, 3>([&](auto bc) {
      constexpr int b = decltype(bc)::value;
      acc[b] = mfma(wg[t & 1][b], xf[t], acc[b]);
      if (t < 31) ld1b(t + 1, b);
      if (p == ((b + 2 * NJ) & 3) || (s == 15 && p == 2 && ((b + 2 * NJ) & 3) == 3)) { if (!(s == 15 && p == 3)) cs[b] = mfma16(sel, wg[t & 1][b], cs[b]); }
      SB0();
    });
    if (!NJ) op_at(tc, IC<0>{});
    op_at(tc, IC<1>{});
    if (Tg == 39) flag_store(rs_ws, WS_FQKV + (unsigned)((rb * 16 + g) * 64 + w * 8), tg, lane);
    if (Tg == 4 * ATT_B - 1 && w == 0) fq = ld16c_asm(rs_ws, qv, 0u);
    SB0();
  });
  STAMP(5); CSTAMP(19);
  const float r2 = rstd * rstd;
#pragma unroll
  for (int b = 0; b < 3; ++b) {
    float v[16];
#pragma unroll
    for (int i = 0; i < 4; ++i) { const f32x4 c4 = lds_f4(L_CSW + w * 384 + b * 128 + (8 * i + 4 * h) * 4);
#pragma unroll
      for (int e = 0; e < 4; ++e) v[4 * i + e] = acc[b][4 * i + e] - mean * c4[e]; }
    float hv[8];
#pragma unroll
    for (int i = 0; i < 8; ++i) hv[i] = fmaxf(v[i], 0.f) * v[8 + i] * r2;
    const u32x4 o = {pk(hv[0], hv[1]), pk(hv[2], hv[3]), pk(hv[4], hv[5]), pk(hv[6], hv[7])};
    if (NJ * 3 + b < nu) st16c(o, rs_ws, (unsigned)lane * 16u, WS_H + (unsigned)(sb * NU + u0 + NJ * 3 + b) * 1024u);
  }
  STAMP(6);
  if (NJ == 0 || nu == 6) wait_vm<3>(); else wait_vm<2>();
  return early;
}

template <bool FULL, class F1, class FM, class F2>
DI u32x4 attn_task(int j0, int qb, int hd, int lane, F1 side1, FM mid, F2 side2) {
  const int q = lane & 31, hh = lane >> 5, tq = q - 4 * hh;
  const bf16x8 qf = lds_b128(LA_Q + (unsigned)(hd * 4 + qb) * 1024u + lane * 16u);
  const unsigned kbase = LA_K + (unsigned)(hd * 12 + qb) * 1024u + lane * 16u;
  const float NEG = -__builtin_inff();
  bf16x8 kf[9];
#pragma unroll
  for (int j = 0; j < 9; ++j) if (FULL || j >= j0) kf[j] = lds_b128(kbase + j * 1024u);
  f32x16 c;
#pragma unroll
  for (int i = 0; i < 16; ++i) c[i] = 0.f;
  auto on = [&](int j) { return FULL || j >= j0; };
  float m0 = NEG, m1 = NEG;
  f32x16 s[2];
  {
    const f32x16 s8 = mfma(kf[8], qf, c);
    f32x16 s0;
    if (FULL) s0 = mfma(kf[0], qf, c);
    if (on(1)) s[1] = mfma(kf[1], qf, c);
    SB0();
#pragma unroll
    for (int i = 0; i < 16; i += 2) {
      const int c0 = (i & 3) + 8 * (i >> 2);
      const float a = c0 <= tq ? s8[i] : (FULL ? s0[i] : NEG), b = c0 + 1 <= tq ? s8[i + 1] : (FULL ? s0[i + 1] : NEG);
      if (i & 2) m1 = fmaxf(fmaxf(m1, a), b); else m0 = fmaxf(fmaxf(m0, a), b);
    }
    side1(0);
    SB0();
  }
#pragma unroll
  for (int j = 1; j < 8; ++j) {
    if (j < 7 && on(j + 1)) s[(j + 1) & 1] = mfma(kf[j + 1], qf, c);
    SB0();
    if (on(j)) {
#pragma unroll
      for (int i = 0; i < 8; i += 2) { m0 = fmaxf(fmaxf(m0, s[j & 1][i]), s[j & 1][i + 1]); m1 = fmaxf(fmaxf(m1, s[j & 1][8 + i]), s[j & 1][9 + i]); }
    }
    side1(j);
    SB0();
  }
  const float m = xhalf_max(fmaxf(m0, m1));
  mid();
#pragma unroll
  for (int i = 0; i < 16; ++i) c[i] = -m;
  const int grp = (lane >> 4) & 1, q4 = (lane & 15) >> 2, p4 = lane & 3;
  const unsigned vb = grp ? L_ONES + 128u : LA_V + (unsigned)(hd * 12 + qb) * 1024u + (unsigned)((p4 & 1) * 512 + (((4 * hh + q4) ^ (4 * (p4 & 1))) * 16) + (p4 >> 1) * 8);
  bf16x8 vf[2][2];
  auto ldv = [&](int j, int slot) {
#pragma unroll
    for (int u = 0; u < 2; ++u) vf[slot][u] = cat(lds_tr(vb + j * 1024u + u * 256u), lds_tr(vb + j * 1024u + u * 256u + 128u));
  };
  f32x16 acc;
#pragma unroll
  for (int i = 0; i < 16; ++i) acc[i] = 0.f;
  auto pv = [&](int slot, const unsigned (&pp)[8]) {
#pragma unroll
    for (int u = 0; u < 2; ++u) acc = mfma(vf[slot][u], __builtin_bit_cast(bf16x8, u32x4{pp[4 * u], pp[4 * u + 1], pp[4 * u + 2], pp[4 * u + 3]}), acc);
  };
  f32x16 s8, s0;
  if (on(1)) { s[1] = mfma(kf[1], qf, c); ldv(1, 1); }
#pragma unroll
  for (int j = 1; j < 8; ++j) {
    if (j < 7) { if (on(j + 1)) { s[(j + 1) & 1] = mfma(kf[j + 1], qf, c); ldv(j + 1, (j + 1) & 1); } }
    else { s8 = mfma(kf[8], qf, c); if (FULL) s0 = mfma(kf[0], qf, c); ldv(8, 0); }
    SB0();
    if (on(j)) {
      unsigned pp[8];
#pragma unroll
      for (int i = 0; i < 8; ++i) pp[i] = pk(__builtin_amdgcn_exp2f(s[j & 1][2 * i]), __builtin_amdgcn_exp2f(s[j & 1][2 * i + 1]));
      pv(j & 1, pp);
    }
    side2(j);
    SB0();
  }
  {
    if (FULL) ldv(0, 1);
    unsigned p8[8], p0[8];
#pragma unroll
    for (int i = 0; i < 8; ++i) {
      const int c0 = ((2 * i) & 3) + 8 * (i >> 1);
      const bool va = c0 <= tq, vb_ = c0 + 1 <= tq;
      const float a = va ? s8[2 * i] : (FULL ? s0[2 * i] : NEG), b = vb_ ? s8[2 * i + 1] : (FULL ? s0[2 * i + 1] : NEG);
      const unsigned pp = pk(__builtin_amdgcn_exp2f(a), __builtin_amdgcn_exp2f(b));
      const unsigned mk = (va ? 0xffffu : 0u) | (vb_ ? 0xffff0000u : 0u);
      p8[i] = pp & mk; p0[i] = pp & ~mk;
    }
    pv(0, p8);
    if (FULL) pv(1, p0);
  }
  const float inv = __builtin_amdgcn_rcpf(acc[8]);
  return u32x4{pk(acc[0] * inv, acc[1] * inv), pk(acc[2] * inv, acc[3] * inv), pk(acc[4] * inv, acc[5] * inv), pk(acc[6] * inv, acc[7] * inv)};
}

#define H_FLAG_AT 2
constexpr int DW = 10, DA = 8, NK3 = 30, NDN = 22, OCHK = NDN - (DA - 1), OPOLL = OCHK - 5, XLD = 20;
struct Sched3 {
  int before[NK3 + 1] = {}, lastW[NK3] = {}, lastA[NK3] = {};
  constexpr Sched3() {
    int c = 0;
    for (int i = 0; i < DW - 1; ++i) { c += 2; lastW[i] = c; }
    for (int i = 0; i < DA - 1; ++i) { c += 2; lastA[i] = c; }
    for (int i = 0; i < NK3; ++i) {
      before[i] = c;
      if (i + DW - 1 < NK3) { c += 2; lastW[i + DW - 1] = c; }
      if (i + DA - 1 < NK3) { c += 2; lastA[i + DA - 1] = c; }
      if (i == XLD) c += 4;
    }
    before[NK3] = c;
  }
  constexpr int wait(int i) const { const int w = i + 1 < NK3 ? lastW[i + 1] : 0; return before[i] - (lastA[i] > w ? lastA[i] : w); }
};
DI unsigned l3w(int kq) { return kq < 3 ? 86016u + (unsigned)kq * (DW * 2048u) : 0u; }

DI void phase23(const Ptrs& P, bool early, int rb1, int g1, u64 epoch, int rb, int hp, int rb64, int cb, int rot, int w, int lane, Tag tg, Stamps& T) {
  const rsrc_t rs_ws = mk_rsrc(P.ws, WS_BYTES);
  for (unsigned i = threadIdx.x; i < ONES_BYTES / 16; i += 512) *(LAS u32x4*)(lds + L_ONES + i * 16) = u32x4{ONES2, ONES2, ONES2, ONES2};
  NOTE(63, early ? 1 : 2);
  if (!early) {
    bool act; const unsigned qv = qkv_flag_voff(rb, hp, lane, act);
    if (w == 0) flags_spin(rs_ws, qv, act, tg);
    bar();
    const AttnSrc asrc = attn_src(rb, hp, w);
#pragma unroll
    for (int i = 0; i < 7; ++i) attn_issue(rs_ws, asrc, w, lane, i, true);
    wait_vm<0>();
  }
  bar();
  STAMP(9);
  const unsigned fh_off = WS_FH + (unsigned)((rb1 * 16 + g1) * 64 + w * 8);
  const int h = lane >> 5, nsub = (lane >> 4) & 1, q4 = (lane & 15) >> 2, p4 = lane & 3;
  f32x16 acc[4];
  u32x2 xr[4];
  const int kq = w - 4;
  const bool keeper = blockIdx.x == 0 && w == 4;
  u32x4 fe[4];
  const rsrc_t rs_d = mk_rsrc(P.wd, FF * DM * 2), rs_o = mk_rsrc(P.wo, DM * DM * 2);
  const int ulo = kq < 3 ? 22 * kq : 65, nun = kq < 2 ? 22 : 21;
  unsigned vw[2];
#pragma unroll
  for (int j = 0; j < 2; ++j) vw[j] = (unsigned)(8 * j + (lane >> 3)) * 1024u + (unsigned)(((lane & 7) ^ (((lane >> 4) & 1) << 2)) * 16);
  const unsigned ring = __builtin_amdgcn_readfirstlane(l3w(kq)), vo = (unsigned)lane * 16u;
  auto unit = [&](int i) { const int r = i + rot; return i < NDN ? (i < nun ? ulo + (r >= nun ? r - nun : r) : -1) : 8 * kq + ((i - NDN + rot) & 7); };
  auto reqW = [&](int i) {
    const int u = unit(i);
#pragma unroll
    for (int j = 0; j < 2; ++j) dma16(ring + (unsigned)(i % DW) * 2048u + j * 1024u, u < 0 ? 0x40000000u : vw[j], i < NDN ? rs_d : rs_o, (unsigned)cb * 128u + (unsigned)(u < 0 ? 0 : u) * 16384u);
  };
  const unsigned hv = WS_FH + (unsigned)((rb64 >> 1) * 1024 + lane * 16), ov = WS_FO + (unsigned)((rb64 >> 1) * 1024 + lane * 16);
  const int pg = lane >> 2;
  const bool pair_ok = ((lane & 3) & 1) == (rb64 & 1);
  const bool h_act = pair_ok && (((pg + 1) * NU) >> 4) > ulo && ((pg * NU) >> 4) < ulo + nun, o_act = pair_ok && (pg >> 2) == kq;
  u32x4 fh;
  if (w >= 4) __builtin_amdgcn_s_setprio(3);
  {
    const int qb = w & 3, hd = w >> 2, sb = 4 * rb + qb;
    auto side1 = [&](int j) {
      if (w >= 4 && j >= 1) reqW(j - 1);
    };
    auto mid = [&]() { STAMP(10); };
    auto side2 = [&](int j) {
      if (w >= 4 && j <= 2) reqW(6 + j);
      if (j == H_FLAG_AT) { if (w >= 4) wait_vm<18>(); else wait_vm<0>(); flag_store(rs_ws, fh_off, tg, lane); }
      if (w >= 4 && j == 7) fh = ld16c_asm(rs_ws, hv, 0u);
    };
    u32x4 o;
    if (sb >= 8) o = attn_task<true>(0, qb, hd, lane, side1, mid, side2); else o = attn_task<false>(8 - sb, qb, hd, lane, side1, mid, side2);
    st16c(o, rs_ws, (unsigned)lane * 16u, WS_O + (unsigned)(sb * 32 + 2 * hp + hd) * 1024u);
    STAMP(11);
  }
  const unsigned fo_off = WS_FO + (unsigned)((rb * 16 + hp) * 64 + w * 8);
  if (w < 4) {
    wait_vm<0>();
    flag_store(rs_ws, fo_off, tg, lane);
    STAMP(12);
  } else {
    wait_vm_tie<1>(fh);
    NOTE(55, flags_ok(fh, h_act, tg) ? 1 : 2);
    if (!flags_ok(fh, h_act, tg)) flags_spin(rs_ws, hv, h_act, tg);
    STAMP(13);
    u32x4 A[DA][2];
    auto reqA = [&](int i) {
      const int u = unit(i);
#pragma unroll
      for (int mb = 0; mb < 2; ++mb)
        A[i % DA][mb] = ld16c_asm(rs_ws, u < 0 ? 0x40000000u : vo, i < NDN ? WS_H + (unsigned)((2 * rb64 + mb) * NU + (u < 0 ? 0 : u)) * 1024u : WS_O + (unsigned)((2 * rb64 + mb) * 32 + u) * 1024u);
    };
#pragma unroll
    for (int i = 0; i < DA - 1; ++i) reqA(i);
#pragma unroll
    for (int b = 0; b < 4; ++b)
#pragma unroll
      for (int i = 0; i < 16; ++i) acc[b][i] = 0.f;
    unsigned wa[2];
#pragma unroll
    for (int nb = 0; nb < 2; ++nb) wa[nb] = ring + (unsigned)h * 512u + q4 * 128u + (unsigned)((nb ^ (q4 >> 1)) * 64) + nsub * 32u + p4 * 8u;
    const int row = 64 * rb64 + 32 * (kq >> 1) + (lane & 31), col0 = 64 * cb + 32 * (kq & 1) + 4 * h;
    const unsigned short* xp = (const unsigned short*)P.x + (size_t)row * DM + col0;
    static constexpr Sched3 S3{};
    u32x4 fo;
    bf16x8 wf[2][2];
    auto ldw = [&](int i) {
#pragma unroll
      for (int nb = 0; nb < 2; ++nb) { const unsigned a = wa[nb] + (unsigned)(i % DW) * 2048u; wf[i & 1][nb] = cat(lds_tr(a), lds_tr(a + 1024u)); }
    };
    ldw(0);
    static_for<0, NK3>([&](auto ic) {
      constexpr int i = decltype(ic)::value;
      wait_vm<S3.wait(i)>();
      tie(A[i % DA][0]); tie(A[i % DA][1]);
      if (i == 0) STAMP(14);
      if constexpr (i == OCHK) {
        wait_vm_tie<S3.before[i] - S3.before[OPOLL + 1]>(fo);
        NOTE(54, flags_ok(fo, o_act, tg) ? 1 : 2);
        if (!flags_ok(fo, o_act, tg)) flags_spin(rs_ws, ov, o_act, tg);
      }
      SB0();
      acc[0] = mfma(wf[i & 1][0], __builtin_bit_cast(bf16x8, A[i % DA][0]), acc[0]);
      if constexpr (i + 1 < NK3) ldw(i + 1);
      SB0();
      acc[1] = mfma(wf[i & 1][1], __builtin_bit_cast(bf16x8, A[i % DA][0]), acc[1]);
      SB0();
      if constexpr (i + DW - 1 < NK3) reqW(i + DW - 1);
      acc[2] = mfma(wf[i & 1][0], __builtin_bit_cast(bf16x8, A[i % DA][1]), acc[2]);
      SB0();
      acc[3] = mfma(wf[i & 1][1], __builtin_bit_cast(bf16x8, A[i % DA][1]), acc[3]);
      SB0();
      if constexpr (i + DA - 1 < NK3) reqA(i + DA - 1);
      if (i == 1) flag_store(rs_ws, fo_off, tg, lane);
      if (i == OPOLL) fo = ld16c_asm(rs_ws, ov, 0u);
      if (i == XLD) {
#pragma unroll
        for (int q = 0; q < 4; ++q) xr[q] = *(const GAS u32x2*)(xp + 8 * q);
      }
      if (i == 22 && keeper) {
#pragma unroll
        for (int q = 0; q < 4; ++q) fe[q] = ld16c_asm(rs_ws, WS_FQKV + (unsigned)(q * 64 + lane) * 64u, 0u);
      }
      SB0();
    });
    STAMP(15);
  }
  if (w >= 4) {
#pragma unroll
    for (int b = 0; b < 4; ++b) {
      if (b != kq) {
        const unsigned base = ring + (unsigned)((b - kq - 1 + 4) % 4) * 4096u + lane * 16u;
#pragma unroll
        for (int q = 0; q < 4; ++q) *(LAS f32x4*)(lds + base + q * 1024u) = f32x4{acc[b][4 * q], acc[b][4 * q + 1], acc[b][4 * q + 2], acc[b][4 * q + 3]};
      }
    }
  }
  bar();
  if (w >= 4) {
    f32x16 r = kq == 0 ? acc[0] : (kq == 1 ? acc[1] : (kq == 2 ? acc[2] : acc[3]));
#pragma unroll
    for (int s = 1; s < 4; ++s) {
      const int src = (kq + s) & 3;
      const unsigned base = l3w(src) + (unsigned)((kq - src - 1 + 4) % 4) * 4096u + lane * 16u;
#pragma unroll
      for (int q = 0; q < 4; ++q) { const f32x4 v = lds_f4(base + q * 1024u);
#pragma unroll
        for (int e = 0; e < 4; ++e) r[4 * q + e] += v[e]; }
    }
    const int row = 64 * rb64 + 32 * (kq >> 1) + (lane & 31), col0 = 64 * cb + 32 * (kq & 1);
    unsigned o[8];
#pragma unroll
    for (int q = 0; q < 4; ++q) { o[2 * q] = pk(r[4 * q] + bf_lo(xr[q][0]), r[4 * q + 1] + bf_hi(xr[q][0])); o[2 * q + 1] = pk(r[4 * q + 2] + bf_lo(xr[q][1]), r[4 * q + 3] + bf_hi(xr[q][1])); }
    unsigned short* op = (unsigned short*)P.out + (size_t)row * DM + col0 + 8 * h;
#pragma unroll
    for (int q = 0; q < 4; q += 2) {
      const auto t0 = __builtin_amdgcn_permlane32_swap(o[2 * q], o[2 * q + 2], false, false), t1 = __builtin_amdgcn_permlane32_swap(o[2 * q + 1], o[2 * q + 3], false, false);
      *(GAS u32x4*)(op + 8 * q) = u32x4{t0[0], t1[0], t0[1], t1[1]};
    }
    if (keeper) {
#pragma unroll
      for (int q = 0; q < 4; ++q) { tie(fe[q]); if (!flags_ok(fe[q], true, tg)) flags_spin(rs_ws, WS_FQKV + (unsigned)(q * 64 + lane) * 64u, true, tg); }
      if (lane == 0) __hip_atomic_store((GAS u64*)(P.ws + WS_EPOCH), epoch, __ATOMIC_RELAXED, __HIP_MEMORY_SCOPE_AGENT);
    }
  }
}

__global__ __launch_bounds__(512, 2) void mk_kernel(const void* x, const void* wqkv, const void* wo, const void* wg, const void* wu, const void* wd, void* out, char* ws) {
  u64 pc0 = 0;
  asm volatile("mk_code_begin:\n\ts_getpc_b64 %0" : "=s"(pc0));
  {
    unsigned len;
    asm volatile("s_mov_b32 %0, mk_code_end-mk_code_begin" : "=s"(len));
    const unsigned wv = __builtin_amdgcn_readfirstlane(threadIdx.x >> 6);
    const unsigned per = ((len >> 5) + 1023u) & ~1023u;
    const unsigned o = (blockIdx.x >> 3) * per + wv * 1024u;
    dma16(L_DUMMY + wv * 1024u, (threadIdx.x & 63u) * 16u, mk_rsrc((const void*)(pc0 & ~255ull), len & ~15u), wv * 1024u < per ? o : 0x40000000u);
  }
  const Ptrs P{x, wqkv, wo, wg, wu, wd, out, ws, pc0};
  Stamps T;
  STAMP(0); CSTAMP(20);
  __builtin_amdgcn_s_setprio(1);
  const int b = blockIdx.x, xcd = b & 7, idx = b >> 3;
  const int w = __builtin_amdgcn_readfirstlane(threadIdx.x >> 6), lane = threadIdx.x & 63;
  Tag tg; u64 epoch;
  const int rb = 2 * (idx >> 2) + (xcd >> 2), g = (xcd & 3) * 4 + (idx & 3), rot1 = ((idx >> 2) + 2 * (idx & 3)) & 7;
  bool early;
  if (w < 4) early = phase1<0>(P, rb, g, rot1, w, lane, rb, g, tg, epoch, T); else early = phase1<1>(P, rb, g, rot1, w - 4, lane, rb, g, tg, epoch, T);
  const int rot3 = ((idx & 7) * 2 + 5 * (idx >> 3)) % 21;
  phase23(P, early, rb, g, epoch, rb, g, xcd * 4 + (idx >> 3), idx & 7, rot3, w, lane, tg, T);
  asm volatile("mk_code_end:");
}

}

extern "C" void kernel_launch(void* const* d_in, const int* in_sizes, int n_in, void* d_out, int out_size, void* d_ws, size_t ws_size, hipStream_t stream) {
  const void *x = d_in[0], *wqkv = d_in[1], *wo = d_in[2], *wg = d_in[4], *wu = d_in[5], *wd = d_in[6];
  mk_kernel<<<256, 512, 0, stream>>>(x, wqkv, wo, wg, wu, wd, d_out, (char*)d_ws);
}
```

```cpp
#include <hip/hip_runtime.h>
#include <hip/hip_bf16.h>
#include <stdint.h>

namespace {

typedef short bf16x8 __attribute__((ext_vector_type(8)));
typedef short s16x4 __attribute__((ext_vector_type(4)));
typedef float f32x16 __attribute__((ext_vector_type(16)));
typedef float f32x4 __attribute__((ext_vector_type(4)));
typedef float f32x2 __attribute__((ext_vector_type(2)));
typedef unsigned u32x4 __attribute__((ext_vector_type(4)));
typedef unsigned u32x2 __attribute__((ext_vector_type(2)));
typedef __bf16 bf16x2_t __attribute__((ext_vector_type(2)));
typedef short v4i16_t __attribute__((ext_vector_type(4)));
typedef __amdgpu_buffer_rsrc_t rsrc_t;
#define LAS __attribute__((address_space(3)))
#define GAS __attribute__((address_space(1)))
#define DI __device__ __forceinline__
#define SB0() __builtin_amdgcn_sched_barrier(0)
typedef unsigned long long u64;

constexpr int SEQ = 2048, DM = 512, FF = 1376, NU = 86;
constexpr unsigned WS_EPOCH = 0;
constexpr unsigned WS_FQKV = 16384, WS_FH = 32768, WS_FO = 49152;
constexpr unsigned WS_RS = 65536;
constexpr unsigned WS_QKV = 1u << 20;
constexpr unsigned WS_H = 7u << 20;
constexpr unsigned WS_O = 13u << 20;
constexpr unsigned WS_BYTES = 16u << 20;
constexpr unsigned SLOT1 = 28672;
constexpr unsigned L_CSP = 143360;
constexpr unsigned L_CSW = 146432;
constexpr unsigned L_ONES = 152576;
constexpr unsigned ONES_BYTES = 9984;
constexpr unsigned L_WORD = 163584;
constexpr unsigned LDS_BYTES = 163840;
constexpr unsigned LA_Q = 28672, LA_K = LA_Q + 8192, LA_V = LA_K + 24576;

__shared__ __attribute__((aligned(1024))) char lds[LDS_BYTES];

DI unsigned pk(float a, float b) { f32x2 v = {a, b}; bf16x2_t r = __builtin_convertvector(v, bf16x2_t); return __builtin_bit_cast(unsigned, r); }
DI rsrc_t mk_rsrc(const void* p, unsigned bytes) { return __builtin_amdgcn_make_buffer_rsrc((void*)p, 0, bytes, 0x00020000); }
DI void dma16(unsigned ldsaddr, unsigned voff, rsrc_t rs, unsigned soff) {
  asm volatile("s_mov_b32 m0, %0\n\ts_nop 0\n\tbuffer_load_dwordx4 %1, %2, %3 offen lds" ::"s"(ldsaddr), "v"(voff), "s"(rs), "s"(soff) : "memory");
}
DI void dma16c(unsigned ldsaddr, unsigned voff, rsrc_t rs, unsigned soff) {
  asm volatile("s_mov_b32 m0, %0\n\ts_nop 0\n\tbuffer_load_dwordx4 %1, %2, %3 offen sc1 lds" ::"s"(ldsaddr), "v"(voff), "s"(rs), "s"(soff) : "memory");
}
template <int N> DI void wait_vm() { asm volatile("s_waitcnt vmcnt(%0)" ::"n"(N) : "memory"); }
template <int N> DI void wait_vm_tie(u32x4& v) { asm volatile("s_waitcnt vmcnt(%1)" : "+v"(v) : "n"(N) : "memory"); }
template <int V> struct IC { static constexpr int value = V; };
template <int I, int N, class F> DI void static_for(F&& f) { if constexpr (I < N) { f(IC<I>{}); static_for<I + 1, N>(f); } }
DI void bar_raw() { asm volatile("s_barrier" ::: "memory"); }
DI void tie(u32x4& v) { asm volatile("" : "+v"(v) :: "memory"); }
DI void bar() { asm volatile("s_waitcnt lgkmcnt(0)\n\ts_barrier" ::: "memory"); }
DI f32x16 mfma(bf16x8 a, bf16x8 b, f32x16 c) { return __builtin_amdgcn_mfma_f32_32x32x16_bf16(a, b, c, 0, 0, 0); }
DI bf16x8 lds_b128(unsigned a) { return *(const LAS bf16x8*)(lds + a); }
DI s16x4 lds_tr(unsigned a) { return __builtin_amdgcn_ds_read_tr16_b64_v4i16((LAS v4i16_t*)(lds + a)); }
DI f32x4 mfma16(bf16x8 a, bf16x8 b, f32x4 c) { return __builtin_amdgcn_mfma_f32_16x16x32_bf16(a, b, c, 0, 0, 0); }
DI f32x4 lds_f4(unsigned a) { return *(const LAS f32x4*)(lds + a); }
DI bf16x8 cat(s16x4 lo, s16x4 hi) { return __builtin_shufflevector(lo, hi, 0, 1, 2, 3, 4, 5, 6, 7); }
DI float xhalf_sum(float v) { auto r = __builtin_amdgcn_permlane32_swap(__float_as_uint(v), __float_as_uint(v), false, false); return __uint_as_float(r[0]) + __uint_as_float(r[1]); }
DI float xhalf_max(float v) { auto r = __builtin_amdgcn_permlane32_swap(__float_as_uint(v), __float_as_uint(v), false, false); return fmaxf(__uint_as_float(r[0]), __uint_as_float(r[1])); }
DI void st16c(u32x4 v, rsrc_t rs, unsigned voff, unsigned soff) { __builtin_amdgcn_raw_buffer_store_b128(v, rs, voff + soff, 0, 16); }
DI void st8c(u32x2 v, rsrc_t rs, unsigned voff, unsigned soff) { __builtin_amdgcn_raw_buffer_store_b64(v, rs, voff, soff, 16); }
constexpr unsigned ONES2 = 0x3F803F80u;
constexpr unsigned SPIN_CAP = 1u << 19;
template <int CTRL> DI float dpp_add(float v) { return v + __int_as_float(__builtin_amdgcn_update_dpp(0, __float_as_int(v), CTRL, 0xf, 0xf, true)); }
DI float wave_sum(float v) {
  v = dpp_add<0xB1>(v); v = dpp_add<0x4E>(v); v = dpp_add<0x141>(v); v = dpp_add<0x140>(v);
  auto a = __builtin_amdgcn_permlane16_swap(__float_as_uint(v), __float_as_uint(v), false, false);
  v = __uint_as_float(a[0]) + __uint_as_float(a[1]);
  return xhalf_sum(v);
}
DI float bf_lo(unsigned u) { return __uint_as_float(u << 16); }
DI float bf_hi(unsigned u) { return __uint_as_float(u & 0xffff0000u); }
DI u32x4 ld16_asm(rsrc_t rs, unsigned voff, unsigned soff) { u32x4 v; asm volatile("buffer_load_dwordx4 %0, %1, %2, %3 offen" : "=v"(v) : "v"(voff), "s"(rs), "s"(soff) : "memory"); return v; }
DI u32x4 ld16c_asm(rsrc_t rs, unsigned voff, unsigned soff) { u32x4 v; asm volatile("buffer_load_dwordx4 %0, %1, %2, %3 offen sc1" : "=v"(v) : "v"(voff), "s"(rs), "s"(soff) : "memory"); return v; }
struct Ptrs { const void *x, *wqkv, *wo, *wg, *wu, *wd; void* out; char* ws; };
struct Tag { unsigned a, b; };

DI void flag_store(rsrc_t rs_ws, unsigned off, Tag tg, int lane) { if (lane == 0) st8c(u32x2{tg.a, tg.b}, rs_ws, 0u, off); }
DI bool flags_ok(u32x4 v, bool act, Tag tg) { return __all(!act || (v[0] == tg.a && v[1] == tg.b && v[2] == tg.a && v[3] == tg.b)); }
DI void flags_spin(rsrc_t rs_ws, unsigned voff, bool act, Tag tg) {
  for (unsigned spins = 0; spins < SPIN_CAP; ++spins) {
    u32x4 v = ld16c_asm(rs_ws, voff, 0u); wait_vm_tie<0>(v);
    if (flags_ok(v, act, tg)) break;
    __builtin_amdgcn_s_sleep(1);
  }
}
DI void word_set(int i, unsigned v) { *(volatile LAS unsigned*)(lds + L_WORD + i * 4) = v; }
DI unsigned word_get(int i) { return __builtin_amdgcn_readfirstlane(*(volatile LAS unsigned*)(lds + L_WORD + i * 4)); }

struct AttnSrc { unsigned q, kv; int sb0; };
DI AttnSrc attn_src(int rb, int hp, int w) {
  AttnSrc a;
  a.q = __builtin_amdgcn_readfirstlane(WS_QKV + (unsigned)((2 * hp + (w >> 2)) * 64 + 4 * rb + (w & 3)) * 1024u);
  a.kv = __builtin_amdgcn_readfirstlane(WS_QKV + (unsigned)(((1 + (w >> 2)) * 32 + 2 * hp + ((w >> 1) & 1)) * 64) * 1024u);
  a.sb0 = __builtin_amdgcn_readfirstlane(4 * rb - 8 + 6 * (w & 1));
  return a;
}
DI void attn_issue(rsrc_t rs_ws, const AttnSrc& a, int w, int lane, int piece, bool go) {
  const unsigned vo = (unsigned)lane * 16u + (go ? 0u : 0x40000000u);
  if (piece == 0) { dma16c(LA_Q + (unsigned)w * 1024u, vo, rs_ws, a.q); return; }
  const int sbk = a.sb0 + piece - 1;
  dma16c(LA_K + (unsigned)((w >> 1) * 12 + 6 * (w & 1) + piece - 1) * 1024u, vo, rs_ws, a.kv + (unsigned)(sbk < 0 ? 0 : sbk) * 1024u);
}
DI unsigned qkv_flag_voff(int rb, int hp, int lane, bool& act) {
  const int c = lane >> 2, kind = c == 0 ? 0 : (c <= 3 ? 1 : 2), d = c == 0 ? 0 : (c - 1) % 3, rr = rb - d;
  act = lane < 28 && rr >= 0;
  return act ? WS_FQKV + (unsigned)((rr * 16 + (kind * 16 + hp) / 3) * 64 + (lane & 3) * 16) : 0u;
}

constexpr unsigned L_DUMMY = 152576;
constexpr int ATT_B = 14;
constexpr int OP_NONE = -1, OP_ATT = 400;
template <int NJ> struct Sched1 {
  static constexpr int NA = NJ ? 4 : 3, NST = NJ ? 2 : 4;
  static constexpr int n(int sg) { return sg < 8 ? NA : 3; }
  static constexpr int TB(int s) { return s < 8 ? 4 * s + 2 : 4 * s + 3; }
  static constexpr int extras(int T) { return (T == 5 ? 1 : 0)   + (T == 22 ? 1 : 0)   + (T == 39 ? 1 : 0)  ; }
  static constexpr int NPRO = 3;
  static constexpr int PRO = 1 + NPRO * NA + 1;
  static constexpr int HOLD = 12;
  int op[64][2] = {}, before[65] = {}, last[16] = {};
  constexpr Sched1() {
    int sg = NPRO, j = 0, att = 0, c = PRO;
    last[0] = 1 + NA;
    for (int i = 1; i < NPRO; ++i) last[i] = 1 + NA + 1 + i * NA;
    for (int T = 0; T < 64; ++T) {
      before[T] = c;
      for (int k = 0; k < 2; ++k) {
        int o = OP_NONE;
        if (k == 0 || T < 14) {
          if (sg <= 15 && (sg < 5 || T >= TB(sg - 4)) && (sg < 8 || T >= 4 * sg - HOLD)) {   o = sg * 8 + j; if (++j == n(sg)) { last[sg] = c + 1; ++sg; j = 0; } }
        }
        if (T >= 4 * ATT_B + 3 && att < 7 && (k == 0 || T >= 61)) o = OP_ATT + att++;
        op[T][k] = o;
        if (o != OP_NONE) ++c;
      }
      c += extras(T);
      if (T == 31) c += NST;
    }
    before[64] = c;
  }
  constexpr int wait_stage(int T, int sg) const { return before[T] - last[sg]; }
};

template <int NJ>
DI bool phase1(const Ptrs& P, int rb, int g, int rot, int mi, int lane, int rb2, int hp2, Tag& tg, u64& epoch) {
  typedef Sched1<NJ> L;
  static constexpr Sched1<NJ> SC{};
  const int w = NJ * 4 + mi;
  const rsrc_t rs_x = mk_rsrc(P.x, SEQ * DM * 2), rs_q = mk_rsrc(P.wqkv, DM * 1536 * 2);
  const rsrc_t rs_f = mk_rsrc(NJ ? P.wu : P.wg, DM * FF * 2), rs_ws = mk_rsrc(P.ws, WS_BYTES);
  u32x4 ev;
  if (w == 0) ev = ld16c_asm(rs_ws, 0u, WS_EPOCH);
  const unsigned sx0 = (unsigned)rb * 131072u, sq0 = (unsigned)g * 192u;
  constexpr int NQ = NJ ? 2 : 1;
  constexpr int NA = 2 + NQ;
  unsigned vx[2], vq[2], vf[3];
#pragma unroll
  for (int j = 0; j < 2; ++j) vx[j] = (unsigned)(16 * w + 8 * j + (lane >> 3)) * 1024u + (unsigned)(((lane & 7) ^ (4 * j + (lane >> 4))) * 16);
#pragma unroll
  for (int j = 0; j < NQ; ++j) { const int ins = NJ ? 2 * mi + j : 8 + mi; const int G = ins * 64 + lane;
    vq[j] = (unsigned)(G / 12) * 3072u + (unsigned)(G % 12) * 16u; }
  const int u0 = (g * NU) >> 4, nu = (((g + 1) * NU) >> 4) - u0;
  const unsigned sf0 = (unsigned)u0 * 32u;
  auto piece = [&](int gs, int j) {
    const unsigned slot = (unsigned)(gs % 5) * SLOT1;
    const unsigned c = (unsigned)(gs + rot) & 7u;
    if (gs < 8) {
      if (j < 2) dma16(slot + (unsigned)(2 * w + j) * 1024u, vx[j], rs_x, sx0 + c * 128u);
      else dma16(slot + 16384u + (unsigned)(NJ ? 2 * mi + j - 2 : 8 + mi) * 1024u, vq[j - 2], rs_q, sq0 + c * (64u * 3072u));
    } else dma16(slot + NJ * 12288u + (unsigned)(3 * mi + j) * 1024u, vf[j], rs_f, sf0 + c * (64u * 2752u));
  };
#pragma unroll
  for (int j = 0; j < NA; ++j) piece(0, j);
  const unsigned myrow = (unsigned)(128 * rb + 8 * g + w);
  u32x4 xrow = ld16_asm(rs_x, (unsigned)lane * 16u, myrow * 1024u);
#pragma unroll
  for (int sg = 1; sg < L::NPRO; ++sg)
#pragma unroll
    for (int j = 0; j < NA; ++j) piece(sg, j);
  SB0();
#pragma unroll
  for (int j = 0; j < 3; ++j) { const int G = (3 * mi + j) * 64 + lane, kg = G / 48, rr = G % 48, unit = (rr >> 3) ^ NJ;
    vf[j] = unit < nu ? (unsigned)(4 * kg + ((rr & 7) >> 1)) * 2752u + (unsigned)(unit * 32 + (rr & 1) * 16) : 0x40000000u; }
  const int r = lane & 31, h = lane >> 5, nsub = (lane >> 4) & 1, q4 = (lane & 15) >> 2, p4 = lane & 3;
  unsigned xa[4], ro[4];
#pragma unroll
  for (int p = 0; p < 4; ++p) { const int ks = (p + mi) & 3;
    xa[p] = (unsigned)(32 * mi + r) * 128u + (unsigned)(((2 * ks + h) ^ ((r >> 1) & 7)) * 16);
    ro[p] = (unsigned)ks * 3072u; }
  const unsigned wl = (unsigned)(2 * h) * 768u + q4 * 32u + p4 * 8u;
  const unsigned wa = 16384u + (unsigned)(2 * h) * 768u + q4 * 192u + nsub * 32u + p4 * 8u;
  const unsigned wbE = nsub * 12288u + wl + nsub * 128u;
  const unsigned wbO = nsub * 12288u + wl - nsub * 128u;
  const AttnSrc asrc = attn_src(rb2, hp2, w);
  bool early = false;
  auto op_at = [&](auto tc, auto kc) {
    constexpr int o = SC.op[decltype(tc)::value][decltype(kc)::value];
    if constexpr (o >= OP_ATT) attn_issue(rs_ws, asrc, w, lane, o - OP_ATT, early);
    else if constexpr (o >= 0) piece(o >> 3, o & 7);
  };
  bf16x8 sel;
  { const unsigned v = (lane & 15) == ((lane >> 4) & 1) ? ONES2 : 0u; sel = __builtin_bit_cast(bf16x8, u32x4{v, v, v, v}); }

  bf16x8 xf[32];
  constexpr int NB1 = NJ ? 1 : 2;
  f32x16 acc[3];
  f32x4 cs[3];
#pragma unroll
  for (int b = 0; b < 3; ++b) { cs[b] = f32x4{0.f, 0.f, 0.f, 0.f};
#pragma unroll
    for (int i = 0; i < 16; ++i) acc[b][i] = 0.f; }
  bf16x8 wf[3][NB1], wg[2][3];
  auto ldx = [&](int t) { xf[t] = lds_b128((unsigned)((t >> 2) % 5) * SLOT1 + xa[t & 3]); };
  auto ld1a = [&](int t, int b) {
    const unsigned a = (unsigned)((t >> 2) % 5) * SLOT1 + wa + ro[t & 3] + (NJ ? 2 : b) * 64u; wf[t % 3][b] = cat(lds_tr(a), lds_tr(a + 768u));
  };
  auto ld1b = [&](int t, int b) {
    const int u = NJ * 3 + b;
    const unsigned a = (unsigned)((8 + (t >> 2)) % 5) * SLOT1 + ((u & 1) ? wbO : wbE) + ro[t & 3] + u * 128u; wg[t & 1][b] = cat(lds_tr(a), lds_tr(a + 768u));
  };
  auto cs_put = [&](int nb, int b0) {
#pragma unroll
    for (int b = 0; b < 3; ++b) if (b < nb && lane < 16) { *(LAS float*)(lds + L_CSP + (((b0 + b) * 32 + lane) * 4 + mi) * 4) = cs[b][0]; *(LAS float*)(lds + L_CSP + (((b0 + b) * 32 + 16 + lane) * 4 + mi) * 4) = cs[b][1]; }
  };
  auto cs_get = [&](int nb, int b0) {
#pragma unroll
    for (int b = 0; b < 3; ++b) if (b < nb) { const f32x4 c4 = lds_f4(L_CSP + ((b0 + b) * 32 + r) * 16);
      *(LAS float*)(lds + L_CSW + w * 384 + b * 128 + r * 4) = (c4[0] + c4[1]) + (c4[2] + c4[3]); }
  };
  wait_vm<(L::NPRO - 1) * NA>();
  if (w == 0) { tie(ev); word_set(2, ev[0]); word_set(3, ev[1]); }
  bar();
  const u64 e0 = (u64)word_get(2) | ((u64)word_get(3) << 32);
  epoch = e0 + 1ull;
  tg.a = (unsigned)epoch; tg.b = ((unsigned)(e0 >> 32) ^ 0x5bd1e995u) + tg.a;
  ldx(0); ldx(1);
#pragma unroll
  for (int b = 0; b < NB1; ++b) { ld1a(0, b); ld1a(1, b); }
  u32x4 sv;
  float rmean = 0.f, rrstd = 0.f;
  const unsigned sv_off = WS_RS + (unsigned)(128 * rb + 32 * mi) * 16u;
  static_for<0, 32>([&](auto tc) {
    constexpr int t = decltype(tc)::value, s = t >> 2, p = t & 3;
    if constexpr (p == 2) {
      if (s == 7) cs_put(NB1, NJ ? 2 : 0);
      wait_vm<SC.wait_stage(t, s + 1)>();
      if (s == 7) bar(); else bar_raw();
      if (s == 7) cs_get(NB1, NJ ? 2 : 0);

    }
    if (NJ) op_at(tc, IC<0>{});
    static_for<0, NB1>([&](auto bc) {
      constexpr int b = decltype(bc)::value;
      acc[b] = mfma(wf[t % 3][b], xf[t], acc[b]);
      if (t < 30) { if (b == 0) ldx(t + 2); ld1a(t + 2, b); }
      if (t == 31) { ld1b(0, b); if (b == NB1 - 1) { for (int bb = NB1; bb < 3; ++bb) ld1b(0, bb); } }
      if (p == (NJ ? 1 : b)) cs[b] = mfma16(sel, wf[t % 3][b], cs[b]);
      if (b == 0) {
        if (t == 2 + NJ) {
          tie(xrow);
          float e[8];
#pragma unroll
          for (int i = 0; i < 4; ++i) { e[2 * i] = bf_lo(xrow[i]); e[2 * i + 1] = bf_hi(xrow[i]); }
          rmean = wave_sum(((e[0] + e[1]) + (e[2] + e[3])) + ((e[4] + e[5]) + (e[6] + e[7]))) * (1.f / 512.f);
          asm volatile("" : "+v"(rmean));
        }
        if (t == 4) {
          float q = 0.f;
#pragma unroll
          for (int i = 0; i < 4; ++i) { const float d0 = bf_lo(xrow[i]) - rmean, d1 = bf_hi(xrow[i]) - rmean; q = fmaf(d0, d0, q); q = fmaf(d1, d1, q); }
          rrstd = __builtin_amdgcn_rsqf(wave_sum(q) * (1.f / 512.f) + 1e-5f);
          asm volatile("" : "+v"(rrstd));
        }
      }
      SB0();
      if (b == 0) op_at(tc, IC<NJ ? 1 : 0>{});
    });
    if (!NJ) op_at(tc, IC<1>{});
    if (t == 22) sv = ld16c_asm(rs_ws, (unsigned)r * 16u, sv_off);
    if (t == 5 && lane == 0) st16c(u32x4{__float_as_uint(rmean), tg.a, __float_as_uint(rrstd), tg.b}, rs_ws, 0u, WS_RS + myrow * 16u);
    SB0();
  });
  { constexpr int after = SC.before[32] - L::NST - SC.before[23]; wait_vm_tie<after>(sv); }
  for (unsigned spins = 0; !__all(sv[1] == tg.a && sv[3] == tg.b) && spins < SPIN_CAP; ++spins) {
    __builtin_amdgcn_s_sleep(1);
    sv = ld16c_asm(rs_ws, (unsigned)r * 16u, sv_off); wait_vm_tie<0>(sv);
  }
  const float mean = __uint_as_float(sv[0]), rstd = __uint_as_float(sv[2]);
  const int sb = rb * 4 + mi;
#pragma unroll
  for (int b = 0; b < NB1; ++b) {
    const int blk = 3 * g + (NJ ? 2 : b), which = blk >> 4, hp = blk & 15;
    const float sc = which == 0 ? rstd * (0.25f * 1.4426950408889634f) : rstd;
    float v[16];
#pragma unroll
    for (int i = 0; i < 4; ++i) { const f32x4 c4 = lds_f4(L_CSW + w * 384 + b * 128 + (8 * i + 4 * h) * 4);
#pragma unroll
      for (int e = 0; e < 4; ++e) v[4 * i + e] = (acc[b][4 * i + e] - mean * c4[e]) * sc; }
    const unsigned vo = which == 2 ? (unsigned)(h * 512 + ((r ^ (4 * h)) * 16)) : (unsigned)(lane * 16);
#pragma unroll
    for (int hd = 0; hd < 2; ++hd) {
      const u32x4 o = {pk(v[8 * hd], v[8 * hd + 1]), pk(v[8 * hd + 2], v[8 * hd + 3]), pk(v[8 * hd + 4], v[8 * hd + 5]), pk(v[8 * hd + 6], v[8 * hd + 7])};
      st16c(o, rs_ws, vo, WS_QKV + (unsigned)((which * 32 + 2 * hp + hd) * 64 + sb) * 1024u);
    }
  }
#pragma unroll
  for (int b = 0; b < 3; ++b) { cs[b] = f32x4{0.f, 0.f, 0.f, 0.f};
#pragma unroll
    for (int i = 0; i < 16; ++i) acc[b][i] = 0.f; }
  bool act;
  const unsigned qv = qkv_flag_voff(rb2, hp2, lane, act);
  u32x4 fq;
  SB0();
  static_for<32, 64>([&](auto tc) {
    constexpr int Tg = decltype(tc)::value, t = Tg - 32, s = Tg >> 2, p = t & 3;
    if constexpr (p == 3) {
      if (s == 15) cs_put(3, NJ * 3);
      if constexpr (s == 9) wait_vm<(SC.before[Tg] - SC.before[32] < SC.wait_stage(Tg, 10) ? SC.before[Tg] - SC.before[32] : SC.wait_stage(Tg, 10))>();
      else if constexpr (s == ATT_B) { wait_vm<0>(); if (w == 0) { tie(fq); word_set(0, flags_ok(fq, act, tg) ? 1u : 0u); } }
      else if constexpr (s <= 14) wait_vm<SC.wait_stage(Tg, s + 1)>();
      if (s == ATT_B || s == 15) bar(); else bar_raw();

      if (s == ATT_B) early = word_get(0) != 0u;
      if (s == 15) cs_get(3, NJ * 3);
    }
    if (NJ) op_at(tc, IC<0>{});
    static_for<0, 3>([&](auto bc) {
      constexpr int b = decltype(bc)::value;
      acc[b] = mfma(wg[t & 1][b], xf[t], acc[b]);
      if (t < 31) ld1b(t + 1, b);
      if (p == ((b + 2 * NJ) & 3) || (s == 15 && p == 2 && ((b + 2 * NJ) & 3) == 3)) { if (!(s == 15 && p == 3)) cs[b] = mfma16(sel, wg[t & 1][b], cs[b]); }
      SB0();
    });
    if (!NJ) op_at(tc, IC<0>{});
    op_at(tc, IC<1>{});
    if (Tg == 39) flag_store(rs_ws, WS_FQKV + (unsigned)((rb * 16 + g) * 64 + w * 8), tg, lane);
    if (Tg == 4 * ATT_B - 1 && w == 0) fq = ld16c_asm(rs_ws, qv, 0u);
    SB0();
  });
  const float r2 = rstd * rstd;
  f32x4 cq[3][4];
#pragma unroll
  for (int b = 0; b < 3; ++b)
#pragma unroll
    for (int i = 0; i < 4; ++i) cq[b][i] = lds_f4(L_CSW + w * 384 + b * 128 + (8 * i + 4 * h) * 4);
#pragma unroll
  for (int b = 0; b < 3; ++b) {
    float v[16];
#pragma unroll
    for (int i = 0; i < 4; ++i)
#pragma unroll
      for (int e = 0; e < 4; ++e) v[4 * i + e] = acc[b][4 * i + e] - mean * cq[b][i][e];
    float hv[8];
#pragma unroll
    for (int i = 0; i < 8; ++i) hv[i] = fmaxf(v[i], 0.f) * v[8 + i] * r2;
    const u32x4 o = {pk(hv[0], hv[1]), pk(hv[2], hv[3]), pk(hv[4], hv[5]), pk(hv[6], hv[7])};
    st16c(o, rs_ws, (unsigned)lane * 16u + (NJ * 3 + b < nu ? 0u : 0x40000000u), WS_H + (unsigned)(sb * NU + u0 + NJ * 3 + b) * 1024u);
  }
  wait_vm<3>();
  return early;
}

template <bool FULL, class F1, class F2>
DI u32x4 attn_task(int j0, int qb, int hd, int lane, F1 side1, F2 side2) {
  const int q = lane & 31, hh = lane >> 5, tq = q - 4 * hh;
  const bf16x8 qf = lds_b128(LA_Q + (unsigned)(hd * 4 + qb) * 1024u + lane * 16u);
  const unsigned kbase = LA_K + (unsigned)(hd * 12 + qb) * 1024u + lane * 16u;
  const float NEG = -__builtin_inff();
  bf16x8 kf[9];
#pragma unroll
  for (int j = 0; j < 9; ++j) if (FULL || j >= j0) kf[j] = lds_b128(kbase + j * 1024u);
  f32x16 c;
#pragma unroll
  for (int i = 0; i < 16; ++i) c[i] = 0.f;
  auto on = [&](int j) { return FULL || j >= j0; };
  float m0 = NEG, m1 = NEG;
  f32x16 s[2];
  {
    const f32x16 s8 = mfma(kf[8], qf, c);
    f32x16 s0;
    if (FULL) s0 = mfma(kf[0], qf, c);
    if (on(1)) s[1] = mfma(kf[1], qf, c);
    SB0();
#pragma unroll
    for (int i = 0; i < 16; i += 2) {
      const int c0 = (i & 3) + 8 * (i >> 2);
      const float a = c0 <= tq ? s8[i] : (FULL ? s0[i] : NEG), b = c0 + 1 <= tq ? s8[i + 1] : (FULL ? s0[i + 1] : NEG);
      if (i & 2) m1 = fmaxf(fmaxf(m1, a), b); else m0 = fmaxf(fmaxf(m0, a), b);
    }
    side1(0);
    SB0();
  }
#pragma unroll
  for (int j = 1; j < 8; ++j) {
    if (j < 7 && on(j + 1)) s[(j + 1) & 1] = mfma(kf[j + 1], qf, c);
    SB0();
    if (on(j)) {
#pragma unroll
      for (int i = 0; i < 8; i += 2) { m0 = fmaxf(fmaxf(m0, s[j & 1][i]), s[j & 1][i + 1]); m1 = fmaxf(fmaxf(m1, s[j & 1][8 + i]), s[j & 1][9 + i]); }
    }
    side1(j);
    SB0();
  }
  const float m = xhalf_max(fmaxf(m0, m1));
#pragma unroll
  for (int i = 0; i < 16; ++i) c[i] = -m;
  const int grp = (lane >> 4) & 1, q4 = (lane & 15) >> 2, p4 = lane & 3;
  const unsigned vb = grp ? L_ONES + 128u : LA_V + (unsigned)(hd * 12 + qb) * 1024u + (unsigned)((p4 & 1) * 512 + (((4 * hh + q4) ^ (4 * (p4 & 1))) * 16) + (p4 >> 1) * 8);
  bf16x8 vf[2][2];
  auto ldv = [&](int j, int slot) {
#pragma unroll
    for (int u = 0; u < 2; ++u) vf[slot][u] = cat(lds_tr(vb + j * 1024u + u * 256u), lds_tr(vb + j * 1024u + u * 256u + 128u));
  };
  f32x16 acc;
#pragma unroll
  for (int i = 0; i < 16; ++i) acc[i] = 0.f;
  auto pv = [&](int slot, const unsigned (&pp)[8]) {
#pragma unroll
    for (int u = 0; u < 2; ++u) acc = mfma(vf[slot][u], __builtin_bit_cast(bf16x8, u32x4{pp[4 * u], pp[4 * u + 1], pp[4 * u + 2], pp[4 * u + 3]}), acc);
  };
  f32x16 s8, s0;
  if (on(1)) { s[1] = mfma(kf[1], qf, c); ldv(1, 1); }
#pragma unroll
  for (int j = 1; j < 8; ++j) {
    if (j < 7) { if (on(j + 1)) { s[(j + 1) & 1] = mfma(kf[j + 1], qf, c); ldv(j + 1, (j + 1) & 1); } }
    else { s8 = mfma(kf[8], qf, c); if (FULL) s0 = mfma(kf[0], qf, c); ldv(8, 0); }
    SB0();
    if (on(j)) {
      unsigned pp[8];
#pragma unroll
      for (int i = 0; i < 8; ++i) pp[i] = pk(__builtin_amdgcn_exp2f(s[j & 1][2 * i]), __builtin_amdgcn_exp2f(s[j & 1][2 * i + 1]));
      pv(j & 1, pp);
    }
    side2(j);
    SB0();
  }
  {
    if (FULL) ldv(0, 1);
    unsigned p8[8], p0[8];
#pragma unroll
    for (int i = 0; i < 8; ++i) {
      const int c0 = ((2 * i) & 3) + 8 * (i >> 1);
      const bool va = c0 <= tq, vb_ = c0 + 1 <= tq;
      const float a = va ? s8[2 * i] : (FULL ? s0[2 * i] : NEG), b = vb_ ? s8[2 * i + 1] : (FULL ? s0[2 * i + 1] : NEG);
      const unsigned pp = pk(__builtin_amdgcn_exp2f(a), __builtin_amdgcn_exp2f(b));
      const unsigned mk = (va ? 0xffffu : 0u) | (vb_ ? 0xffff0000u : 0u);
      p8[i] = pp & mk; p0[i] = pp & ~mk;
    }
    pv(0, p8);
    if (FULL) pv(1, p0);
  }
  const float inv = __builtin_amdgcn_rcpf(acc[8]);
  return u32x4{pk(acc[0] * inv, acc[1] * inv), pk(acc[2] * inv, acc[3] * inv), pk(acc[4] * inv, acc[5] * inv), pk(acc[6] * inv, acc[7] * inv)};
}

constexpr int H_FLAG_AT = 2;
constexpr int DW = 10, DA = 8, NK3 = 30, NDN = 22, OCHK = NDN - (DA - 1), OPOLL = OCHK - 5, XLD = 20;
struct Sched3 {
  int before[NK3 + 1] = {}, lastW[NK3] = {}, lastA[NK3] = {};
  constexpr Sched3() {
    int c = 0;
    for (int i = 0; i < DW - 1; ++i) { c += 2; lastW[i] = c; }
    for (int i = 0; i < DA - 1; ++i) { c += 2; lastA[i] = c; }
    for (int i = 0; i < NK3; ++i) {
      before[i] = c;
      if (i + DW - 1 < NK3) { c += 2; lastW[i + DW - 1] = c; }
      if (i + DA - 1 < NK3) { c += 2; lastA[i + DA - 1] = c; }
      if (i == XLD) c += 4;
    }
    before[NK3] = c;
  }
  constexpr int wait(int i) const { const int w = i + 1 < NK3 ? lastW[i + 1] : 0; return before[i] - (lastA[i] > w ? lastA[i] : w); }
};
DI unsigned l3w(int kq) { return kq < 3 ? 86016u + (unsigned)kq * (DW * 2048u) : 0u; }

DI void phase23(const Ptrs& P, bool early, int rb1, int g1, u64 epoch, int rb, int hp, int rb64, int cb, int rot, int w, int lane, Tag tg) {
  const rsrc_t rs_ws = mk_rsrc(P.ws, WS_BYTES);
  for (unsigned i = threadIdx.x; i < ONES_BYTES / 16; i += 512) *(LAS u32x4*)(lds + L_ONES + i * 16) = u32x4{ONES2, ONES2, ONES2, ONES2};
  if (!early) {
    bool act; const unsigned qv = qkv_flag_voff(rb, hp, lane, act);
    if (w == 0) flags_spin(rs_ws, qv, act, tg);
    bar();
    const AttnSrc asrc = attn_src(rb, hp, w);
#pragma unroll
    for (int i = 0; i < 7; ++i) attn_issue(rs_ws, asrc, w, lane, i, true);
    wait_vm<0>();
  }
  bar();
  const unsigned fh_off = WS_FH + (unsigned)((rb1 * 16 + g1) * 64 + w * 8);
  const int h = lane >> 5, nsub = (lane >> 4) & 1, q4 = (lane & 15) >> 2, p4 = lane & 3;
  f32x16 acc[4];
  u32x2 xr[4];
  const int kq = w - 4;
  const bool keeper = blockIdx.x == 0 && w == 4;
  u32x4 fe[4];
  const rsrc_t rs_d = mk_rsrc(P.wd, FF * DM * 2), rs_o = mk_rsrc(P.wo, DM * DM * 2);
  const int ulo = kq < 3 ? 22 * kq : 65, nun = kq < 2 ? 22 : 21;
  unsigned vw[2];
#pragma unroll
  for (int j = 0; j < 2; ++j) vw[j] = (unsigned)(8 * j + (lane >> 3)) * 1024u + (unsigned)(((lane & 7) ^ (((lane >> 4) & 1) << 2)) * 16);
  const unsigned ring = __builtin_amdgcn_readfirstlane(l3w(kq)), vo = (unsigned)lane * 16u;
  auto unit = [&](int i) { const int r = i + rot; return i < NDN ? (i < nun ? ulo + (r >= nun ? r - nun : r) : -1) : 8 * kq + ((i - NDN + rot) & 7); };
  auto reqW = [&](int i) {
    const int u = unit(i);
#pragma unroll
    for (int j = 0; j < 2; ++j) dma16(ring + (unsigned)(i % DW) * 2048u + j * 1024u, u < 0 ? 0x40000000u : vw[j], i < NDN ? rs_d : rs_o, (unsigned)cb * 128u + (unsigned)(u < 0 ? 0 : u) * 16384u);
  };
  const unsigned hv = WS_FH + (unsigned)((rb64 >> 1) * 1024 + lane * 16), ov = WS_FO + (unsigned)((rb64 >> 1) * 1024 + lane * 16);
  const int pg = lane >> 2;
  const bool pair_ok = ((lane & 3) & 1) == (rb64 & 1);
  const bool h_act = pair_ok && (((pg + 1) * NU) >> 4) > ulo && ((pg * NU) >> 4) < ulo + nun, o_act = pair_ok && (pg >> 2) == kq;
  u32x4 fh;
  if (w >= 4) __builtin_amdgcn_s_setprio(3);
  {
    const int qb = w & 3, hd = w >> 2, sb = 4 * rb + qb;
    auto side1 = [&](int j) {
      if (w >= 4 && j >= 1) reqW(j - 1);
    };
      auto side2 = [&](int j) {
      if (w >= 4 && j <= 2) reqW(6 + j);
      if (j == H_FLAG_AT) { if (w >= 4) wait_vm<18>(); else wait_vm<0>(); flag_store(rs_ws, fh_off, tg, lane); }
      if (w >= 4 && j == 7) fh = ld16c_asm(rs_ws, hv, 0u);
    };
    u32x4 o;
    if (sb >= 8) o = attn_task<true>(0, qb, hd, lane, side1, side2); else o = attn_task<false>(8 - sb, qb, hd, lane, side1, side2);
    st16c(o, rs_ws, (unsigned)lane * 16u, WS_O + (unsigned)(sb * 32 + 2 * hp + hd) * 1024u);
  }
  const unsigned fo_off = WS_FO + (unsigned)((rb * 16 + hp) * 64 + w * 8);
  if (w < 4) {
    wait_vm<0>();
    flag_store(rs_ws, fo_off, tg, lane);
  } else {
    wait_vm_tie<1>(fh);
    if (!flags_ok(fh, h_act, tg)) flags_spin(rs_ws, hv, h_act, tg);
    u32x4 A[DA][2];
    auto reqA = [&](int i) {
      const int u = unit(i);
#pragma unroll
      for (int mb = 0; mb < 2; ++mb)
        A[i % DA][mb] = ld16c_asm(rs_ws, u < 0 ? 0x40000000u : vo, i < NDN ? WS_H + (unsigned)((2 * rb64 + mb) * NU + (u < 0 ? 0 : u)) * 1024u : WS_O + (unsigned)((2 * rb64 + mb) * 32 + u) * 1024u);
    };
#pragma unroll
    for (int i = 0; i < DA - 1; ++i) reqA(i);
#pragma unroll
    for (int b = 0; b < 4; ++b)
#pragma unroll
      for (int i = 0; i < 16; ++i) acc[b][i] = 0.f;
    unsigned wa[2];
#pragma unroll
    for (int nb = 0; nb < 2; ++nb) wa[nb] = ring + (unsigned)h * 512u + q4 * 128u + (unsigned)((nb ^ (q4 >> 1)) * 64) + nsub * 32u + p4 * 8u;
    const int row = 64 * rb64 + 32 * (kq >> 1) + (lane & 31), col0 = 64 * cb + 32 * (kq & 1) + 4 * h;
    const unsigned short* xp = (const unsigned short*)P.x + (size_t)row * DM + col0;
    static constexpr Sched3 S3{};
    u32x4 fo;
    bf16x8 wf[2][2];
    auto ldw = [&](int i) {
#pragma unroll
      for (int nb = 0; nb < 2; ++nb) { const unsigned a = wa[nb] + (unsigned)(i % DW) * 2048u; wf[i & 1][nb] = cat(lds_tr(a), lds_tr(a + 1024u)); }
    };
    ldw(0);
    static_for<0, NK3>([&](auto ic) {
      constexpr int i = decltype(ic)::value;
      wait_vm<S3.wait(i)>();
      tie(A[i % DA][0]); tie(A[i % DA][1]);

      if constexpr (i == OCHK) {
        wait_vm_tie<S3.before[i] - S3.before[OPOLL + 1]>(fo);
        if (!flags_ok(fo, o_act, tg)) flags_spin(rs_ws, ov, o_act, tg);
      }
      SB0();
      acc[0] = mfma(wf[i & 1][0], __builtin_bit_cast(bf16x8, A[i % DA][0]), acc[0]);
      if constexpr (i + 1 < NK3) ldw(i + 1);
      SB0();
      acc[1] = mfma(wf[i & 1][1], __builtin_bit_cast(bf16x8, A[i % DA][0]), acc[1]);
      SB0();
      if constexpr (i + DW - 1 < NK3) reqW(i + DW - 1);
      acc[2] = mfma(wf[i & 1][0], __builtin_bit_cast(bf16x8, A[i % DA][1]), acc[2]);
      SB0();
      acc[3] = mfma(wf[i & 1][1], __builtin_bit_cast(bf16x8, A[i % DA][1]), acc[3]);
      SB0();
      if constexpr (i + DA - 1 < NK3) reqA(i + DA - 1);
      if (i == 1) flag_store(rs_ws, fo_off, tg, lane);
      if (i == OPOLL) fo = ld16c_asm(rs_ws, ov, 0u);
      if (i == XLD) {
#pragma unroll
        for (int q = 0; q < 4; ++q) xr[q] = *(const GAS u32x2*)(xp + 8 * q);
      }
      if (i == 22 && keeper) {
#pragma unroll
        for (int q = 0; q < 4; ++q) fe[q] = ld16c_asm(rs_ws, WS_FQKV + (unsigned)(q * 64 + lane) * 64u, 0u);
      }
      SB0();
    });
  }
  if (w >= 4) {
#pragma unroll
    for (int b = 0; b < 4; ++b) {
      if (b != kq) {
        const unsigned base = ring + (unsigned)((b - kq - 1 + 4) % 4) * 4096u + lane * 16u;
#pragma unroll
        for (int q = 0; q < 4; ++q) *(LAS f32x4*)(lds + base + q * 1024u) = f32x4{acc[b][4 * q], acc[b][4 * q + 1], acc[b][4 * q + 2], acc[b][4 * q + 3]};
      }
    }
  }
  bar();
  if (w >= 4) {
    f32x16 r = kq == 0 ? acc[0] : (kq == 1 ? acc[1] : (kq == 2 ? acc[2] : acc[3]));
#pragma unroll
    for (int s = 1; s < 4; ++s) {
      const int src = (kq + s) & 3;
      const unsigned base = l3w(src) + (unsigned)((kq - src - 1 + 4) % 4) * 4096u + lane * 16u;
#pragma unroll
      for (int q = 0; q < 4; ++q) { const f32x4 v = lds_f4(base + q * 1024u);
#pragma unroll
        for (int e = 0; e < 4; ++e) r[4 * q + e] += v[e]; }
    }
    const int row = 64 * rb64 + 32 * (kq >> 1) + (lane & 31), col0 = 64 * cb + 32 * (kq & 1);
    unsigned o[8];
#pragma unroll
    for (int q = 0; q < 4; ++q) { o[2 * q] = pk(r[4 * q] + bf_lo(xr[q][0]), r[4 * q + 1] + bf_hi(xr[q][0])); o[2 * q + 1] = pk(r[4 * q + 2] + bf_lo(xr[q][1]), r[4 * q + 3] + bf_hi(xr[q][1])); }
    unsigned short* op = (unsigned short*)P.out + (size_t)row * DM + col0 + 8 * h;
#pragma unroll
    for (int q = 0; q < 4; q += 2) {
      const auto t0 = __builtin_amdgcn_permlane32_swap(o[2 * q], o[2 * q + 2], false, false), t1 = __builtin_amdgcn_permlane32_swap(o[2 * q + 1], o[2 * q + 3], false, false);
      *(GAS u32x4*)(op + 8 * q) = u32x4{t0[0], t1[0], t0[1], t1[1]};
    }
    if (keeper) {
#pragma unroll
      for (int q = 0; q < 4; ++q) { tie(fe[q]); if (!flags_ok(fe[q], true, tg)) flags_spin(rs_ws, WS_FQKV + (unsigned)(q * 64 + lane) * 64u, true, tg); }
      if (lane == 0) __hip_atomic_store((GAS u64*)(P.ws + WS_EPOCH), epoch, __ATOMIC_RELAXED, __HIP_MEMORY_SCOPE_AGENT);
    }
  }
}

__global__ __launch_bounds__(512, 2) void mk_kernel(const void* x, const void* wqkv, const void* wo, const void* wg, const void* wu, const void* wd, void* out, char* ws) {
  u64 pc0 = 0;
  asm volatile("mk_code_begin:\n\ts_getpc_b64 %0" : "=s"(pc0));
  {
    unsigned len;
    asm volatile("s_mov_b32 %0, mk_code_end-mk_code_begin" : "=s"(len));
    const unsigned wv = __builtin_amdgcn_readfirstlane(threadIdx.x >> 6);
    const unsigned per = ((len >> 5) + 1023u) & ~1023u;
    const unsigned o = (blockIdx.x >> 3) * per + wv * 1024u;
    dma16(L_DUMMY + wv * 1024u, (threadIdx.x & 63u) * 16u, mk_rsrc((const void*)(pc0 & ~255ull), len & ~15u), wv * 1024u < per ? o : 0x40000000u);
  }
  const Ptrs P{x, wqkv, wo, wg, wu, wd, out, ws};
  __builtin_amdgcn_s_setprio(1);
  const int b = blockIdx.x, xcd = b & 7, idx = b >> 3;
  const int w = __builtin_amdgcn_readfirstlane(threadIdx.x >> 6), lane = threadIdx.x & 63;
  Tag tg; u64 epoch;
  const int rb = 2 * (idx >> 2) + (xcd >> 2), g = (xcd & 3) * 4 + (idx & 3), rot1 = ((idx >> 2) + 2 * (idx & 3)) & 7;
  bool early;
  if (w < 4) early = phase1<0>(P, rb, g, rot1, w, lane, rb, g, tg, epoch); else early = phase1<1>(P, rb, g, rot1, w - 4, lane, rb, g, tg, epoch);
  const int rot3 = ((idx & 7) * 2 + 5 * (idx >> 3)) % 21;
  phase23(P, early, rb, g, epoch, rb, g, xcd * 4 + (idx >> 3), idx & 7, rot3, w, lane, tg);
  asm volatile("mk_code_end:");
}

}

extern "C" void kernel_launch(void* const* d_in, const int* in_sizes, int n_in, void* d_out, int out_size, void* d_ws, size_t ws_size, hipStream_t stream) {
  const void *x = d_in[0], *wqkv = d_in[1], *wo = d_in[2], *wg = d_in[4], *wu = d_in[5], *wd = d_in[6];
  mk_kernel<<<256, 512, 0, stream>>>(x, wqkv, wo, wg, wu, wd, d_out, (char*)d_ws);
}
```

```cpp
#include <hip/hip_runtime.h>
#include <hip/hip_bf16.h>
#include <stdint.h>

namespace {

typedef short bf16x8 __attribute__((ext_vector_type(8)));
typedef short s16x4 __attribute__((ext_vector_type(4)));
typedef float f32x16 __attribute__((ext_vector_type(16)));
typedef float f32x4 __attribute__((ext_vector_type(4)));
typedef float f32x2 __attribute__((ext_vector_type(2)));
typedef unsigned u32x4 __attribute__((ext_vector_type(4)));
typedef unsigned u32x2 __attribute__((ext_vector_type(2)));
typedef __bf16 bf16x2_t __attribute__((ext_vector_type(2)));
typedef short v4i16_t __attribute__((ext_vector_type(4)));
typedef __amdgpu_buffer_rsrc_t rsrc_t;
#define LAS __attribute__((address_space(3)))
#define GAS __attribute__((address_space(1)))
#define DI __device__ __forceinline__
#define SB0() __builtin_amdgcn_sched_barrier(0)
typedef unsigned long long u64;

constexpr int SEQ = 2048, DM = 512, FF = 1376, NU = 86;
constexpr unsigned WS_EPOCH = 0;
constexpr unsigned WS_FQKV = 16384, WS_FH = 32768, WS_FO = 49152;
constexpr unsigned WS_RS = 65536;
constexpr unsigned WS_QKV = 1u << 20;
constexpr unsigned WS_H = 7u << 20;
constexpr unsigned WS_O = 13u << 20;
constexpr unsigned WS_BYTES = 16u << 20;
constexpr unsigned SLOT1 = 28672;
constexpr unsigned L_CSP = 143360;
constexpr unsigned L_CSW = 146432;
constexpr unsigned L_ONES = 152576;
constexpr unsigned ONES_BYTES = 9984;
constexpr unsigned L_WORD = 163584;
constexpr unsigned LDS_BYTES = 163840;
constexpr unsigned LA_Q = 28672, LA_K = LA_Q + 8192, LA_V = LA_K + 24576;

__shared__ __attribute__((aligned(1024))) char lds[LDS_BYTES];

DI unsigned pk(float a, float b) { f32x2 v = {a, b}; bf16x2_t r = __builtin_convertvector(v, bf16x2_t); return __builtin_bit_cast(unsigned, r); }
DI float dot2(unsigned a, unsigned b, float c) { return __builtin_amdgcn_fdot2_f32_bf16(__builtin_bit_cast(bf16x2_t, a), __builtin_bit_cast(bf16x2_t, b), c, false); }
DI rsrc_t mk_rsrc(const void* p, unsigned bytes) { return __builtin_amdgcn_make_buffer_rsrc((void*)p, 0, bytes, 0x00020000); }
#define LD_POL ""
#define ST_AUX 16
DI void dma16(unsigned ldsaddr, unsigned voff, rsrc_t rs, unsigned soff) {
  asm volatile("s_mov_b32 m0, %0\n\ts_nop 0\n\tbuffer_load_dwordx4 %1, %2, %3 offen" LD_POL " lds" ::"s"(ldsaddr), "v"(voff), "s"(rs), "s"(soff) : "memory");
}
DI void dma16c(unsigned ldsaddr, unsigned voff, rsrc_t rs, unsigned soff) {
  asm volatile("s_mov_b32 m0, %0\n\ts_nop 0\n\tbuffer_load_dwordx4 %1, %2, %3 offen sc1 lds" ::"s"(ldsaddr), "v"(voff), "s"(rs), "s"(soff) : "memory");
}
DI void pf_lines(unsigned ldsaddr, unsigned voff, rsrc_t rs) {
  asm volatile("s_mov_b32 m0, %0\n\ts_nop 0\n\tbuffer_load_dword %1, %2, 0 offen lds" ::"s"(ldsaddr), "v"(voff), "s"(rs) : "memory");
}
template <int N> DI void wait_vm() { asm volatile("s_waitcnt vmcnt(%0)" ::"n"(N) : "memory"); }
template <int N> DI void wait_vm_tie(u32x4& v) { asm volatile("s_waitcnt vmcnt(%1)" : "+v"(v) : "n"(N) : "memory"); }
template <int V> struct IC { static constexpr int value = V; };
template <int I, int N, class F> DI void static_for(F&& f) { if constexpr (I < N) { f(IC<I>{}); static_for<I + 1, N>(f); } }
DI void bar_raw() { asm volatile("s_barrier" ::: "memory"); }
DI void tie(u32x4& v) { asm volatile("" : "+v"(v) :: "memory"); }
DI void bar() { asm volatile("s_waitcnt lgkmcnt(0)\n\ts_barrier" ::: "memory"); }
DI f32x16 mfma(bf16x8 a, bf16x8 b, f32x16 c) { return __builtin_amdgcn_mfma_f32_32x32x16_bf16(a, b, c, 0, 0, 0); }
DI bf16x8 lds_b128(unsigned a) { return *(const LAS bf16x8*)(lds + a); }
#define MK_REAL 1
DI s16x4 lds_tr(unsigned a) { return __builtin_amdgcn_ds_read_tr16_b64_v4i16((LAS v4i16_t*)(lds + a)); }
DI f32x4 mfma16(bf16x8 a, bf16x8 b, f32x4 c) { return __builtin_amdgcn_mfma_f32_16x16x32_bf16(a, b, c, 0, 0, 0); }
DI f32x4 lds_f4(unsigned a) { return *(const LAS f32x4*)(lds + a); }
DI bf16x8 cat(s16x4 lo, s16x4 hi) { return __builtin_shufflevector(lo, hi, 0, 1, 2, 3, 4, 5, 6, 7); }
DI float xhalf_sum(float v) { auto r = __builtin_amdgcn_permlane32_swap(__float_as_uint(v), __float_as_uint(v), false, false); return __uint_as_float(r[0]) + __uint_as_float(r[1]); }
DI float xhalf_max(float v) { auto r = __builtin_amdgcn_permlane32_swap(__float_as_uint(v), __float_as_uint(v), false, false); return fmaxf(__uint_as_float(r[0]), __uint_as_float(r[1])); }
DI void st16c(u32x4 v, rsrc_t rs, unsigned voff, unsigned soff) { __builtin_amdgcn_raw_buffer_store_b128(v, rs, voff + soff, 0, ST_AUX); }
DI void st8c(u32x2 v, rsrc_t rs, unsigned voff, unsigned soff) { __builtin_amdgcn_raw_buffer_store_b64(v, rs, voff, soff, 16); }
constexpr unsigned ONES2 = 0x3F803F80u;
constexpr unsigned SPIN_CAP = 1u << 19;
template <int CTRL> DI float dpp_add(float v) { return v + __int_as_float(__builtin_amdgcn_update_dpp(0, __float_as_int(v), CTRL, 0xf, 0xf, true)); }
DI float wave_sum(float v) {
  v = dpp_add<0xB1>(v); v = dpp_add<0x4E>(v); v = dpp_add<0x141>(v); v = dpp_add<0x140>(v);
  auto a = __builtin_amdgcn_permlane16_swap(__float_as_uint(v), __float_as_uint(v), false, false);
  v = __uint_as_float(a[0]) + __uint_as_float(a[1]);
  return xhalf_sum(v);
}
DI float bf_lo(unsigned u) { return __uint_as_float(u << 16); }
DI float bf_hi(unsigned u) { return __uint_as_float(u & 0xffff0000u); }
DI u32x4 ld16_asm(rsrc_t rs, unsigned voff, unsigned soff) { u32x4 v; asm volatile("buffer_load_dwordx4 %0, %1, %2, %3 offen" : "=v"(v) : "v"(voff), "s"(rs), "s"(soff) : "memory"); return v; }
DI u32x4 ld16c_asm(rsrc_t rs, unsigned voff, unsigned soff) { u32x4 v; asm volatile("buffer_load_dwordx4 %0, %1, %2, %3 offen sc1" : "=v"(v) : "v"(voff), "s"(rs), "s"(soff) : "memory"); return v; }
#define STAMP2(i) do {} while (0)
struct Stamps {};
#define STAMP(i) do {} while (0)
#define CSTAMP(i) do {} while (0)
#define NOTE(i, v) do {} while (0)

struct Ptrs { const void *x, *wqkv, *wo, *wg, *wu, *wd; void* out; char* ws; u64 pc; };
struct Tag { unsigned a, b; };

DI void flag_store(rsrc_t rs_ws, unsigned off, Tag tg, int lane) { if (lane == 0) st8c(u32x2{tg.a, tg.b}, rs_ws, 0u, off); }
DI bool flags_ok(u32x4 v, bool act, Tag tg) { return __all(!act || (v[0] == tg.a && v[1] == tg.b && v[2] == tg.a && v[3] == tg.b)); }
DI void flags_spin(rsrc_t rs_ws, unsigned voff, bool act, Tag tg) {
  for (unsigned spins = 0; spins < SPIN_CAP; ++spins) {
    u32x4 v = ld16c_asm(rs_ws, voff, 0u); wait_vm_tie<0>(v);
    if (flags_ok(v, act, tg)) break;
    __builtin_amdgcn_s_sleep(1);
  }
}
DI void word_set(int i, unsigned v) { *(volatile LAS unsigned*)(lds + L_WORD + i * 4) = v; }
DI unsigned word_get(int i) { return __builtin_amdgcn_readfirstlane(*(volatile LAS unsigned*)(lds + L_WORD + i * 4)); }

struct AttnSrc { unsigned q, kv; int sb0; };
DI AttnSrc attn_src(int rb, int hp, int w) {
  AttnSrc a;
  a.q = __builtin_amdgcn_readfirstlane(WS_QKV + (unsigned)((2 * hp + (w >> 2)) * 64 + 4 * rb + (w & 3)) * 1024u);
  a.kv = __builtin_amdgcn_readfirstlane(WS_QKV + (unsigned)(((1 + (w >> 2)) * 32 + 2 * hp + ((w >> 1) & 1)) * 64) * 1024u);
  a.sb0 = __builtin_amdgcn_readfirstlane(4 * rb - 8 + 6 * (w & 1));
  return a;
}
DI void attn_issue(rsrc_t rs_ws, const AttnSrc& a, int w, int lane, int piece, bool go) {
  const unsigned vo = (unsigned)lane * 16u + (go ? 0u : 0x40000000u);
  if (piece == 0) { dma16c(LA_Q + (unsigned)w * 1024u, vo, rs_ws, a.q); return; }
  const int sbk = a.sb0 + piece - 1;
  dma16c(LA_K + (unsigned)((w >> 1) * 12 + 6 * (w & 1) + piece - 1) * 1024u, vo, rs_ws, a.kv + (unsigned)(sbk < 0 ? 0 : sbk) * 1024u);
}
DI unsigned qkv_flag_voff(int rb, int hp, int lane, bool& act) {
  const int c = lane >> 2, kind = c == 0 ? 0 : (c <= 3 ? 1 : 2), d = c == 0 ? 0 : (c - 1) % 3, rr = rb - d;
  act = lane < 28 && rr >= 0;
  return act ? WS_FQKV + (unsigned)((rr * 16 + (kind * 16 + hp) / 3) * 64 + (lane & 3) * 16) : 0u;
}

constexpr unsigned L_DUMMY = 152576;
#define TLBW_N 0
#define PFX_N 0
#define PF3_N 0
#define PF3_T 33
#define P0_T 99
#define P1_NPRO 3
#define ATT_B 14
#define P1_HOLD 12
constexpr int OP_NONE = -1, OP_PFW = 200, OP_ATT = 400;
template <int NJ> struct Sched1 {
  static constexpr int NA = NJ ? 4 : 3, NST = NJ ? 2 : 4;
  static constexpr int n(int sg) { return sg < 8 ? NA : 3; }
  static constexpr int TB(int s) { return s < 8 ? 4 * s + 2 : 4 * s + 3; }
  static constexpr int extras(int T) { return (T == 5 ? 1 : 0)   + (T == 22 ? 1 : 0)   + (T == 39 ? 1 : 0)  ; }
  static constexpr int NPRO = P1_NPRO;
  static constexpr int NTW = TLBW_N;
  static constexpr int NPX = PFX_N;
  static constexpr int PRO = 1 + NPX + NPRO * NA + 1 + NTW;
  static constexpr int HOLD = P1_HOLD;
  int op[64][2] = {}, before[65] = {}, last[16] = {};
  constexpr Sched1() {
    int sg = NPRO, j = 0, pf = 0, att = 0, c = PRO;
    last[0] = 1 + NPX + NA;
    for (int i = 1; i < NPRO; ++i) last[i] = 1 + NPX + NA + 1 + NTW + i * NA;
    for (int T = 0; T < 64; ++T) {
      before[T] = c;
      for (int k = 0; k < 2; ++k) {
        int o = OP_NONE;
        if (k == 0 || T < 14) {
          if (sg <= 15 && (sg < 5 || T >= TB(sg - 4)) && (sg < 8 || T >= 4 * sg - HOLD)) {   o = sg * 8 + j; if (++j == n(sg)) { last[sg] = c + 1; ++sg; j = 0; } }
          else if (pf == 0 && T >= P0_T) o = OP_PFW + pf++;
          else if (pf >= 1 && pf < 1 + PF3_N && T >= PF3_T) o = OP_PFW + pf++;
        }
        if (T >= 4 * ATT_B + 3 && att < 7 && (k == 0 || T >= 61)) o = OP_ATT + att++;
        op[T][k] = o;
        if (o != OP_NONE) ++c;
      }
      c += extras(T);
      if (T == 31) c += NST;
    }
    before[64] = c;
  }
  constexpr int wait_stage(int T, int sg) const { return before[T] - last[sg]; }
};

template <int NJ>
DI bool phase1(const Ptrs& P, int rb, int g, int rot, int mi, int lane, int rb2, int hp2, Tag& tg, u64& epoch, Stamps& T_) {
  Stamps& T = T_;
  typedef Sched1<NJ> L;
  static constexpr Sched1<NJ> SC{};
  const int w = NJ * 4 + mi;
  const rsrc_t rs_x = mk_rsrc(P.x, SEQ * DM * 2), rs_q = mk_rsrc(P.wqkv, DM * 1536 * 2);
  const rsrc_t rs_f = mk_rsrc(NJ ? P.wu : P.wg, DM * FF * 2), rs_ws = mk_rsrc(P.ws, WS_BYTES);
  u32x4 ev;
  if (w == 0) ev = ld16c_asm(rs_ws, 0u, WS_EPOCH);
  const unsigned sx0 = (unsigned)rb * 131072u, sq0 = (unsigned)g * 192u;
  constexpr int NQ = NJ ? 2 : 1;
  constexpr int NA = 2 + NQ;
  unsigned vx[2], vq[2], vf[3];
#pragma unroll
  for (int j = 0; j < 2; ++j) vx[j] = (unsigned)(16 * w + 8 * j + (lane >> 3)) * 1024u + (unsigned)(((lane & 7) ^ (4 * j + (lane >> 4))) * 16);
#pragma unroll
  for (int j = 0; j < NQ; ++j) { const int ins = NJ ? 2 * mi + j : 8 + mi; const int G = ins * 64 + lane;
    vq[j] = (unsigned)(G / 12) * 3072u + (unsigned)(G % 12) * 16u; }
  const int u0 = (g * NU) >> 4, nu = (((g + 1) * NU) >> 4) - u0;
  const unsigned sf0 = (unsigned)u0 * 32u;
  auto piece = [&](int gs, int j) {
    const unsigned slot = (unsigned)(gs % 5) * SLOT1;
    const unsigned c = (unsigned)(gs + rot) & 7u;
    if (gs < 8) {
      if (j < 2) dma16(slot + (unsigned)(2 * w + j) * 1024u, vx[j], rs_x, sx0 + c * 128u);
      else dma16(slot + 16384u + (unsigned)(NJ ? 2 * mi + j - 2 : 8 + mi) * 1024u, vq[j - 2], rs_q, sq0 + c * (64u * 3072u));
    } else dma16(slot + NJ * 12288u + (unsigned)(3 * mi + j) * 1024u, vf[j], rs_f, sf0 + c * (64u * 2752u));
  };
#pragma unroll
  for (int j = 0; j < NA; ++j) piece(0, j);
  const unsigned myrow = (unsigned)(128 * rb + 8 * g + w);
  u32x4 xrow = ld16_asm(rs_x, (unsigned)lane * 16u, myrow * 1024u);
#pragma unroll
  for (int sg = 1; sg < L::NPRO; ++sg)
#pragma unroll
    for (int j = 0; j < NA; ++j) piece(sg, j);
  SB0();
  STAMP(1);
#pragma unroll
  for (int j = 0; j < 3; ++j) { const int G = (3 * mi + j) * 64 + lane, kg = G / 48, rr = G % 48, unit = (rr >> 3) ^ NJ;
    vf[j] = unit < nu ? (unsigned)(4 * kg + ((rr & 7) >> 1)) * 2752u + (unsigned)(unit * 32 + (rr & 1) * 16) : 0x40000000u; }
  const int r = lane & 31, h = lane >> 5, nsub = (lane >> 4) & 1, q4 = (lane & 15) >> 2, p4 = lane & 3;
  unsigned xa[4], ro[4];
#pragma unroll
  for (int p = 0; p < 4; ++p) { const int ks = (p + mi) & 3;
    xa[p] = (unsigned)(32 * mi + r) * 128u + (unsigned)(((2 * ks + h) ^ ((r >> 1) & 7)) * 16);
    ro[p] = (unsigned)ks * 3072u; }
  const unsigned wl = (unsigned)(2 * h) * 768u + q4 * 32u + p4 * 8u;
  const unsigned wa = 16384u + (unsigned)(2 * h) * 768u + q4 * 192u + nsub * 32u + p4 * 8u;
  const unsigned wbE = nsub * 12288u + wl + nsub * 128u;
  const unsigned wbO = nsub * 12288u + wl - nsub * 128u;
  const AttnSrc asrc = attn_src(rb2, hp2, w);
  bool early = false;
  auto warm = [&](int i) {
    const unsigned xl = blockIdx.x >> 3, dst = L_DUMMY + (unsigned)w * 256u;
    if (i == 0) {
      const unsigned q = (unsigned)(w & 1) * 64u + (unsigned)lane, rr = q / 7u, ln = q - rr * 7u;
      const unsigned ub = (unsigned)((((blockIdx.x & 3) * 4 * NU) >> 4) * 32) & ~127u;
      pf_lines(dst, (w < 4 && q < 112u) ? (16u * xl + rr) * 2752u + ub + ln * 128u : 0x40000000u, mk_rsrc((w & 2) ? P.wu : P.wg, DM * FF * 2));
    } else {
      const unsigned q = xl * 8u + (unsigned)w + 256u * (unsigned)(i - 1);
      dma16(L_DUMMY + 2048u + (unsigned)w * 1024u, (unsigned)lane * 16u + (q < 1888u ? 0u : 0x40000000u), mk_rsrc(q < 1376u ? P.wd : P.wo, q < 1376u ? FF * DM * 2 : DM * DM * 2), (q < 1376u ? q : q - 1376u) * 1024u);
    }
  };
  auto op_at = [&](auto tc, auto kc) {
    constexpr int o = SC.op[decltype(tc)::value][decltype(kc)::value];
    if constexpr (o >= OP_ATT) attn_issue(rs_ws, asrc, w, lane, o - OP_ATT, early);
    else if constexpr (o >= OP_PFW) warm(o - OP_PFW);
    else if constexpr (o >= 0) piece(o >> 3, o & 7);
  };
  bf16x8 sel;
  { const unsigned v = (lane & 15) == ((lane >> 4) & 1) ? ONES2 : 0u; sel = __builtin_bit_cast(bf16x8, u32x4{v, v, v, v}); }

  bf16x8 xf[32];
  constexpr int NB1 = NJ ? 1 : 2;
  f32x16 acc[3];
  f32x4 cs[3];
#pragma unroll
  for (int b = 0; b < 3; ++b) { cs[b] = f32x4{0.f, 0.f, 0.f, 0.f};
#pragma unroll
    for (int i = 0; i < 16; ++i) acc[b][i] = 0.f; }
  bf16x8 wf[3][NB1], wg[2][3];
  auto ldx = [&](int t) { xf[t] = lds_b128((unsigned)((t >> 2) % 5) * SLOT1 + xa[t & 3]); };
  auto ld1a = [&](int t, int b) {
    const unsigned a = (unsigned)((t >> 2) % 5) * SLOT1 + wa + ro[t & 3] + (NJ ? 2 : b) * 64u; wf[t % 3][b] = cat(lds_tr(a), lds_tr(a + 768u));
  };
  auto ld1b = [&](int t, int b) {
    const int u = NJ * 3 + b;
    const unsigned a = (unsigned)((8 + (t >> 2)) % 5) * SLOT1 + ((u & 1) ? wbO : wbE) + ro[t & 3] + u * 128u; wg[t & 1][b] = cat(lds_tr(a), lds_tr(a + 768u));
  };
  auto cs_put = [&](int nb, int b0) {
#pragma unroll
    for (int b = 0; b < 3; ++b) if (b < nb && lane < 16) { *(LAS float*)(lds + L_CSP + (((b0 + b) * 32 + lane) * 4 + mi) * 4) = cs[b][0]; *(LAS float*)(lds + L_CSP + (((b0 + b) * 32 + 16 + lane) * 4 + mi) * 4) = cs[b][1]; }
  };
  auto cs_get = [&](int nb, int b0) {
#pragma unroll
    for (int b = 0; b < 3; ++b) if (b < nb) { const f32x4 c4 = lds_f4(L_CSP + ((b0 + b) * 32 + r) * 16);
      *(LAS float*)(lds + L_CSW + w * 384 + b * 128 + r * 4) = (c4[0] + c4[1]) + (c4[2] + c4[3]); }
  };
  wait_vm<(L::NPRO - 1) * NA + L::NTW>();
  if (w == 0) { tie(ev); word_set(2, ev[0]); word_set(3, ev[1]); }
  bar();
  const u64 e0 = (u64)word_get(2) | ((u64)word_get(3) << 32);
  epoch = e0 + 1ull;
  tg.a = (unsigned)epoch; tg.b = ((unsigned)(e0 >> 32) ^ 0x5bd1e995u) + tg.a;
  STAMP(2); CSTAMP(23);
  ldx(0); ldx(1);
#pragma unroll
  for (int b = 0; b < NB1; ++b) { ld1a(0, b); ld1a(1, b); }
  u32x4 sv;
  float rmean = 0.f, rrstd = 0.f;
  const unsigned sv_off = WS_RS + (unsigned)(128 * rb + 32 * mi) * 16u;
  static_for<0, 32>([&](auto tc) {
    constexpr int t = decltype(tc)::value, s = t >> 2, p = t & 3;
    if constexpr (p == 2) {
      if (s == 7) cs_put(NB1, NJ ? 2 : 0);
      wait_vm<SC.wait_stage(t, s + 1)>();
      if (s == 7) bar(); else bar_raw();
      STAMP2(24 + s);
      if (s == 7) cs_get(NB1, NJ ? 2 : 0);
      if (s == 1) CSTAMP(60); if (s == 4) CSTAMP(61); if (s == 7) CSTAMP(62);
    }
    if (NJ) op_at(tc, IC<0>{});
    static_for<0, NB1>([&](auto bc) {
      constexpr int b = decltype(bc)::value;
      acc[b] = mfma(wf[t % 3][b], xf[t], acc[b]);
      if (t < 30) { if (b == 0) ldx(t + 2); ld1a(t + 2, b); }
      if (t == 31) { ld1b(0, b); if (b == NB1 - 1) { for (int bb = NB1; bb < 3; ++bb) ld1b(0, bb); } }
      if (p == (NJ ? 1 : b)) cs[b] = mfma16(sel, wf[t % 3][b], cs[b]);
      if (b == 0) {
        if (t == 2 + NJ) {
          tie(xrow);
          float e[8];
#pragma unroll
          for (int i = 0; i < 4; ++i) { e[2 * i] = bf_lo(xrow[i]); e[2 * i + 1] = bf_hi(xrow[i]); }
          rmean = wave_sum(((e[0] + e[1]) + (e[2] + e[3])) + ((e[4] + e[5]) + (e[6] + e[7]))) * (1.f / 512.f);
          asm volatile("" : "+v"(rmean));
        }
        if (t == 4) {
          float q = 0.f;
#pragma unroll
          for (int i = 0; i < 4; ++i) { const float d0 = bf_lo(xrow[i]) - rmean, d1 = bf_hi(xrow[i]) - rmean; q = fmaf(d0, d0, q); q = fmaf(d1, d1, q); }
          rrstd = __builtin_amdgcn_rsqf(wave_sum(q) * (1.f / 512.f) + 1e-5f);
          asm volatile("" : "+v"(rrstd));
        }
      }
      SB0();
      if (b == 0) op_at(tc, IC<NJ ? 1 : 0>{});
    });
    if (!NJ) op_at(tc, IC<1>{});
    if (t == 22) sv = ld16c_asm(rs_ws, (unsigned)r * 16u, sv_off);
    if (t == 5 && lane == 0) st16c(u32x4{__float_as_uint(rmean), tg.a, __float_as_uint(rrstd), tg.b}, rs_ws, 0u, WS_RS + myrow * 16u);
    SB0();
  });
  STAMP(3); CSTAMP(22);
  { constexpr int after = SC.before[32] - L::NST - SC.before[23]; wait_vm_tie<after>(sv); }
  NOTE(53, __all(sv[1] == tg.a && sv[3] == tg.b) ? 1 : 2);
  for (unsigned spins = 0; !__all(sv[1] == tg.a && sv[3] == tg.b) && spins < SPIN_CAP; ++spins) {
    __builtin_amdgcn_s_sleep(1);
    sv = ld16c_asm(rs_ws, (unsigned)r * 16u, sv_off); wait_vm_tie<0>(sv);
  }
  const float mean = __uint_as_float(sv[0]), rstd = __uint_as_float(sv[2]);
  const int sb = rb * 4 + mi;
#pragma unroll
  for (int b = 0; b < NB1; ++b) {
    const int blk = 3 * g + (NJ ? 2 : b), which = blk >> 4, hp = blk & 15;
    const float sc = which == 0 ? rstd * (0.25f * 1.4426950408889634f) : rstd;
    float v[16];
#pragma unroll
    for (int i = 0; i < 4; ++i) { const f32x4 c4 = lds_f4(L_CSW + w * 384 + b * 128 + (8 * i + 4 * h) * 4);
#pragma unroll
      for (int e = 0; e < 4; ++e) v[4 * i + e] = (acc[b][4 * i + e] - mean * c4[e]) * sc; }
    const unsigned vo = which == 2 ? (unsigned)(h * 512 + ((r ^ (4 * h)) * 16)) : (unsigned)(lane * 16);
#pragma unroll
    for (int hd = 0; hd < 2; ++hd) {
      const u32x4 o = {pk(v[8 * hd], v[8 * hd + 1]), pk(v[8 * hd + 2], v[8 * hd + 3]), pk(v[8 * hd + 4], v[8 * hd + 5]), pk(v[8 * hd + 6], v[8 * hd + 7])};
      st16c(o, rs_ws, vo, WS_QKV + (unsigned)((which * 32 + 2 * hp + hd) * 64 + sb) * 1024u);
    }
  }
  STAMP(4); CSTAMP(18);
#pragma unroll
  for (int b = 0; b < 3; ++b) { cs[b] = f32x4{0.f, 0.f, 0.f, 0.f};
#pragma unroll
    for (int i = 0; i < 16; ++i) acc[b][i] = 0.f; }
  bool act;
  const unsigned qv = qkv_flag_voff(rb2, hp2, lane, act);
  u32x4 fq;
  SB0();
  static_for<32, 64>([&](auto tc) {
    constexpr int Tg = decltype(tc)::value, t = Tg - 32, s = Tg >> 2, p = t & 3;
    if constexpr (p == 3) {
      if (s == 15) cs_put(3, NJ * 3);
      if constexpr (s == 9) wait_vm<(SC.before[Tg] - SC.before[32] < SC.wait_stage(Tg, 10) ? SC.before[Tg] - SC.before[32] : SC.wait_stage(Tg, 10))>();
      else if constexpr (s == ATT_B) { wait_vm<0>(); if (w == 0) { tie(fq); word_set(0, flags_ok(fq, act, tg) ? 1u : 0u); } }
      else if constexpr (s <= 14) wait_vm<SC.wait_stage(Tg, s + 1)>();
      if (s == ATT_B || s == 15) bar(); else bar_raw();
      STAMP2(24 + s);
      if (s == 8) CSTAMP(56); if (s == 11) CSTAMP(57); if (s == 13) CSTAMP(58); if (s == 15) CSTAMP(59);
      if (s == ATT_B) early = word_get(0) != 0u;
      if (s == 15) cs_get(3, NJ * 3);
    }
    if (NJ) op_at(tc, IC<0>{});
    static_for<0, 3>([&](auto bc) {
      constexpr int b = decltype(bc)::value;
      acc[b] = mfma(wg[t & 1][b], xf[t], acc[b]);
      if (t < 31) ld1b(t + 1, b);
      if (p == ((b + 2 * NJ) & 3) || (s == 15 && p == 2 && ((b + 2 * NJ) & 3) == 3)) { if (!(s == 15 && p == 3)) cs[b] = mfma16(sel, wg[t & 1][b], cs[b]); }
      SB0();
    });
    if (!NJ) op_at(tc, IC<0>{});
    op_at(tc, IC<1>{});
    if (Tg == 39) flag_store(rs_ws, WS_FQKV + (unsigned)((rb * 16 + g) * 64 + w * 8), tg, lane);
    if (Tg == 4 * ATT_B - 1 && w == 0) fq = ld16c_asm(rs_ws, qv, 0u);
    SB0();
  });
  STAMP(5); CSTAMP(19);
  const float r2 = rstd * rstd;
  f32x4 cq[3][4];
#pragma unroll
  for (int b = 0; b < 3; ++b)
#pragma unroll
    for (int i = 0; i < 4; ++i) cq[b][i] = lds_f4(L_CSW + w * 384 + b * 128 + (8 * i + 4 * h) * 4);
#pragma unroll
  for (int b = 0; b < 3; ++b) {
    float v[16];
#pragma unroll
    for (int i = 0; i < 4; ++i)
#pragma unroll
      for (int e = 0; e < 4; ++e) v[4 * i + e] = acc[b][4 * i + e] - mean * cq[b][i][e];
    float hv[8];
#pragma unroll
    for (int i = 0; i < 8; ++i) hv[i] = fmaxf(v[i], 0.f) * v[8 + i] * r2;
    const u32x4 o = {pk(hv[0], hv[1]), pk(hv[2], hv[3]), pk(hv[4], hv[5]), pk(hv[6], hv[7])};
    st16c(o, rs_ws, (unsigned)lane * 16u + (NJ * 3 + b < nu ? 0u : 0x40000000u), WS_H + (unsigned)(sb * NU + u0 + NJ * 3 + b) * 1024u);
  }
  STAMP(6);
  wait_vm<3>();
  return early;
}

template <bool FULL, class F1, class FM, class F2>
DI u32x4 attn_task(int j0, int qb, int hd, int lane, F1 side1, FM mid, F2 side2) {
  const int q = lane & 31, hh = lane >> 5, tq = q - 4 * hh;
  const bf16x8 qf = lds_b128(LA_Q + (unsigned)(hd * 4 + qb) * 1024u + lane * 16u);
  const unsigned kbase = LA_K + (unsigned)(hd * 12 + qb) * 1024u + lane * 16u;
  const float NEG = -__builtin_inff();
  bf16x8 kf[9];
#pragma unroll
  for (int j = 0; j < 9; ++j) if (FULL || j >= j0) kf[j] = lds_b128(kbase + j * 1024u);
  f32x16 c;
#pragma unroll
  for (int i = 0; i < 16; ++i) c[i] = 0.f;
  auto on = [&](int j) { return FULL || j >= j0; };
  float m0 = NEG, m1 = NEG;
  f32x16 s[2];
  {
    const f32x16 s8 = mfma(kf[8], qf, c);
    f32x16 s0;
    if (FULL) s0 = mfma(kf[0], qf, c);
    if (on(1)) s[1] = mfma(kf[1], qf, c);
    SB0();
#pragma unroll
    for (int i = 0; i < 16; i += 2) {
      const int c0 = (i & 3) + 8 * (i >> 2);
      const float a = c0 <= tq ? s8[i] : (FULL ? s0[i] : NEG), b = c0 + 1 <= tq ? s8[i + 1] : (FULL ? s0[i + 1] : NEG);
      if (i & 2) m1 = fmaxf(fmaxf(m1, a), b); else m0 = fmaxf(fmaxf(m0, a), b);
    }
    side1(0);
    SB0();
  }
#pragma unroll
  for (int j = 1; j < 8; ++j) {
    if (j < 7 && on(j + 1)) s[(j + 1) & 1] = mfma(kf[j + 1], qf, c);
    SB0();
    if (on(j)) {
#pragma unroll
      for (int i = 0; i < 8; i += 2) { m0 = fmaxf(fmaxf(m0, s[j & 1][i]), s[j & 1][i + 1]); m1 = fmaxf(fmaxf(m1, s[j & 1][8 + i]), s[j & 1][9 + i]); }
    }
    side1(j);
    SB0();
  }
  const float m = xhalf_max(fmaxf(m0, m1));
  mid();
#pragma unroll
  for (int i = 0; i < 16; ++i) c[i] = -m;
  const int grp = (lane >> 4) & 1, q4 = (lane & 15) >> 2, p4 = lane & 3;
  const unsigned vb = grp ? L_ONES + 128u : LA_V + (unsigned)(hd * 12 + qb) * 1024u + (unsigned)((p4 & 1) * 512 + (((4 * hh + q4) ^ (4 * (p4 & 1))) * 16) + (p4 >> 1) * 8);
  bf16x8 vf[2][2];
  auto ldv = [&](int j, int slot) {
#pragma unroll
    for (int u = 0; u < 2; ++u) vf[slot][u] = cat(lds_tr(vb + j * 1024u + u * 256u), lds_tr(vb + j * 1024u + u * 256u + 128u));
  };
  f32x16 acc;
#pragma unroll
  for (int i = 0; i < 16; ++i) acc[i] = 0.f;
  auto pv = [&](int slot, const unsigned (&pp)[8]) {
#pragma unroll
    for (int u = 0; u < 2; ++u) acc = mfma(vf[slot][u], __builtin_bit_cast(bf16x8, u32x4{pp[4 * u], pp[4 * u + 1], pp[4 * u + 2], pp[4 * u + 3]}), acc);
  };
  f32x16 s8, s0;
  if (on(1)) { s[1] = mfma(kf[1], qf, c); ldv(1, 1); }
#pragma unroll
  for (int j = 1; j < 8; ++j) {
    if (j < 7) { if (on(j + 1)) { s[(j + 1) & 1] = mfma(kf[j + 1], qf, c); ldv(j + 1, (j + 1) & 1); } }
    else { s8 = mfma(kf[8], qf, c); if (FULL) s0 = mfma(kf[0], qf, c); ldv(8, 0); }
    SB0();
    if (on(j)) {
      unsigned pp[8];
#pragma unroll
      for (int i = 0; i < 8; ++i) pp[i] = pk(__builtin_amdgcn_exp2f(s[j & 1][2 * i]), __builtin_amdgcn_exp2f(s[j & 1][2 * i + 1]));
      pv(j & 1, pp);
    }
    side2(j);
    SB0();
  }
  {
    if (FULL) ldv(0, 1);
    unsigned p8[8], p0[8];
#pragma unroll
    for (int i = 0; i < 8; ++i) {
      const int c0 = ((2 * i) & 3) + 8 * (i >> 1);
      const bool va = c0 <= tq, vb_ = c0 + 1 <= tq;
      const float a = va ? s8[2 * i] : (FULL ? s0[2 * i] : NEG), b = vb_ ? s8[2 * i + 1] : (FULL ? s0[2 * i + 1] : NEG);
      const unsigned pp = pk(__builtin_amdgcn_exp2f(a), __builtin_amdgcn_exp2f(b));
      const unsigned mk = (va ? 0xffffu : 0u) | (vb_ ? 0xffff0000u : 0u);
      p8[i] = pp & mk; p0[i] = pp & ~mk;
    }
    pv(0, p8);
    if (FULL) pv(1, p0);
  }
  const float inv = __builtin_amdgcn_rcpf(acc[8]);
  return u32x4{pk(acc[0] * inv, acc[1] * inv), pk(acc[2] * inv, acc[3] * inv), pk(acc[4] * inv, acc[5] * inv), pk(acc[6] * inv, acc[7] * inv)};
}

#define H_FLAG_AT 2
constexpr int DW = 10, DA = 8, NK3 = 30, NDN = 22, OCHK = NDN - (DA - 1), OPOLL = OCHK - 5, XLD = 20;
struct Sched3 {
  int before[NK3 + 1] = {}, lastW[NK3] = {}, lastA[NK3] = {};
  constexpr Sched3() {
    int c = 0;
    for (int i = 0; i < DW - 1; ++i) { c += 2; lastW[i] = c; }
    for (int i = 0; i < DA - 1; ++i) { c += 2; lastA[i] = c; }
    for (int i = 0; i < NK3; ++i) {
      before[i] = c;
      if (i + DW - 1 < NK3) { c += 2; lastW[i + DW - 1] = c; }
      if (i + DA - 1 < NK3) { c += 2; lastA[i + DA - 1] = c; }
      if (i == XLD) c += 4;
    }
    before[NK3] = c;
  }
  constexpr int wait(int i) const { const int w = i + 1 < NK3 ? lastW[i + 1] : 0; return before[i] - (lastA[i] > w ? lastA[i] : w); }
};
DI unsigned l3w(int kq) { return kq < 3 ? 86016u + (unsigned)kq * (DW * 2048u) : 0u; }

DI void phase23(const Ptrs& P, bool early, int rb1, int g1, u64 epoch, int rb, int hp, int rb64, int cb, int rot, int w, int lane, Tag tg, Stamps& T) {
  const rsrc_t rs_ws = mk_rsrc(P.ws, WS_BYTES);
  for (unsigned i = threadIdx.x; i < ONES_BYTES / 16; i += 512) *(LAS u32x4*)(lds + L_ONES + i * 16) = u32x4{ONES2, ONES2, ONES2, ONES2};
  NOTE(63, early ? 1 : 2);
  if (!early) {
    bool act; const unsigned qv = qkv_flag_voff(rb, hp, lane, act);
    if (w == 0) flags_spin(rs_ws, qv, act, tg);
    bar();
    const AttnSrc asrc = attn_src(rb, hp, w);
#pragma unroll
    for (int i = 0; i < 7; ++i) attn_issue(rs_ws, asrc, w, lane, i, true);
    wait_vm<0>();
  }
  bar();
  STAMP(9);
  const unsigned fh_off = WS_FH + (unsigned)((rb1 * 16 + g1) * 64 + w * 8);
  const int h = lane >> 5, nsub = (lane >> 4) & 1, q4 = (lane & 15) >> 2, p4 = lane & 3;
  f32x16 acc[4];
  u32x2 xr[4];
  const int kq = w - 4;
  const bool keeper = blockIdx.x == 0 && w == 4;
  u32x4 fe[4];
  const rsrc_t rs_d = mk_rsrc(P.wd, FF * DM * 2), rs_o = mk_rsrc(P.wo, DM * DM * 2);
  const int ulo = kq < 3 ? 22 * kq : 65, nun = kq < 2 ? 22 : 21;
  unsigned vw[2];
#pragma unroll
  for (int j = 0; j < 2; ++j) vw[j] = (unsigned)(8 * j + (lane >> 3)) * 1024u + (unsigned)(((lane & 7) ^ (((lane >> 4) & 1) << 2)) * 16);
  const unsigned ring = __builtin_amdgcn_readfirstlane(l3w(kq)), vo = (unsigned)lane * 16u;
  auto unit = [&](int i) { const int r = i + rot; return i < NDN ? (i < nun ? ulo + (r >= nun ? r - nun : r) : -1) : 8 * kq + ((i - NDN + rot) & 7); };
  auto reqW = [&](int i) {
    const int u = unit(i);
#pragma unroll
    for (int j = 0; j < 2; ++j) dma16(ring + (unsigned)(i % DW) * 2048u + j * 1024u, u < 0 ? 0x40000000u : vw[j], i < NDN ? rs_d : rs_o, (unsigned)cb * 128u + (unsigned)(u < 0 ? 0 : u) * 16384u);
  };
  const unsigned hv = WS_FH + (unsigned)((rb64 >> 1) * 1024 + lane * 16), ov = WS_FO + (unsigned)((rb64 >> 1) * 1024 + lane * 16);
  const int pg = lane >> 2;
  const bool pair_ok = ((lane & 3) & 1) == (rb64 & 1);
  const bool h_act = pair_ok && (((pg + 1) * NU) >> 4) > ulo && ((pg * NU) >> 4) < ulo + nun, o_act = pair_ok && (pg >> 2) == kq;
  u32x4 fh;
  if (w >= 4) __builtin_amdgcn_s_setprio(3);
  {
    const int qb = w & 3, hd = w >> 2, sb = 4 * rb + qb;
    auto side1 = [&](int j) {
      if (w >= 4 && j >= 1) reqW(j - 1);
    };
    auto mid = [&]() { STAMP(10); };
    auto side2 = [&](int j) {
      if (w >= 4 && j <= 2) reqW(6 + j);
      if (j == H_FLAG_AT) { if (w >= 4) wait_vm<18>(); else wait_vm<0>(); flag_store(rs_ws, fh_off, tg, lane); }
      if (w >= 4 && j == 7) fh = ld16c_asm(rs_ws, hv, 0u);
    };
    u32x4 o;
    if (sb >= 8) o = attn_task<true>(0, qb, hd, lane, side1, mid, side2); else o = attn_task<false>(8 - sb, qb, hd, lane, side1, mid, side2);
    st16c(o, rs_ws, (unsigned)lane * 16u, WS_O + (unsigned)(sb * 32 + 2 * hp + hd) * 1024u);
    STAMP(11);
  }
  const unsigned fo_off = WS_FO + (unsigned)((rb * 16 + hp) * 64 + w * 8);
  if (w < 4) {
    wait_vm<0>();
    flag_store(rs_ws, fo_off, tg, lane);
    STAMP(12);
  } else {
    wait_vm_tie<1>(fh);
    NOTE(55, flags_ok(fh, h_act, tg) ? 1 : 2);
    if (!flags_ok(fh, h_act, tg)) flags_spin(rs_ws, hv, h_act, tg);
    STAMP(13);
    u32x4 A[DA][2];
    auto reqA = [&](int i) {
      const int u = unit(i);
#pragma unroll
      for (int mb = 0; mb < 2; ++mb)
        A[i % DA][mb] = ld16c_asm(rs_ws, u < 0 ? 0x40000000u : vo, i < NDN ? WS_H + (unsigned)((2 * rb64 + mb) * NU + (u < 0 ? 0 : u)) * 1024u : WS_O + (unsigned)((2 * rb64 + mb) * 32 + u) * 1024u);
    };
#pragma unroll
    for (int i = 0; i < DA - 1; ++i) reqA(i);
#pragma unroll
    for (int b = 0; b < 4; ++b)
#pragma unroll
      for (int i = 0; i < 16; ++i) acc[b][i] = 0.f;
    unsigned wa[2];
#pragma unroll
    for (int nb = 0; nb < 2; ++nb) wa[nb] = ring + (unsigned)h * 512u + q4 * 128u + (unsigned)((nb ^ (q4 >> 1)) * 64) + nsub * 32u + p4 * 8u;
    const int row = 64 * rb64 + 32 * (kq >> 1) + (lane & 31), col0 = 64 * cb + 32 * (kq & 1) + 4 * h;
    const unsigned short* xp = (const unsigned short*)P.x + (size_t)row * DM + col0;
    static constexpr Sched3 S3{};
    u32x4 fo;
    bf16x8 wf[2][2];
    auto ldw = [&](int i) {
#pragma unroll
      for (int nb = 0; nb < 2; ++nb) { const unsigned a = wa[nb] + (unsigned)(i % DW) * 2048u; wf[i & 1][nb] = cat(lds_tr(a), lds_tr(a + 1024u)); }
    };
    ldw(0);
    static_for<0, NK3>([&](auto ic) {
      constexpr int i = decltype(ic)::value;
      wait_vm<S3.wait(i)>();
      tie(A[i % DA][0]); tie(A[i % DA][1]);
      if (i == 0) STAMP(14);
      if constexpr (i == OCHK) {
        wait_vm_tie<S3.before[i] - S3.before[OPOLL + 1]>(fo);
        NOTE(54, flags_ok(fo, o_act, tg) ? 1 : 2);
        if (!flags_ok(fo, o_act, tg)) flags_spin(rs_ws, ov, o_act, tg);
      }
      SB0();
      acc[0] = mfma(wf[i & 1][0], __builtin_bit_cast(bf16x8, A[i % DA][0]), acc[0]);
      if constexpr (i + 1 < NK3) ldw(i + 1);
      SB0();
      acc[1] = mfma(wf[i & 1][1], __builtin_bit_cast(bf16x8, A[i % DA][0]), acc[1]);
      SB0();
      if constexpr (i + DW - 1 < NK3) reqW(i + DW - 1);
      acc[2] = mfma(wf[i & 1][0], __builtin_bit_cast(bf16x8, A[i % DA][1]), acc[2]);
      SB0();
      acc[3] = mfma(wf[i & 1][1], __builtin_bit_cast(bf16x8, A[i % DA][1]), acc[3]);
      SB0();
      if constexpr (i + DA - 1 < NK3) reqA(i + DA - 1);
      if (i == 1) flag_store(rs_ws, fo_off, tg, lane);
      if (i == OPOLL) fo = ld16c_asm(rs_ws, ov, 0u);
      if (i == XLD) {
#pragma unroll
        for (int q = 0; q < 4; ++q) xr[q] = *(const GAS u32x2*)(xp + 8 * q);
      }
      if (i == 22 && keeper) {
#pragma unroll
        for (int q = 0; q < 4; ++q) fe[q] = ld16c_asm(rs_ws, WS_FQKV + (unsigned)(q * 64 + lane) * 64u, 0u);
      }
      SB0();
    });
    STAMP(15);
  }
  if (w >= 4) {
#pragma unroll
    for (int b = 0; b < 4; ++b) {
      if (b != kq) {
        const unsigned base = ring + (unsigned)((b - kq - 1 + 4) % 4) * 4096u + lane * 16u;
#pragma unroll
        for (int q = 0; q < 4; ++q) *(LAS f32x4*)(lds + base + q * 1024u) = f32x4{acc[b][4 * q], acc[b][4 * q + 1], acc[b][4 * q + 2], acc[b][4 * q + 3]};
      }
    }
  }
  bar();
  if (w >= 4) {
    f32x16 r = kq == 0 ? acc[0] : (kq == 1 ? acc[1] : (kq == 2 ? acc[2] : acc[3]));
#pragma unroll
    for (int s = 1; s < 4; ++s) {
      const int src = (kq + s) & 3;
      const unsigned base = l3w(src) + (unsigned)((kq - src - 1 + 4) % 4) * 4096u + lane * 16u;
#pragma unroll
      for (int q = 0; q < 4; ++q) { const f32x4 v = lds_f4(base + q * 1024u);
#pragma unroll
        for (int e = 0; e < 4; ++e) r[4 * q + e] += v[e]; }
    }
    const int row = 64 * rb64 + 32 * (kq >> 1) + (lane & 31), col0 = 64 * cb + 32 * (kq & 1);
    unsigned o[8];
#pragma unroll
    for (int q = 0; q < 4; ++q) { o[2 * q] = pk(r[4 * q] + bf_lo(xr[q][0]), r[4 * q + 1] + bf_hi(xr[q][0])); o[2 * q + 1] = pk(r[4 * q + 2] + bf_lo(xr[q][1]), r[4 * q + 3] + bf_hi(xr[q][1])); }
    unsigned short* op = (unsigned short*)P.out + (size_t)row * DM + col0 + 8 * h;
#pragma unroll
    for (int q = 0; q < 4; q += 2) {
      const auto t0 = __builtin_amdgcn_permlane32_swap(o[2 * q], o[2 * q + 2], false, false), t1 = __builtin_amdgcn_permlane32_swap(o[2 * q + 1], o[2 * q + 3], false, false);
      *(GAS u32x4*)(op + 8 * q) = u32x4{t0[0], t1[0], t0[1], t1[1]};
    }
    if (keeper) {
#pragma unroll
      for (int q = 0; q < 4; ++q) { tie(fe[q]); if (!flags_ok(fe[q], true, tg)) flags_spin(rs_ws, WS_FQKV + (unsigned)(q * 64 + lane) * 64u, true, tg); }
      if (lane == 0) __hip_atomic_store((GAS u64*)(P.ws + WS_EPOCH), epoch, __ATOMIC_RELAXED, __HIP_MEMORY_SCOPE_AGENT);
    }
  }
}

__global__ __launch_bounds__(512, 2) void mk_kernel(const void* x, const void* wqkv, const void* wo, const void* wg, const void* wu, const void* wd, void* out, char* ws) {
  u64 pc0 = 0;
  asm volatile("mk_code_begin:\n\ts_getpc_b64 %0" : "=s"(pc0));
  {
    unsigned len;
    asm volatile("s_mov_b32 %0, mk_code_end-mk_code_begin" : "=s"(len));
    const unsigned wv = __builtin_amdgcn_readfirstlane(threadIdx.x >> 6);
    const unsigned per = ((len >> 5) + 1023u) & ~1023u;
    const unsigned o = (blockIdx.x >> 3) * per + wv * 1024u;
    dma16(L_DUMMY + wv * 1024u, (threadIdx.x & 63u) * 16u, mk_rsrc((const void*)(pc0 & ~255ull), len & ~15u), wv * 1024u < per ? o : 0x40000000u);
  }
  const Ptrs P{x, wqkv, wo, wg, wu, wd, out, ws, pc0};
  Stamps T;
  STAMP(0); CSTAMP(20);
  __builtin_amdgcn_s_setprio(1);
  const int b = blockIdx.x, xcd = b & 7, idx = b >> 3;
  const int w = __builtin_amdgcn_readfirstlane(threadIdx.x >> 6), lane = threadIdx.x & 63;
  Tag tg; u64 epoch;
  const int rb = 2 * (idx >> 2) + (xcd >> 2), g = (xcd & 3) * 4 + (idx & 3), rot1 = ((idx >> 2) + 2 * (idx & 3)) & 7;
  bool early;
  if (w < 4) early = phase1<0>(P, rb, g, rot1, w, lane, rb, g, tg, epoch, T); else early = phase1<1>(P, rb, g, rot1, w - 4, lane, rb, g, tg, epoch, T);
  const int rot3 = ((idx & 7) * 2 + 5 * (idx >> 3)) % 21;
  phase23(P, early, rb, g, epoch, rb, g, xcd * 4 + (idx >> 3), idx & 7, rot3, w, lane, tg, T);
  asm volatile("mk_code_end:");
}

}

extern "C" void kernel_launch(void* const* d_in, const int* in_sizes, int n_in, void* d_out, int out_size, void* d_ws, size_t ws_size, hipStream_t stream) {
  const void *x = d_in[0], *wqkv = d_in[1], *wo = d_in[2], *wg = d_in[4], *wu = d_in[5], *wd = d_in[6];
  char* ws = (char*)d_ws + ((ws_size / 2) & ~(size_t)((2u << 20) - 1));
  mk_kernel<<<256, 512, 0, stream>>>(x, wqkv, wo, wg, wu, wd, d_out, ws);
}
```

```cpp
#include <hip/hip_runtime.h>
#include <hip/hip_bf16.h>
#include <stdint.h>

namespace {

typedef short bf16x8 __attribute__((ext_vector_type(8)));
typedef short s16x4 __attribute__((ext_vector_type(4)));
typedef float f32x16 __attribute__((ext_vector_type(16)));
typedef float f32x4 __attribute__((ext_vector_type(4)));
typedef float f32x2 __attribute__((ext_vector_type(2)));
typedef unsigned u32x4 __attribute__((ext_vector_type(4)));
typedef unsigned u32x2 __attribute__((ext_vector_type(2)));
typedef __bf16 bf16x2_t __attribute__((ext_vector_type(2)));
typedef short v4i16_t __attribute__((ext_vector_type(4)));
typedef __amdgpu_buffer_rsrc_t rsrc_t;
#define LAS __attribute__((address_space(3)))
#define GAS __attribute__((address_space(1)))
#define DI __device__ __forceinline__
#define SB0() __builtin_amdgcn_sched_barrier(0)
typedef unsigned long long u64;

constexpr int SEQ = 2048, DM = 512, FF = 1376, NU = 86;
constexpr unsigned WS_EPOCH = 0;
constexpr unsigned WS_FQKV = 16384, WS_FH = 32768, WS_FO = 49152;
constexpr unsigned WS_RS = 65536;
constexpr unsigned WS_QKV = 1u << 20;
constexpr unsigned WS_H = 7u << 20;
constexpr unsigned WS_O = 13u << 20;
constexpr unsigned WS_BYTES = 16u << 20;
constexpr unsigned SLOT1 = 28672;
constexpr unsigned L_CSP = 143360;
constexpr unsigned L_CSW = 146432;
constexpr unsigned L_ONES = 152576;
constexpr unsigned ONES_BYTES = 9984;
constexpr unsigned L_WORD = 163584;
constexpr unsigned LDS_BYTES = 163840;
constexpr unsigned LA_Q = 28672, LA_K = LA_Q + 8192, LA_V = LA_K + 24576;

__shared__ __attribute__((aligned(1024))) char lds[LDS_BYTES];

DI unsigned pk(float a, float b) { f32x2 v = {a, b}; bf16x2_t r = __builtin_convertvector(v, bf16x2_t); return __builtin_bit_cast(unsigned, r); }
DI float dot2(unsigned a, unsigned b, float c) { return __builtin_amdgcn_fdot2_f32_bf16(__builtin_bit_cast(bf16x2_t, a), __builtin_bit_cast(bf16x2_t, b), c, false); }
DI rsrc_t mk_rsrc(const void* p, unsigned bytes) { return __builtin_amdgcn_make_buffer_rsrc((void*)p, 0, bytes, 0x00020000); }
#define LD_POL ""
#define ST_AUX 16
DI void dma16(unsigned ldsaddr, unsigned voff, rsrc_t rs, unsigned soff) {
  asm volatile("s_mov_b32 m0, %0\n\ts_nop 0\n\tbuffer_load_dwordx4 %1, %2, %3 offen" LD_POL " lds" ::"s"(ldsaddr), "v"(voff), "s"(rs), "s"(soff) : "memory");
}
DI void dma16c(unsigned ldsaddr, unsigned voff, rsrc_t rs, unsigned soff) {
  asm volatile("s_mov_b32 m0, %0\n\ts_nop 0\n\tbuffer_load_dwordx4 %1, %2, %3 offen sc1 lds" ::"s"(ldsaddr), "v"(voff), "s"(rs), "s"(soff) : "memory");
}
DI void pf_lines(unsigned ldsaddr, unsigned voff, rsrc_t rs) {
  asm volatile("s_mov_b32 m0, %0\n\ts_nop 0\n\tbuffer_load_dword %1, %2, 0 offen lds" ::"s"(ldsaddr), "v"(voff), "s"(rs) : "memory");
}
template <int N> DI void wait_vm() { asm volatile("s_waitcnt vmcnt(%0)" ::"n"(N) : "memory"); }
template <int N> DI void wait_vm_tie(u32x4& v) { asm volatile("s_waitcnt vmcnt(%1)" : "+v"(v) : "n"(N) : "memory"); }
template <int V> struct IC { static constexpr int value = V; };
template <int I, int N, class F> DI void static_for(F&& f) { if constexpr (I < N) { f(IC<I>{}); static_for<I + 1, N>(f); } }
DI void bar_raw() { asm volatile("s_barrier" ::: "memory"); }
DI void tie(u32x4& v) { asm volatile("" : "+v"(v) :: "memory"); }
DI void bar() { asm volatile("s_waitcnt lgkmcnt(0)\n\ts_barrier" ::: "memory"); }
DI f32x16 mfma(bf16x8 a, bf16x8 b, f32x16 c) { return __builtin_amdgcn_mfma_f32_32x32x16_bf16(a, b, c, 0, 0, 0); }
DI bf16x8 lds_b128(unsigned a) { return *(const LAS bf16x8*)(lds + a); }
#define MK_REAL 1
DI s16x4 lds_tr(unsigned a) { return __builtin_amdgcn_ds_read_tr16_b64_v4i16((LAS v4i16_t*)(lds + a)); }
DI f32x4 mfma16(bf16x8 a, bf16x8 b, f32x4 c) { return __builtin_amdgcn_mfma_f32_16x16x32_bf16(a, b, c, 0, 0, 0); }
DI f32x4 lds_f4(unsigned a) { return *(const LAS f32x4*)(lds + a); }
DI bf16x8 cat(s16x4 lo, s16x4 hi) { return __builtin_shufflevector(lo, hi, 0, 1, 2, 3, 4, 5, 6, 7); }
DI float xhalf_sum(float v) { auto r = __builtin_amdgcn_permlane32_swap(__float_as_uint(v), __float_as_uint(v), false, false); return __uint_as_float(r[0]) + __uint_as_float(r[1]); }
DI float xhalf_max(float v) { auto r = __builtin_amdgcn_permlane32_swap(__float_as_uint(v), __float_as_uint(v), false, false); return fmaxf(__uint_as_float(r[0]), __uint_as_float(r[1])); }
DI void st16c(u32x4 v, rsrc_t rs, unsigned voff, unsigned soff) { __builtin_amdgcn_raw_buffer_store_b128(v, rs, voff + soff, 0, ST_AUX); }
DI void st8c(u32x2 v, rsrc_t rs, unsigned voff, unsigned soff) { __builtin_amdgcn_raw_buffer_store_b64(v, rs, voff, soff, 16); }
constexpr unsigned ONES2 = 0x3F803F80u;
constexpr unsigned SPIN_CAP = 1u << 19;
template <int CTRL> DI float dpp_add(float v) { return v + __int_as_float(__builtin_amdgcn_update_dpp(0, __float_as_int(v), CTRL, 0xf, 0xf, true)); }
DI float wave_sum(float v) {
  v = dpp_add<0xB1>(v); v = dpp_add<0x4E>(v); v = dpp_add<0x141>(v); v = dpp_add<0x140>(v);
  auto a = __builtin_amdgcn_permlane16_swap(__float_as_uint(v), __float_as_uint(v), false, false);
  v = __uint_as_float(a[0]) + __uint_as_float(a[1]);
  return xhalf_sum(v);
}
DI float bf_lo(unsigned u) { return __uint_as_float(u << 16); }
DI float bf_hi(unsigned u) { return __uint_as_float(u & 0xffff0000u); }
DI u32x4 ld16_asm(rsrc_t rs, unsigned voff, unsigned soff) { u32x4 v; asm volatile("buffer_load_dwordx4 %0, %1, %2, %3 offen" : "=v"(v) : "v"(voff), "s"(rs), "s"(soff) : "memory"); return v; }
DI u32x4 ld16c_asm(rsrc_t rs, unsigned voff, unsigned soff) { u32x4 v; asm volatile("buffer_load_dwordx4 %0, %1, %2, %3 offen sc1" : "=v"(v) : "v"(voff), "s"(rs), "s"(soff) : "memory"); return v; }
#define STAMP2(i) do {} while (0)
struct Stamps {};
#define STAMP(i) do {} while (0)
#define CSTAMP(i) do {} while (0)
#define NOTE(i, v) do {} while (0)

struct Ptrs { const void *x, *wqkv, *wo, *wg, *wu, *wd; void* out; char* ws; u64 pc; };
struct Tag { unsigned a, b; };

DI void flag_store(rsrc_t rs_ws, unsigned off, Tag tg, int lane) { if (lane == 0) st8c(u32x2{tg.a, tg.b}, rs_ws, 0u, off); }
DI bool flags_ok(u32x4 v, bool act, Tag tg) { return __all(!act || (v[0] == tg.a && v[1] == tg.b && v[2] == tg.a && v[3] == tg.b)); }
DI void flags_spin(rsrc_t rs_ws, unsigned voff, bool act, Tag tg) {
  for (unsigned spins = 0; spins < SPIN_CAP; ++spins) {
    u32x4 v = ld16c_asm(rs_ws, voff, 0u); wait_vm_tie<0>(v);
    if (flags_ok(v, act, tg)) break;
    __builtin_amdgcn_s_sleep(1);
  }
}
DI void word_set(int i, unsigned v) { *(volatile LAS unsigned*)(lds + L_WORD + i * 4) = v; }
DI unsigned word_get(int i) { return __builtin_amdgcn_readfirstlane(*(volatile LAS unsigned*)(lds + L_WORD + i * 4)); }

struct AttnSrc { unsigned q, kv; int sb0; };
DI AttnSrc attn_src(int rb, int hp, int w) {
  AttnSrc a;
  a.q = __builtin_amdgcn_readfirstlane(WS_QKV + (unsigned)((2 * hp + (w >> 2)) * 64 + 4 * rb + (w & 3)) * 1024u);
  a.kv = __builtin_amdgcn_readfirstlane(WS_QKV + (unsigned)(((1 + (w >> 2)) * 32 + 2 * hp + ((w >> 1) & 1)) * 64) * 1024u);
  a.sb0 = __builtin_amdgcn_readfirstlane(4 * rb - 8 + 6 * (w & 1));
  return a;
}
DI void attn_issue(rsrc_t rs_ws, const AttnSrc& a, int w, int lane, int piece, bool go) {
  const unsigned vo = (unsigned)lane * 16u + (go ? 0u : 0x40000000u);
  if (piece == 0) { dma16c(LA_Q + (unsigned)w * 1024u, vo, rs_ws, a.q); return; }
  const int sbk = a.sb0 + piece - 1;
  dma16c(LA_K + (unsigned)((w >> 1) * 12 + 6 * (w & 1) + piece - 1) * 1024u, vo, rs_ws, a.kv + (unsigned)(sbk < 0 ? 0 : sbk) * 1024u);
}
DI unsigned qkv_flag_voff(int rb, int hp, int lane, bool& act) {
  const int c = lane >> 2, kind = c == 0 ? 0 : (c <= 3 ? 1 : 2), d = c == 0 ? 0 : (c - 1) % 3, rr = rb - d;
  act = lane < 28 && rr >= 0;
  return act ? WS_FQKV + (unsigned)((rr * 16 + (kind * 16 + hp) / 3) * 64 + (lane & 3) * 16) : 0u;
}

constexpr unsigned L_DUMMY = 152576;
#define TLBW_N 0
#define PFX_N 0
#define PF3_N 0
#define PF3_T 33
#define P0_T 99
#define P1_NPRO 3
#define ATT_B 14
#define P1_HOLD 12
constexpr int OP_NONE = -1, OP_PFW = 200, OP_ATT = 400;
template <int NJ> struct Sched1 {
  static constexpr int NA = NJ ? 4 : 3, NST = NJ ? 2 : 4;
  static constexpr int n(int sg) { return sg < 8 ? NA : 3; }
  static constexpr int TB(int s) { return s < 8 ? 4 * s + 2 : 4 * s + 3; }
  static constexpr int extras(int T) { return (T == 5 ? 1 : 0)   + (T == 22 ? 1 : 0)   + (T == 39 ? 1 : 0)  ; }
  static constexpr int NPRO = P1_NPRO;
  static constexpr int NTW = TLBW_N;
  static constexpr int NPX = PFX_N;
  static constexpr int PRO = 1 + NPX + NPRO * NA + 1 + NTW;
  static constexpr int HOLD = P1_HOLD;
  int op[64][2] = {}, before[65] = {}, last[16] = {};
  constexpr Sched1() {
    int sg = NPRO, j = 0, pf = 0, att = 0, c = PRO;
    last[0] = 1 + NPX + NA;
    for (int i = 1; i < NPRO; ++i) last[i] = 1 + NPX + NA + 1 + NTW + i * NA;
    for (int T = 0; T < 64; ++T) {
      before[T] = c;
      for (int k = 0; k < 2; ++k) {
        int o = OP_NONE;
        if (k == 0 || T < 14) {
          if (sg <= 15 && (sg < 5 || T >= TB(sg - 4)) && (sg < 8 || T >= 4 * sg - HOLD)) {   o = sg * 8 + j; if (++j == n(sg)) { last[sg] = c + 1; ++sg; j = 0; } }
          else if (pf == 0 && T >= P0_T) o = OP_PFW + pf++;
          else if (pf >= 1 && pf < 1 + PF3_N && T >= PF3_T) o = OP_PFW + pf++;
        }
        if (T >= 4 * ATT_B + 3 && att < 7 && (k == 0 || T >= 61)) o = OP_ATT + att++;
        op[T][k] = o;
        if (o != OP_NONE) ++c;
      }
      c += extras(T);
      if (T == 31) c += NST;
    }
    before[64] = c;
  }
  constexpr int wait_stage(int T, int sg) const { return before[T] - last[sg]; }
};

template <int NJ>
DI bool phase1(const Ptrs& P, int rb, int g, int rot, int mi, int lane, int rb2, int hp2, Tag& tg, u64& epoch, Stamps& T_) {
  Stamps& T = T_;
  typedef Sched1<NJ> L;
  static constexpr Sched1<NJ> SC{};
  const int w = NJ * 4 + mi;
  const rsrc_t rs_x = mk_rsrc(P.x, SEQ * DM * 2), rs_q = mk_rsrc(P.wqkv, DM * 1536 * 2);
  const rsrc_t rs_f = mk_rsrc(NJ ? P.wu : P.wg, DM * FF * 2), rs_ws = mk_rsrc(P.ws, WS_BYTES);
  u32x4 ev;
  if (w == 0) ev = ld16c_asm(rs_ws, 0u, WS_EPOCH);
  const unsigned sx0 = (unsigned)rb * 131072u, sq0 = (unsigned)g * 192u;
  constexpr int NQ = NJ ? 2 : 1;
  constexpr int NA = 2 + NQ;
  unsigned vx[2], vq[2], vf[3];
#pragma unroll
  for (int j = 0; j < 2; ++j) vx[j] = (unsigned)(16 * w + 8 * j + (lane >> 3)) * 1024u + (unsigned)(((lane & 7) ^ (4 * j + (lane >> 4))) * 16);
#pragma unroll
  for (int j = 0; j < NQ; ++j) { const int ins = NJ ? 2 * mi + j : 8 + mi; const int G = ins * 64 + lane;
    vq[j] = (unsigned)(G / 12) * 3072u + (unsigned)(G % 12) * 16u; }
  const int u0 = (g * NU) >> 4, nu = (((g + 1) * NU) >> 4) - u0;
  const unsigned sf0 = (unsigned)u0 * 32u;
  auto piece = [&](int gs, int j) {
    const unsigned slot = (unsigned)(gs % 5) * SLOT1;
    const unsigned c = (unsigned)(gs + rot) & 7u;
    if (gs < 8) {
      if (j < 2) dma16(slot + (unsigned)(2 * w + j) * 1024u, vx[j], rs_x, sx0 + c * 128u);
      else dma16(slot + 16384u + (unsigned)(NJ ? 2 * mi + j - 2 : 8 + mi) * 1024u, vq[j - 2], rs_q, sq0 + c * (64u * 3072u));
    } else dma16(slot + NJ * 12288u + (unsigned)(3 * mi + j) * 1024u, vf[j], rs_f, sf0 + c * (64u * 2752u));
  };
#pragma unroll
  for (int j = 0; j < NA; ++j) piece(0, j);
  const unsigned myrow = (unsigned)(128 * rb + 8 * g + w);
  u32x4 xrow = ld16_asm(rs_x, (unsigned)lane * 16u, myrow * 1024u);
#pragma unroll
  for (int sg = 1; sg < L::NPRO; ++sg)
#pragma unroll
    for (int j = 0; j < NA; ++j) piece(sg, j);
  SB0();
  STAMP(1);
#pragma unroll
  for (int j = 0; j < 3; ++j) { const int G = (3 * mi + j) * 64 + lane, kg = G / 48, rr = G % 48, unit = (rr >> 3) ^ NJ;
    vf[j] = unit < nu ? (unsigned)(4 * kg + ((rr & 7) >> 1)) * 2752u + (unsigned)(unit * 32 + (rr & 1) * 16) : 0x40000000u; }
  const int r = lane & 31, h = lane >> 5, nsub = (lane >> 4) & 1, q4 = (lane & 15) >> 2, p4 = lane & 3;
  unsigned xa[4], ro[4];
#pragma unroll
  for (int p = 0; p < 4; ++p) { const int ks = (p + mi) & 3;
    xa[p] = (unsigned)(32 * mi + r) * 128u + (unsigned)(((2 * ks + h) ^ ((r >> 1) & 7)) * 16);
    ro[p] = (unsigned)ks * 3072u; }
  const unsigned wl = (unsigned)(2 * h) * 768u + q4 * 32u + p4 * 8u;
  const unsigned wa = 16384u + (unsigned)(2 * h) * 768u + q4 * 192u + nsub * 32u + p4 * 8u;
  const unsigned wbE = nsub * 12288u + wl + nsub * 128u;
  const unsigned wbO = nsub * 12288u + wl - nsub * 128u;
  const AttnSrc asrc = attn_src(rb2, hp2, w);
  bool early = false;
  auto warm = [&](int i) {
    const unsigned xl = blockIdx.x >> 3, dst = L_DUMMY + (unsigned)w * 256u;
    if (i == 0) {
      const unsigned q = (unsigned)(w & 1) * 64u + (unsigned)lane, rr = q / 7u, ln = q - rr * 7u;
      const unsigned ub = (unsigned)((((blockIdx.x & 3) * 4 * NU) >> 4) * 32) & ~127u;
      pf_lines(dst, (w < 4 && q < 112u) ? (16u * xl + rr) * 2752u + ub + ln * 128u : 0x40000000u, mk_rsrc((w & 2) ? P.wu : P.wg, DM * FF * 2));
    } else {
      const unsigned q = xl * 8u + (unsigned)w + 256u * (unsigned)(i - 1);
      dma16(L_DUMMY + 2048u + (unsigned)w * 1024u, (unsigned)lane * 16u + (q < 1888u ? 0u : 0x40000000u), mk_rsrc(q < 1376u ? P.wd : P.wo, q < 1376u ? FF * DM * 2 : DM * DM * 2), (q < 1376u ? q : q - 1376u) * 1024u);
    }
  };
  auto op_at = [&](auto tc, auto kc) {
    constexpr int o = SC.op[decltype(tc)::value][decltype(kc)::value];
    if constexpr (o >= OP_ATT) attn_issue(rs_ws, asrc, w, lane, o - OP_ATT, early);
    else if constexpr (o >= OP_PFW) warm(o - OP_PFW);
    else if constexpr (o >= 0) piece(o >> 3, o & 7);
  };
  bf16x8 sel;
  { const unsigned v = (lane & 15) == ((lane >> 4) & 1) ? ONES2 : 0u; sel = __builtin_bit_cast(bf16x8, u32x4{v, v, v, v}); }

  bf16x8 xf[32];
  constexpr int NB1 = NJ ? 1 : 2;
  f32x16 acc[3];
  f32x4 cs[3];
#pragma unroll
  for (int b = 0; b < 3; ++b) { cs[b] = f32x4{0.f, 0.f, 0.f, 0.f};
#pragma unroll
    for (int i = 0; i < 16; ++i) acc[b][i] = 0.f; }
  bf16x8 wf[3][NB1], wg[2][3];
  auto ldx = [&](int t) { xf[t] = lds_b128((unsigned)((t >> 2) % 5) * SLOT1 + xa[t & 3]); };
  auto ld1a = [&](int t, int b) {
    const unsigned a = (unsigned)((t >> 2) % 5) * SLOT1 + wa + ro[t & 3] + (NJ ? 2 : b) * 64u; wf[t % 3][b] = cat(lds_tr(a), lds_tr(a + 768u));
  };
  auto ld1b = [&](int t, int b) {
    const int u = NJ * 3 + b;
    const unsigned a = (unsigned)((8 + (t >> 2)) % 5) * SLOT1 + ((u & 1) ? wbO : wbE) + ro[t & 3] + u * 128u; wg[t & 1][b] = cat(lds_tr(a), lds_tr(a + 768u));
  };
  auto cs_put = [&](int nb, int b0) {
#pragma unroll
    for (int b = 0; b < 3; ++b) if (b < nb && lane < 16) { *(LAS float*)(lds + L_CSP + (((b0 + b) * 32 + lane) * 4 + mi) * 4) = cs[b][0]; *(LAS float*)(lds + L_CSP + (((b0 + b) * 32 + 16 + lane) * 4 + mi) * 4) = cs[b][1]; }
  };
  auto cs_get = [&](int nb, int b0) {
#pragma unroll
    for (int b = 0; b < 3; ++b) if (b < nb) { const f32x4 c4 = lds_f4(L_CSP + ((b0 + b) * 32 + r) * 16);
      *(LAS float*)(lds + L_CSW + w * 384 + b * 128 + r * 4) = (c4[0] + c4[1]) + (c4[2] + c4[3]); }
  };
  wait_vm<(L::NPRO - 1) * NA + L::NTW>();
  if (w == 0) { tie(ev); word_set(2, ev[0]); word_set(3, ev[1]); }
  bar();
  const u64 e0 = (u64)word_get(2) | ((u64)word_get(3) << 32);
  epoch = e0 + 1ull;
  tg.a = (unsigned)epoch; tg.b = ((unsigned)(e0 >> 32) ^ 0x5bd1e995u) + tg.a;
  STAMP(2); CSTAMP(23);
  ldx(0); ldx(1);
#pragma unroll
  for (int b = 0; b < NB1; ++b) { ld1a(0, b); ld1a(1, b); }
  u32x4 sv;
  float rmean = 0.f, rrstd = 0.f;
  const unsigned sv_off = WS_RS + (unsigned)(128 * rb + 32 * mi) * 16u;
  static_for<0, 32>([&](auto tc) {
    constexpr int t = decltype(tc)::value, s = t >> 2, p = t & 3;
    if constexpr (p == 2) {
      if (s == 7) cs_put(NB1, NJ ? 2 : 0);
      wait_vm<SC.wait_stage(t, s + 1)>();
      if (s == 7) bar(); else bar_raw();
      STAMP2(24 + s);
      if (s == 7) cs_get(NB1, NJ ? 2 : 0);
      if (s == 1) CSTAMP(60); if (s == 4) CSTAMP(61); if (s == 7) CSTAMP(62);
    }
    if (NJ) op_at(tc, IC<0>{});
    static_for<0, NB1>([&](auto bc) {
      constexpr int b = decltype(bc)::value;
      acc[b] = mfma(wf[t % 3][b], xf[t], acc[b]);
      if (t < 30) { if (b == 0) ldx(t + 2); ld1a(t + 2, b); }
      if (t == 31) { ld1b(0, b); if (b == NB1 - 1) { for (int bb = NB1; bb < 3; ++bb) ld1b(0, bb); } }
      if (p == (NJ ? 1 : b)) cs[b] = mfma16(sel, wf[t % 3][b], cs[b]);
      if (b == 0) {
        if (t == 2 + NJ) {
          tie(xrow);
          float e[8];
#pragma unroll
          for (int i = 0; i < 4; ++i) { e[2 * i] = bf_lo(xrow[i]); e[2 * i + 1] = bf_hi(xrow[i]); }
          rmean = wave_sum(((e[0] + e[1]) + (e[2] + e[3])) + ((e[4] + e[5]) + (e[6] + e[7]))) * (1.f / 512.f);
          asm volatile("" : "+v"(rmean));
        }
        if (t == 4) {
          float q = 0.f;
#pragma unroll
          for (int i = 0; i < 4; ++i) { const float d0 = bf_lo(xrow[i]) - rmean, d1 = bf_hi(xrow[i]) - rmean; q = fmaf(d0, d0, q); q = fmaf(d1, d1, q); }
          rrstd = __builtin_amdgcn_rsqf(wave_sum(q) * (1.f / 512.f) + 1e-5f);
          asm volatile("" : "+v"(rrstd));
        }
      }
      SB0();
      if (b == 0) op_at(tc, IC<NJ ? 1 : 0>{});
    });
    if (!NJ) op_at(tc, IC<1>{});
    if (t == 22) sv = ld16c_asm(rs_ws, (unsigned)r * 16u, sv_off);
    if (t == 5 && lane == 0) st16c(u32x4{__float_as_uint(rmean), tg.a, __float_as_uint(rrstd), tg.b}, rs_ws, 0u, WS_RS + myrow * 16u);
    SB0();
  });
  STAMP(3); CSTAMP(22);
  { constexpr int after = SC.before[32] - L::NST - SC.before[23]; wait_vm_tie<after>(sv); }
  NOTE(53, __all(sv[1] == tg.a && sv[3] == tg.b) ? 1 : 2);
  for (unsigned spins = 0; !__all(sv[1] == tg.a && sv[3] == tg.b) && spins < SPIN_CAP; ++spins) {
    __builtin_amdgcn_s_sleep(1);
    sv = ld16c_asm(rs_ws, (unsigned)r * 16u, sv_off); wait_vm_tie<0>(sv);
  }
  const float mean = __uint_as_float(sv[0]), rstd = __uint_as_float(sv[2]);
  const int sb = rb * 4 + mi;
#pragma unroll
  for (int b = 0; b < NB1; ++b) {
    const int blk = 3 * g + (NJ ? 2 : b), which = blk >> 4, hp = blk & 15;
    const float sc = which == 0 ? rstd * (0.25f * 1.4426950408889634f) : rstd;
    float v[16];
#pragma unroll
    for (int i = 0; i < 4; ++i) { const f32x4 c4 = lds_f4(L_CSW + w * 384 + b * 128 + (8 * i + 4 * h) * 4);
#pragma unroll
      for (int e = 0; e < 4; ++e) v[4 * i + e] = (acc[b][4 * i + e] - mean * c4[e]) * sc; }
    const unsigned vo = which == 2 ? (unsigned)(h * 512 + ((r ^ (4 * h)) * 16)) : (unsigned)(lane * 16);
#pragma unroll
    for (int hd = 0; hd < 2; ++hd) {
      const u32x4 o = {pk(v[8 * hd], v[8 * hd + 1]), pk(v[8 * hd + 2], v[8 * hd + 3]), pk(v[8 * hd + 4], v[8 * hd + 5]), pk(v[8 * hd + 6], v[8 * hd + 7])};
      st16c(o, rs_ws, vo, WS_QKV + (unsigned)((which * 32 + 2 * hp + hd) * 64 + sb) * 1024u);
    }
  }
  STAMP(4); CSTAMP(18);
#pragma unroll
  for (int b = 0; b < 3; ++b) { cs[b] = f32x4{0.f, 0.f, 0.f, 0.f};
#pragma unroll
    for (int i = 0; i < 16; ++i) acc[b][i] = 0.f; }
  bool act;
  const unsigned qv = qkv_flag_voff(rb2, hp2, lane, act);
  u32x4 fq;
  SB0();
  static_for<32, 64>([&](auto tc) {
    constexpr int Tg = decltype(tc)::value, t = Tg - 32, s = Tg >> 2, p = t & 3;
    if constexpr (p == 3) {
      if (s == 15) cs_put(3, NJ * 3);
      if constexpr (s == 9) wait_vm<(SC.before[Tg] - SC.before[32] < SC.wait_stage(Tg, 10) ? SC.before[Tg] - SC.before[32] : SC.wait_stage(Tg, 10))>();
      else if constexpr (s == ATT_B) { wait_vm<0>(); if (w == 0) { tie(fq); word_set(0, flags_ok(fq, act, tg) ? 1u : 0u); } }
      else if constexpr (s <= 14) wait_vm<SC.wait_stage(Tg, s + 1)>();
      if (s == ATT_B || s == 15) bar(); else bar_raw();
      STAMP2(24 + s);
      if (s == 8) CSTAMP(56); if (s == 11) CSTAMP(57); if (s == 13) CSTAMP(58); if (s == 15) CSTAMP(59);
      if (s == ATT_B) early = word_get(0) != 0u;
      if (s == 15) cs_get(3, NJ * 3);
    }
    if (NJ) op_at(tc, IC<0>{});
    static_for<0, 3>([&](auto bc) {
      constexpr int b = decltype(bc)::value;
      acc[b] = mfma(wg[t & 1][b], xf[t], acc[b]);
      if (t < 31) ld1b(t + 1, b);
      if (p == ((b + 2 * NJ) & 3) || (s == 15 && p == 2 && ((b + 2 * NJ) & 3) == 3)) { if (!(s == 15 && p == 3)) cs[b] = mfma16(sel, wg[t & 1][b], cs[b]); }
      SB0();
    });
    if (!NJ) op_at(tc, IC<0>{});
    op_at(tc, IC<1>{});
    if (Tg == 39) flag_store(rs_ws, WS_FQKV + (unsigned)((rb * 16 + g) * 64 + w * 8), tg, lane);
    if (Tg == 4 * ATT_B - 1 && w == 0) fq = ld16c_asm(rs_ws, qv, 0u);
    SB0();
  });
  STAMP(5); CSTAMP(19);
  const float r2 = rstd * rstd;
  f32x4 cq[3][4];
#pragma unroll
  for (int b = 0; b < 3; ++b)
#pragma unroll
    for (int i = 0; i < 4; ++i) cq[b][i] = lds_f4(L_CSW + w * 384 + b * 128 + (8 * i + 4 * h) * 4);
#pragma unroll
  for (int b = 0; b < 3; ++b) {
    float v[16];
#pragma unroll
    for (int i = 0; i < 4; ++i)
#pragma unroll
      for (int e = 0; e < 4; ++e) v[4 * i + e] = acc[b][4 * i + e] - mean * cq[b][i][e];
    float hv[8];
#pragma unroll
    for (int i = 0; i < 8; ++i) hv[i] = fmaxf(v[i], 0.f) * v[8 + i] * r2;
    const u32x4 o = {pk(hv[0], hv[1]), pk(hv[2], hv[3]), pk(hv[4], hv[5]), pk(hv[6], hv[7])};
    st16c(o, rs_ws, (unsigned)lane * 16u + (NJ * 3 + b < nu ? 0u : 0x40000000u), WS_H + (unsigned)(sb * NU + u0 + NJ * 3 + b) * 1024u);
  }
  STAMP(6);
  wait_vm<3>();
  return early;
}

template <bool FULL, class F1, class F3, class F2>
DI void attn_pair(int j0, int qb, int lane, F1 side1, F3 side2, F2 head_done, u32x4 (&o)[2]) {
  const int q = lane & 31, hh = lane >> 5, tq = q - 4 * hh;
  const float NEG = -__builtin_inff();
  bf16x8 qf[2];
#pragma unroll
  for (int hd = 0; hd < 2; ++hd) qf[hd] = lds_b128(LA_Q + (unsigned)(hd * 4 + qb) * 1024u + lane * 16u);
  unsigned kb = LA_K + (unsigned)qb * 1024u + lane * 16u;
  auto ldk = [&](int hd, int j) { return lds_b128(kb + (unsigned)(hd * 12 + j) * 1024u); };
  auto pin = [&]() { asm volatile("" : "+v"(kb)); };
  auto on = [&](int j) { return FULL || j >= j0; };
  f32x16 c[2], s[2];
  bf16x8 kf[2];
#pragma unroll
  for (int i = 0; i < 16; ++i) c[0][i] = 0.f;
  float m0[2], m1[2];
  {
    f32x16 s8[2], s0[2];
#pragma unroll
    for (int hd = 0; hd < 2; ++hd) { s8[hd] = mfma(ldk(hd, 8), qf[hd], c[0]); if (FULL) s0[hd] = mfma(ldk(hd, 0), qf[hd], c[0]); }
    if (on(1)) { kf[0] = ldk(0, 1); kf[1] = ldk(1, 1); }
    SB0();
#pragma unroll
    for (int hd = 0; hd < 2; ++hd) {
      m0[hd] = NEG; m1[hd] = NEG;
#pragma unroll
      for (int i = 0; i < 16; i += 2) {
        const int c0 = (i & 3) + 8 * (i >> 2);
        const float a = c0 <= tq ? s8[hd][i] : (FULL ? s0[hd][i] : NEG), b = c0 + 1 <= tq ? s8[hd][i + 1] : (FULL ? s0[hd][i + 1] : NEG);
        if (i & 2) m1[hd] = fmaxf(fmaxf(m1[hd], a), b); else m0[hd] = fmaxf(fmaxf(m0[hd], a), b);
      }
      asm volatile("" : "+v"(m0[hd]), "+v"(m1[hd]));
      if (hd == 0) { if (on(1)) s[0] = mfma(kf[0], qf[0], c[0]); if (on(2)) kf[0] = ldk(0, 2); }
      SB0();
    }
  }
  static_for<0, 14>([&](auto nc) {
    constexpr int n = decltype(nc)::value, hd = n & 1, j = 1 + (n >> 1), hn = (n + 1) & 1, jn = 1 + ((n + 1) >> 1);
    pin();
    if (n + 1 < 14) { if (on(jn)) s[hn] = mfma(kf[hn], qf[hn], c[0]); if (jn < 7 && on(jn + 1)) kf[hn] = ldk(hn, jn + 1); }
    SB0();
    if (on(j)) {
#pragma unroll
      for (int i = 0; i < 8; i += 2) { m0[hd] = fmaxf(fmaxf(m0[hd], s[hd][i]), s[hd][i + 1]); m1[hd] = fmaxf(fmaxf(m1[hd], s[hd][8 + i]), s[hd][9 + i]); }
      asm volatile("" : "+v"(m0[hd]), "+v"(m1[hd]));
    }
    side1(n);
    SB0();
  });
#pragma unroll
  for (int hd = 0; hd < 2; ++hd) { const float m = xhalf_max(fmaxf(m0[hd], m1[hd]));
#pragma unroll
    for (int i = 0; i < 16; ++i) c[hd][i] = -m; }
  const int grp = (lane >> 4) & 1, q4 = (lane & 15) >> 2, p4 = lane & 3;
  const unsigned vl = (unsigned)((p4 & 1) * 512 + (((4 * hh + q4) ^ (4 * (p4 & 1))) * 16) + (p4 >> 1) * 8);
  unsigned vb[2];
#pragma unroll
  for (int hd = 0; hd < 2; ++hd) vb[hd] = grp ? L_ONES + 128u : LA_V + (unsigned)(hd * 12 + qb) * 1024u + vl;
  bf16x8 vf[2][2];
  auto ldv = [&](int hd, int j) {
#pragma unroll
    for (int u = 0; u < 2; ++u) vf[hd][u] = cat(lds_tr(vb[hd] + j * 1024u + u * 256u), lds_tr(vb[hd] + j * 1024u + u * 256u + 128u));
  };
  f32x16 acc[2];
#pragma unroll
  for (int hd = 0; hd < 2; ++hd)
#pragma unroll
    for (int i = 0; i < 16; ++i) acc[hd][i] = 0.f;
  auto pv = [&](int hd, const unsigned (&pp)[8]) {
#pragma unroll
    for (int u = 0; u < 2; ++u) acc[hd] = mfma(vf[hd][u], __builtin_bit_cast(bf16x8, u32x4{pp[4 * u], pp[4 * u + 1], pp[4 * u + 2], pp[4 * u + 3]}), acc[hd]);
  };
  if (on(1)) { kf[0] = ldk(0, 1); kf[1] = ldk(1, 1); s[0] = mfma(kf[0], qf[0], c[0]); ldv(0, 1); }
  if (on(2)) kf[0] = ldk(0, 2);
  static_for<0, 14>([&](auto nc) {
    constexpr int n = decltype(nc)::value, hd = n & 1, j = 1 + (n >> 1), hn = (n + 1) & 1, jn = 1 + ((n + 1) >> 1);
    pin();
    if (n + 1 < 14) { if (on(jn)) { s[hn] = mfma(kf[hn], qf[hn], c[hn]); ldv(hn, jn); } if (jn < 7 && on(jn + 1)) kf[hn] = ldk(hn, jn + 1); }
    SB0();
    if (on(j)) {
      unsigned pp[8];
#pragma unroll
      for (int i = 0; i < 8; ++i) pp[i] = pk(__builtin_amdgcn_exp2f(s[hd][2 * i]), __builtin_amdgcn_exp2f(s[hd][2 * i + 1]));
      pv(hd, pp);
    }
    side2(n);
    SB0();
  });
  pin();
  f32x16 s8[2], s0[2];
#pragma unroll
  for (int hd = 0; hd < 2; ++hd) { s8[hd] = mfma(ldk(hd, 8), qf[hd], c[hd]); if (FULL) s0[hd] = mfma(ldk(hd, 0), qf[hd], c[hd]); }
#pragma unroll
  for (int hd = 0; hd < 2; ++hd) {
    ldv(hd, 8);
    unsigned p8[8], p0[8];
#pragma unroll
    for (int i = 0; i < 8; ++i) {
      const int c0 = ((2 * i) & 3) + 8 * (i >> 1);
      const bool va = c0 <= tq, vb_ = c0 + 1 <= tq;
      const float a = va ? s8[hd][2 * i] : (FULL ? s0[hd][2 * i] : NEG), b = vb_ ? s8[hd][2 * i + 1] : (FULL ? s0[hd][2 * i + 1] : NEG);
      const unsigned pp = pk(__builtin_amdgcn_exp2f(a), __builtin_amdgcn_exp2f(b));
      const unsigned mk = (va ? 0xffffu : 0u) | (vb_ ? 0xffff0000u : 0u);
      p8[i] = pp & mk; p0[i] = pp & ~mk;
    }
    pv(hd, p8);
    if (FULL) { ldv(hd, 0); pv(hd, p0); }
    const float inv = __builtin_amdgcn_rcpf(acc[hd][8]);
    o[hd] = u32x4{pk(acc[hd][0] * inv, acc[hd][1] * inv), pk(acc[hd][2] * inv, acc[hd][3] * inv), pk(acc[hd][4] * inv, acc[hd][5] * inv), pk(acc[hd][6] * inv, acc[hd][7] * inv)};
    head_done(hd);
  }
}

constexpr int DW = 10, DA = 6, NR = 12, NK3 = 30, NDN = 22, OCHK = NDN - (DA - 1), OPOLL = OCHK - 6, XLD = 18;
struct Sched3 {
  int park[NR] = {}, before[NK3 + 1] = {}, lastW[NK3] = {}, lastA[NK3] = {};
  constexpr Sched3() {
    int c = 0;
    for (int i = 0; i < DW - 1; ++i) { c += 2; lastW[i] = c; }
    c += 1;
    for (int i = 0; i < NR; ++i) { park[i] = c - lastW[i]; c += 2; lastW[i + DW - 1] = c; }
    c += 2; lastW[NR + DW - 1] = c;
    for (int i = 0; i < DA - 1; ++i) { c += 2; lastA[i] = c; }
    for (int i = 0; i < NK3; ++i) {
      before[i] = c;
      if (i > NR && i + DW - 1 < NK3) { c += 2; lastW[i + DW - 1] = c; }
      if (i + DA - 1 < NK3) { c += 2; lastA[i + DA - 1] = c; }
      if (i == XLD) c += 4;
    }
    before[NK3] = c;
  }
  constexpr int wait(int i) const { const int w = i + 1 < NK3 ? lastW[i + 1] : 0; return before[i] - (lastA[i] > w ? lastA[i] : w); }
};
DI unsigned l3w(int kq) { return kq < 3 ? 86016u + (unsigned)kq * (DW * 2048u) : 0u; }

DI void phase23(const Ptrs& P, bool early, int rb1, int g1, u64 epoch, int rb, int hp, int rb64, int cb, int rot, int w, int lane, Tag tg, Stamps& T) {
  const rsrc_t rs_ws = mk_rsrc(P.ws, WS_BYTES);
  for (unsigned i = threadIdx.x; i < ONES_BYTES / 16; i += 512) *(LAS u32x4*)(lds + L_ONES + i * 16) = u32x4{ONES2, ONES2, ONES2, ONES2};
  NOTE(63, early ? 1 : 2);
  if (!early) {
    bool act; const unsigned qv = qkv_flag_voff(rb, hp, lane, act);
    if (w == 0) flags_spin(rs_ws, qv, act, tg);
    bar();
    const AttnSrc asrc = attn_src(rb, hp, w);
#pragma unroll
    for (int i = 0; i < 7; ++i) attn_issue(rs_ws, asrc, w, lane, i, true);
    wait_vm<0>();
  }
  bar();
  STAMP(9);
  const unsigned fh_off = WS_FH + (unsigned)((rb1 * 16 + g1) * 64 + w * 8);
  const int h = lane >> 5, nsub = (lane >> 4) & 1, q4 = (lane & 15) >> 2, p4 = lane & 3;
  f32x16 acc[4];
  u32x2 xr[4];
  const int kq = w - 4;
  const bool keeper = blockIdx.x == 0 && w == 4;
  u32x4 fe[4];
  const int ulo = kq < 3 ? 22 * kq : 65, nun = kq < 2 ? 22 : 21;
  const unsigned ring = __builtin_amdgcn_readfirstlane(l3w(kq));
  if (w < 4) {
    __builtin_amdgcn_s_setprio(3);
    const int sb = 4 * rb + w;
    const unsigned o_off = WS_O + (unsigned)(sb * 32 + 2 * hp) * 1024u, fo_off = WS_FO + (unsigned)((rb * 16 + hp) * 64 + w * 8);
    auto side1 = [&](int n) { if (n == 13) STAMP(10); };
    auto side_h = [&](int n) { if (n == 2) { wait_vm<0>(); flag_store(rs_ws, fh_off, tg, lane); } };
    u32x4 o[2];
    auto store_o = [&](int hd) { st16c(o[hd], rs_ws, (unsigned)lane * 16u, o_off + (unsigned)hd * 1024u); };
    if (sb >= 8) attn_pair<true>(0, w, lane, side1, side_h, store_o, o); else attn_pair<false>(8 - sb, w, lane, side1, side_h, store_o, o);
    STAMP(11);
    wait_vm<0>();
    flag_store(rs_ws, fo_off, tg, lane); flag_store(rs_ws, fo_off + 32u, tg, lane);
    STAMP(12);
  } else {
    const rsrc_t rs_d = mk_rsrc(P.wd, FF * DM * 2), rs_o = mk_rsrc(P.wo, DM * DM * 2);
    unsigned vw[2];
#pragma unroll
    for (int j = 0; j < 2; ++j) vw[j] = (unsigned)(8 * j + (lane >> 3)) * 1024u + (unsigned)(((lane & 7) ^ (((lane >> 4) & 1) << 2)) * 16);
    const unsigned vo = (unsigned)lane * 16u;
    auto unit = [&](int i) { const int r = i + rot; return i < NDN ? (i < nun ? ulo + (r >= nun ? r - nun : r) : -1) : 8 * kq + ((i - NDN + rot) & 7); };
    auto reqW = [&](int i) {
      const int u = unit(i);
#pragma unroll
      for (int j = 0; j < 2; ++j) dma16(ring + (unsigned)(i % DW) * 2048u + j * 1024u, u < 0 ? 0x40000000u : vw[j], i < NDN ? rs_d : rs_o, (unsigned)cb * 128u + (unsigned)(u < 0 ? 0 : u) * 16384u);
    };
    const unsigned hv = WS_FH + (unsigned)((rb64 >> 1) * 1024 + lane * 16), ov = WS_FO + (unsigned)((rb64 >> 1) * 1024 + lane * 16);
    const int pg = lane >> 2;
    const bool pair_ok = ((lane & 3) & 1) == (rb64 & 1);
    const bool h_act = pair_ok && (((pg + 1) * NU) >> 4) > ulo && ((pg * NU) >> 4) < ulo + nun, o_act = pair_ok && (pg >> 2) == kq;
    unsigned wa[2];
#pragma unroll
    for (int nb = 0; nb < 2; ++nb) wa[nb] = ring + (unsigned)h * 512u + q4 * 128u + (unsigned)((nb ^ (q4 >> 1)) * 64) + nsub * 32u + p4 * 8u;
    auto rdw = [&](int i, int nb) { const unsigned a = wa[nb] + (unsigned)(i % DW) * 2048u; return cat(lds_tr(a), lds_tr(a + 1024u)); };
    static constexpr Sched3 S3{};
#pragma unroll
    for (int i = 0; i < DW - 1; ++i) reqW(i);
    wait_vm<2 * (DW - 1)>(); flag_store(rs_ws, fh_off, tg, lane);
    bf16x8 wr[NR][2];
    static_for<0, NR>([&](auto ic) {
      constexpr int i = decltype(ic)::value;
      wait_vm<S3.park[i]>();
      wr[i][0] = rdw(i, 0); wr[i][1] = rdw(i, 1);
      reqW(i + DW - 1);
      asm volatile("s_waitcnt lgkmcnt(0)" : "+v"(wr[i][0]), "+v"(wr[i][1]) :: "memory");
    });
    reqW(NR + DW - 1);
    flags_spin(rs_ws, hv, h_act, tg);
    STAMP(13);
    u32x4 A[DA][2];
    auto reqA = [&](int i) {
      const int u = unit(i);
#pragma unroll
      for (int mb = 0; mb < 2; ++mb)
        A[i % DA][mb] = ld16c_asm(rs_ws, u < 0 ? 0x40000000u : vo, i < NDN ? WS_H + (unsigned)((2 * rb64 + mb) * NU + (u < 0 ? 0 : u)) * 1024u : WS_O + (unsigned)((2 * rb64 + mb) * 32 + u) * 1024u);
    };
#pragma unroll
    for (int i = 0; i < DA - 1; ++i) reqA(i);
#pragma unroll
    for (int b = 0; b < 4; ++b)
#pragma unroll
      for (int i = 0; i < 16; ++i) acc[b][i] = 0.f;
    const int row = 64 * rb64 + 32 * (kq >> 1) + (lane & 31), col0 = 64 * cb + 32 * (kq & 1) + 4 * h;
    const unsigned short* xp = (const unsigned short*)P.x + (size_t)row * DM + col0;
    u32x4 fo;
    bf16x8 wf[2][2];
    static_for<0, NK3>([&](auto ic) {
      constexpr int i = decltype(ic)::value;
      wait_vm<S3.wait(i)>();
      tie(A[i % DA][0]); tie(A[i % DA][1]);
      if (i == 0) STAMP(14);
      if constexpr (i == OCHK) {
        wait_vm_tie<S3.before[i] - S3.before[OPOLL + 1]>(fo);
        NOTE(54, flags_ok(fo, o_act, tg) ? 1 : 2);
        if (!flags_ok(fo, o_act, tg)) flags_spin(rs_ws, ov, o_act, tg);
      }
      const bf16x8 w0 = i < NR ? wr[i < NR ? i : 0][0] : wf[i & 1][0], w1 = i < NR ? wr[i < NR ? i : 0][1] : wf[i & 1][1];
      SB0();
      acc[0] = mfma(w0, __builtin_bit_cast(bf16x8, A[i % DA][0]), acc[0]);
      if constexpr (i + 1 >= NR && i + 1 < NK3) { wf[(i + 1) & 1][0] = rdw(i + 1, 0); wf[(i + 1) & 1][1] = rdw(i + 1, 1); }
      SB0();
      acc[1] = mfma(w1, __builtin_bit_cast(bf16x8, A[i % DA][0]), acc[1]);
      SB0();
      if constexpr (i > NR && i + DW - 1 < NK3) reqW(i + DW - 1);
      acc[2] = mfma(w0, __builtin_bit_cast(bf16x8, A[i % DA][1]), acc[2]);
      SB0();
      acc[3] = mfma(w1, __builtin_bit_cast(bf16x8, A[i % DA][1]), acc[3]);
      SB0();
      if constexpr (i + DA - 1 < NK3) reqA(i + DA - 1);
      if (i == OPOLL) fo = ld16c_asm(rs_ws, ov, 0u);
      if (i == XLD) {
#pragma unroll
        for (int q = 0; q < 4; ++q) xr[q] = *(const GAS u32x2*)(xp + 8 * q);
      }
      if (i == 20 && keeper) {
#pragma unroll
        for (int q = 0; q < 4; ++q) fe[q] = ld16c_asm(rs_ws, WS_FQKV + (unsigned)(q * 64 + lane) * 64u, 0u);
      }
      SB0();
    });
    STAMP(15);
  }
  if (w >= 4) {
#pragma unroll
    for (int b = 0; b < 4; ++b) {
      if (b != kq) {
        const unsigned base = ring + (unsigned)((b - kq - 1 + 4) % 4) * 4096u + lane * 16u;
#pragma unroll
        for (int q = 0; q < 4; ++q) *(LAS f32x4*)(lds + base + q * 1024u) = f32x4{acc[b][4 * q], acc[b][4 * q + 1], acc[b][4 * q + 2], acc[b][4 * q + 3]};
      }
    }
  }
  bar();
  if (w >= 4) {
    f32x16 r = kq == 0 ? acc[0] : (kq == 1 ? acc[1] : (kq == 2 ? acc[2] : acc[3]));
#pragma unroll
    for (int s = 1; s < 4; ++s) {
      const int src = (kq + s) & 3;
      const unsigned base = l3w(src) + (unsigned)((kq - src - 1 + 4) % 4) * 4096u + lane * 16u;
#pragma unroll
      for (int q = 0; q < 4; ++q) { const f32x4 v = lds_f4(base + q * 1024u);
#pragma unroll
        for (int e = 0; e < 4; ++e) r[4 * q + e] += v[e]; }
    }
    const int row = 64 * rb64 + 32 * (kq >> 1) + (lane & 31), col0 = 64 * cb + 32 * (kq & 1);
    unsigned o[8];
#pragma unroll
    for (int q = 0; q < 4; ++q) { o[2 * q] = pk(r[4 * q] + bf_lo(xr[q][0]), r[4 * q + 1] + bf_hi(xr[q][0])); o[2 * q + 1] = pk(r[4 * q + 2] + bf_lo(xr[q][1]), r[4 * q + 3] + bf_hi(xr[q][1])); }
    unsigned short* op = (unsigned short*)P.out + (size_t)row * DM + col0 + 8 * h;
#pragma unroll
    for (int q = 0; q < 4; q += 2) {
      const auto t0 = __builtin_amdgcn_permlane32_swap(o[2 * q], o[2 * q + 2], false, false), t1 = __builtin_amdgcn_permlane32_swap(o[2 * q + 1], o[2 * q + 3], false, false);
      *(GAS u32x4*)(op + 8 * q) = u32x4{t0[0], t1[0], t0[1], t1[1]};
    }
    if (keeper) {
#pragma unroll
      for (int q = 0; q < 4; ++q) { tie(fe[q]); if (!flags_ok(fe[q], true, tg)) flags_spin(rs_ws, WS_FQKV + (unsigned)(q * 64 + lane) * 64u, true, tg); }
      if (lane == 0) __hip_atomic_store((GAS u64*)(P.ws + WS_EPOCH), epoch, __ATOMIC_RELAXED, __HIP_MEMORY_SCOPE_AGENT);
    }
  }
}

__global__ __launch_bounds__(512, 2) void mk_kernel(const void* x, const void* wqkv, const void* wo, const void* wg, const void* wu, const void* wd, void* out, char* ws) {
  u64 pc0 = 0;
  asm volatile("mk_code_begin:\n\ts_getpc_b64 %0" : "=s"(pc0));
  {
    unsigned len;
    asm volatile("s_mov_b32 %0, mk_code_end-mk_code_begin" : "=s"(len));
    const unsigned wv = __builtin_amdgcn_readfirstlane(threadIdx.x >> 6);
    const unsigned per = ((len >> 5) + 1023u) & ~1023u;
    const unsigned o = (blockIdx.x >> 3) * per + wv * 1024u;
    dma16(L_DUMMY + wv * 1024u, (threadIdx.x & 63u) * 16u, mk_rsrc((const void*)(pc0 & ~255ull), len & ~15u), wv * 1024u < per ? o : 0x40000000u);
  }
  const Ptrs P{x, wqkv, wo, wg, wu, wd, out, ws, pc0};
  Stamps T;
  STAMP(0); CSTAMP(20);
  __builtin_amdgcn_s_setprio(1);
  const int b = blockIdx.x, xcd = b & 7, idx = b >> 3;
  const int w = __builtin_amdgcn_readfirstlane(threadIdx.x >> 6), lane = threadIdx.x & 63;
  Tag tg; u64 epoch;
  const int rb = 2 * (idx >> 2) + (xcd >> 2), g = (xcd & 3) * 4 + (idx & 3), rot1 = ((idx >> 2) + 2 * (idx & 3)) & 7;
  bool early;
  if (w < 4) early = phase1<0>(P, rb, g, rot1, w, lane, rb, g, tg, epoch, T); else early = phase1<1>(P, rb, g, rot1, w - 4, lane, rb, g, tg, epoch, T);
  const int rot3 = ((idx & 7) * 2 + 5 * (idx >> 3)) % 21;
  phase23(P, early, rb, g, epoch, rb, g, xcd * 4 + (idx >> 3), idx & 7, rot3, w, lane, tg, T);
  asm volatile("mk_code_end:");
}

}

extern "C" void kernel_launch(void* const* d_in, const int* in_sizes, int n_in, void* d_out, int out_size, void* d_ws, size_t ws_size, hipStream_t stream) {
  const void *x = d_in[0], *wqkv = d_in[1], *wo = d_in[2], *wg = d_in[4], *wu = d_in[5], *wd = d_in[6];
  mk_kernel<<<256, 512, 0, stream>>>(x, wqkv, wo, wg, wu, wd, d_out, (char*)d_ws);
}
```

```cpp
#include <hip/hip_runtime.h>
#include <hip/hip_bf16.h>
#include <stdint.h>

#define P1_NPRO 3
namespace {

typedef short bf16x8 __attribute__((ext_vector_type(8)));
typedef short s16x4 __attribute__((ext_vector_type(4)));
typedef float f32x16 __attribute__((ext_vector_type(16)));
typedef float f32x4 __attribute__((ext_vector_type(4)));
typedef float f32x2 __attribute__((ext_vector_type(2)));
typedef unsigned u32x4 __attribute__((ext_vector_type(4)));
typedef unsigned u32x2 __attribute__((ext_vector_type(2)));
typedef __bf16 bf16x2_t __attribute__((ext_vector_type(2)));
typedef short v4i16_t __attribute__((ext_vector_type(4)));
typedef __amdgpu_buffer_rsrc_t rsrc_t;
#define LAS __attribute__((address_space(3)))
#define GAS __attribute__((address_space(1)))
#define DI __device__ __forceinline__
#define SB0() __builtin_amdgcn_sched_barrier(0)
typedef unsigned long long u64;

constexpr int SEQ = 2048, DM = 512, FF = 1376, NU = 86;
constexpr unsigned WS_EPOCH = 0;
constexpr unsigned WS_FQKV = 16384, WS_FH = 32768, WS_FO = 49152;
constexpr unsigned WS_RS = 65536;
constexpr unsigned WS_QKV = 1u << 20;
constexpr unsigned WS_H = 7u << 20;
constexpr unsigned WS_O = 13u << 20;
constexpr unsigned WS_BYTES = 16u << 20;
constexpr unsigned SLOT1 = 28672;
constexpr unsigned L_CSP = 143360;
constexpr unsigned L_CSW = 146432;
constexpr unsigned L_ONES = 152576;
constexpr unsigned ONES_BYTES = 9984;
constexpr unsigned L_DUMMY = 152576;
constexpr unsigned L_WORD = 163584;
constexpr unsigned LDS_BYTES = 163840;
constexpr unsigned LA_Q = 28672, LA_K = LA_Q + 8192, LA_V = LA_K + 24576;

__shared__ __attribute__((aligned(1024))) char lds[LDS_BYTES];

DI unsigned pk(float a, float b) { f32x2 v = {a, b}; bf16x2_t r = __builtin_convertvector(v, bf16x2_t); return __builtin_bit_cast(unsigned, r); }
DI float dot2(unsigned a, unsigned b, float c) { return __builtin_amdgcn_fdot2_f32_bf16(__builtin_bit_cast(bf16x2_t, a), __builtin_bit_cast(bf16x2_t, b), c, false); }
DI rsrc_t mk_rsrc(const void* p, unsigned bytes) { return __builtin_amdgcn_make_buffer_rsrc((void*)p, 0, bytes, 0x00020000); }
#define LD_POL ""
#define ST_AUX 16
DI void dma16(unsigned ldsaddr, unsigned voff, rsrc_t rs, unsigned soff) {
  asm volatile("s_mov_b32 m0, %0\n\ts_nop 0\n\tbuffer_load_dwordx4 %1, %2, %3 offen" LD_POL " lds" ::"s"(ldsaddr), "v"(voff), "s"(rs), "s"(soff) : "memory");
}
DI void dma16c(unsigned ldsaddr, unsigned voff, rsrc_t rs, unsigned soff) {
  asm volatile("s_mov_b32 m0, %0\n\ts_nop 0\n\tbuffer_load_dwordx4 %1, %2, %3 offen sc1 lds" ::"s"(ldsaddr), "v"(voff), "s"(rs), "s"(soff) : "memory");
}
DI void pf_lines(unsigned ldsaddr, unsigned voff, rsrc_t rs) {
  asm volatile("s_mov_b32 m0, %0\n\ts_nop 0\n\tbuffer_load_dword %1, %2, 0 offen lds" ::"s"(ldsaddr), "v"(voff), "s"(rs) : "memory");
}
template <int N> DI void wait_vm() { asm volatile("s_waitcnt vmcnt(%0)" ::"n"(N) : "memory"); }
template <int N> DI void wait_vm_tie(u32x4& v) { asm volatile("s_waitcnt vmcnt(%1)" : "+v"(v) : "n"(N) : "memory"); }
template <int V> struct IC { static constexpr int value = V; };
template <int I, int N, class F> DI void static_for(F&& f) { if constexpr (I < N) { f(IC<I>{}); static_for<I + 1, N>(f); } }
DI void bar_raw() { asm volatile("s_barrier" ::: "memory"); }
DI void tie(u32x4& v) { asm volatile("" : "+v"(v) :: "memory"); }
DI void bar() { asm volatile("s_waitcnt lgkmcnt(0)\n\ts_barrier" ::: "memory"); }
DI f32x16 mfma(bf16x8 a, bf16x8 b, f32x16 c) { return __builtin_amdgcn_mfma_f32_32x32x16_bf16(a, b, c, 0, 0, 0); }
DI bf16x8 lds_b128(unsigned a) { return *(const LAS bf16x8*)(lds + a); }
#define MK_REAL 1
DI s16x4 lds_tr(unsigned a) { return __builtin_amdgcn_ds_read_tr16_b64_v4i16((LAS v4i16_t*)(lds + a)); }
DI f32x4 mfma16(bf16x8 a, bf16x8 b, f32x4 c) { return __builtin_amdgcn_mfma_f32_16x16x32_bf16(a, b, c, 0, 0, 0); }
DI f32x4 lds_f4(unsigned a) { return *(const LAS f32x4*)(lds + a); }
DI bf16x8 cat(s16x4 lo, s16x4 hi) { return __builtin_shufflevector(lo, hi, 0, 1, 2, 3, 4, 5, 6, 7); }
DI float xhalf_sum(float v) { auto r = __builtin_amdgcn_permlane32_swap(__float_as_uint(v), __float_as_uint(v), false, false); return __uint_as_float(r[0]) + __uint_as_float(r[1]); }
DI float xhalf_max(float v) { auto r = __builtin_amdgcn_permlane32_swap(__float_as_uint(v), __float_as_uint(v), false, false); return fmaxf(__uint_as_float(r[0]), __uint_as_float(r[1])); }
DI void st16c(u32x4 v, rsrc_t rs, unsigned voff, unsigned soff) { __builtin_amdgcn_raw_buffer_store_b128(v, rs, voff + soff, 0, ST_AUX); }
DI void st8c(u32x2 v, rsrc_t rs, unsigned voff, unsigned soff) { __builtin_amdgcn_raw_buffer_store_b64(v, rs, voff, soff, 16); }
constexpr unsigned ONES2 = 0x3F803F80u;
constexpr unsigned SPIN_CAP = 1u << 19;
template <int CTRL> DI float dpp_add(float v) { return v + __int_as_float(__builtin_amdgcn_update_dpp(0, __float_as_int(v), CTRL, 0xf, 0xf, true)); }
DI float wave_sum(float v) {
  v = dpp_add<0xB1>(v); v = dpp_add<0x4E>(v); v = dpp_add<0x141>(v); v = dpp_add<0x140>(v);
  auto a = __builtin_amdgcn_permlane16_swap(__float_as_uint(v), __float_as_uint(v), false, false);
  v = __uint_as_float(a[0]) + __uint_as_float(a[1]);
  return xhalf_sum(v);
}
DI float bf_lo(unsigned u) { return __uint_as_float(u << 16); }
DI float bf_hi(unsigned u) { return __uint_as_float(u & 0xffff0000u); }
DI u32x4 ld16_asm(rsrc_t rs, unsigned voff, unsigned soff) { u32x4 v; asm volatile("buffer_load_dwordx4 %0, %1, %2, %3 offen" : "=v"(v) : "v"(voff), "s"(rs), "s"(soff) : "memory"); return v; }
DI u32x4 ld16c_asm(rsrc_t rs, unsigned voff, unsigned soff) { u32x4 v; asm volatile("buffer_load_dwordx4 %0, %1, %2, %3 offen sc1" : "=v"(v) : "v"(voff), "s"(rs), "s"(soff) : "memory"); return v; }
#define STAMP2(i) do {} while (0)
struct Stamps {};
#define STAMP(i) do {} while (0)
#define CSTAMP(i) do {} while (0)
#define NOTE(i, v) do {} while (0)

struct Ptrs { const void *x, *wqkv, *wo, *wg, *wu, *wd; void* out; char* ws; u64 pc; };
struct Tag { unsigned a, b; };

DI void flag_store(rsrc_t rs_ws, unsigned off, Tag tg, int lane) { if (lane == 0) st8c(u32x2{tg.a, tg.b}, rs_ws, 0u, off); }
DI bool flags_ok(u32x4 v, bool act, Tag tg) { return __all(!act || (v[0] == tg.a && v[1] == tg.b && v[2] == tg.a && v[3] == tg.b)); }
DI void flags_spin(rsrc_t rs_ws, unsigned voff, bool act, Tag tg) {
  for (unsigned spins = 0; spins < SPIN_CAP; ++spins) {
    u32x4 v = ld16c_asm(rs_ws, voff, 0u); wait_vm_tie<0>(v);
    if (flags_ok(v, act, tg)) break;
    __builtin_amdgcn_s_sleep(1);
  }
}
DI void word_set(int i, unsigned v) { *(volatile LAS unsigned*)(lds + L_WORD + i * 4) = v; }
DI unsigned word_get(int i) { return __builtin_amdgcn_readfirstlane(*(volatile LAS unsigned*)(lds + L_WORD + i * 4)); }

struct AttnSrc { unsigned q, kv; int sb0; };
DI AttnSrc attn_src(int rb, int hp, int w) {
  AttnSrc a;
  a.q = __builtin_amdgcn_readfirstlane(WS_QKV + (unsigned)((2 * hp + (w >> 2)) * 64 + 4 * rb + (w & 3)) * 1024u);
  a.kv = __builtin_amdgcn_readfirstlane(WS_QKV + (unsigned)(((1 + (w >> 2)) * 32 + 2 * hp + ((w >> 1) & 1)) * 64) * 1024u);
  a.sb0 = __builtin_amdgcn_readfirstlane(4 * rb - 8 + 6 * (w & 1));
  return a;
}
DI void attn_issue(rsrc_t rs_ws, const AttnSrc& a, int w, int lane, int piece, bool go) {
  const unsigned vo = (unsigned)lane * 16u + (go ? 0u : 0x40000000u);
  if (piece == 0) { dma16c(LA_Q + (unsigned)w * 1024u, vo, rs_ws, a.q); return; }
  const int sbk = a.sb0 + piece - 1;
  dma16c(LA_K + (unsigned)((w >> 1) * 12 + 6 * (w & 1) + piece - 1) * 1024u, vo, rs_ws, a.kv + (unsigned)(sbk < 0 ? 0 : sbk) * 1024u);
}
DI unsigned qkv_flag_voff(int rb, int hp, int lane, bool& act) {
  const int c = lane >> 2, kind = c == 0 ? 0 : (c <= 3 ? 1 : 2), d = c == 0 ? 0 : (c - 1) % 3, rr = rb - d;
  act = lane < 28 && rr >= 0;
  return act ? WS_FQKV + (unsigned)((rr * 16 + (kind * 16 + hp) / 3) * 64 + (lane & 3) * 16) : 0u;
}

struct Pro { unsigned vx[2], vq[2]; };
DI void prologue(const Ptrs& P, int rb, int g, int rot, int w, int lane, Pro& o) {
  const rsrc_t rs_x = mk_rsrc(P.x, SEQ * DM * 2), rs_q = mk_rsrc(P.wqkv, DM * 1536 * 2);
  const int nj = w >> 2, mi = w & 3;
#pragma unroll
  for (int j = 0; j < 2; ++j) o.vx[j] = (unsigned)(16 * w + 8 * j + (lane >> 3)) * 1024u + (unsigned)(((lane & 7) ^ (4 * j + (lane >> 4))) * 16);
#pragma unroll
  for (int j = 0; j < 2; ++j) { const int G = (nj ? 2 * mi + j : 8 + mi) * 64 + lane;
    o.vq[j] = (unsigned)(G / 12) * 3072u + (unsigned)(G % 12) * 16u; }
  const unsigned sx0 = (unsigned)rb * 131072u, sq0 = (unsigned)g * 192u;
  const unsigned dq0 = 16384u + (unsigned)(nj ? 2 * mi : 8 + mi) * 1024u;
#pragma unroll
  for (int sg = 0; sg < P1_NPRO; ++sg) {
    const unsigned slot = (unsigned)sg * SLOT1, c = (unsigned)(sg + rot) & 7u;
    dma16(slot + (unsigned)(2 * w) * 1024u, o.vx[0], rs_x, sx0 + c * 128u);
    dma16(slot + (unsigned)(2 * w + 1) * 1024u, o.vx[1], rs_x, sx0 + c * 128u);
    dma16(slot + dq0, o.vq[0], rs_q, sq0 + c * (64u * 3072u));
    dma16(nj ? slot + dq0 + 1024u : L_DUMMY + 4096u, nj ? o.vq[1] : 0x40000000u, rs_q, sq0 + c * (64u * 3072u));
  }
}

#define TLBW_N 0
#define PFX_N 0
#define PF3_N 0
#define PF3_T 33
#define P0_T 99
#define ATT_B 14
#define P1_HOLD 12
constexpr int OP_NONE = -1, OP_PFW = 200, OP_ATT = 400;
template <int NJ> struct Sched1 {
  static constexpr int NA = NJ ? 4 : 3, NST = NJ ? 2 : 4;
  static constexpr int n(int sg) { return sg < 8 ? NA : 3; }
  static constexpr int TB(int s) { return s < 8 ? 4 * s + 2 : 4 * s + 3; }
  static constexpr int extras(int T) { return (T == 9 ? 1 : 0)   + (T == 24 ? 1 : 0)   + (T == 39 ? 1 : 0)  ; }
  static constexpr int NPRO = P1_NPRO;
  static constexpr int NTW = TLBW_N;
  static constexpr int NPX = PFX_N;
  static constexpr int PRO = 1 + NPX + NPRO * 4 + 1 + NTW;
  static constexpr int HOLD = P1_HOLD;
  int op[64][2] = {}, before[65] = {}, last[16] = {};
  constexpr Sched1() {
    int sg = NPRO, j = 0, pf = 0, att = 0, c = PRO;
    for (int i = 0; i < NPRO; ++i) last[i] = 1 + 4 * (i + 1);
    for (int T = 0; T < 64; ++T) {
      before[T] = c;
      for (int k = 0; k < 2; ++k) {
        int o = OP_NONE;
        if (k == 0 || T < 14) {
          if (sg <= 15 && (sg < 5 || T >= TB(sg - 4)) && (sg < 8 || T >= 4 * sg - HOLD)) {   o = sg * 8 + j; if (++j == n(sg)) { last[sg] = c + 1; ++sg; j = 0; } }
          else if (pf == 0 && T >= P0_T) o = OP_PFW + pf++;
          else if (pf >= 1 && pf < 1 + PF3_N && T >= PF3_T) o = OP_PFW + pf++;
        }
        if (T >= 4 * ATT_B + 3 && att < 7 && (k == 0 || T >= 61)) o = OP_ATT + att++;
        op[T][k] = o;
        if (o != OP_NONE) ++c;
      }
      c += extras(T);
      if (T == 31) c += NST;
    }
    before[64] = c;
  }
  constexpr int wait_stage(int T, int sg) const { return before[T] - last[sg]; }
};

template <int NJ>
DI bool phase1(const Ptrs& P, const Pro& pro, int rb, int g, int rot, int mi, int lane, int rb2, int hp2, Tag& tg, u64& epoch, Stamps& T_) {
  Stamps& T = T_;
  typedef Sched1<NJ> L;
  static constexpr Sched1<NJ> SC{};
  const int w = NJ * 4 + mi;
  const rsrc_t rs_x = mk_rsrc(P.x, SEQ * DM * 2), rs_q = mk_rsrc(P.wqkv, DM * 1536 * 2);
  const rsrc_t rs_f = mk_rsrc(NJ ? P.wu : P.wg, DM * FF * 2), rs_ws = mk_rsrc(P.ws, WS_BYTES);
  const unsigned sx0 = (unsigned)rb * 131072u, sq0 = (unsigned)g * 192u;
  constexpr int NQ = NJ ? 2 : 1;
  constexpr int NA = 2 + NQ;
  const unsigned vx[2] = {pro.vx[0], pro.vx[1]}, vq[2] = {pro.vq[0], pro.vq[1]};
  unsigned vf[3];
  const int u0 = (g * NU) >> 4, nu = (((g + 1) * NU) >> 4) - u0;
  const unsigned sf0 = (unsigned)u0 * 32u;
  auto piece = [&](int gs, int j) {
    const unsigned slot = (unsigned)(gs % 5) * SLOT1;
    const unsigned c = (unsigned)(gs + rot) & 7u;
    if (gs < 8) {
      if (j < 2) dma16(slot + (unsigned)(2 * w + j) * 1024u, vx[j], rs_x, sx0 + c * 128u);
      else dma16(slot + 16384u + (unsigned)(NJ ? 2 * mi + j - 2 : 8 + mi) * 1024u, vq[j - 2], rs_q, sq0 + c * (64u * 3072u));
    } else dma16(slot + NJ * 12288u + (unsigned)(3 * mi + j) * 1024u, vf[j], rs_f, sf0 + c * (64u * 2752u));
  };
  const unsigned myrow = (unsigned)(128 * rb + 8 * g + w);
  u32x4 xrow = ld16_asm(rs_x, (unsigned)lane * 16u, myrow * 1024u), ev;
  if (w == 0) ev = ld16c_asm(rs_ws, 0u, WS_EPOCH);
  STAMP(1);
#pragma unroll
  for (int j = 0; j < 3; ++j) { const int G = (3 * mi + j) * 64 + lane, kg = G / 48, rr = G % 48, unit = (rr >> 3) ^ NJ;
    vf[j] = unit < nu ? (unsigned)(4 * kg + ((rr & 7) >> 1)) * 2752u + (unsigned)(unit * 32 + (rr & 1) * 16) : 0x40000000u; }
  const int r = lane & 31, h = lane >> 5, nsub = (lane >> 4) & 1, q4 = (lane & 15) >> 2, p4 = lane & 3;
  unsigned xa[4], ro[4];
#pragma unroll
  for (int p = 0; p < 4; ++p) { const int ks = (p + mi) & 3;
    xa[p] = (unsigned)(32 * mi + r) * 128u + (unsigned)(((2 * ks + h) ^ ((r >> 1) & 7)) * 16);
    ro[p] = (unsigned)ks * 3072u; }
  const unsigned wl = (unsigned)(2 * h) * 768u + q4 * 32u + p4 * 8u;
  const unsigned wa = 16384u + (unsigned)(2 * h) * 768u + q4 * 192u + nsub * 32u + p4 * 8u;
  const unsigned wbE = nsub * 12288u + wl + nsub * 128u;
  const unsigned wbO = nsub * 12288u + wl - nsub * 128u;
  const AttnSrc asrc = attn_src(rb2, hp2, w);
  bool early = false;
  auto warm = [&](int i) {
    const unsigned xl = blockIdx.x >> 3, dst = L_DUMMY + (unsigned)w * 256u;
    if (i == 0) {
      const unsigned q = (unsigned)(w & 1) * 64u + (unsigned)lane, rr = q / 7u, ln = q - rr * 7u;
      const unsigned ub = (unsigned)((((blockIdx.x & 3) * 4 * NU) >> 4) * 32) & ~127u;
      pf_lines(dst, (w < 4 && q < 112u) ? (16u * xl + rr) * 2752u + ub + ln * 128u : 0x40000000u, mk_rsrc((w & 2) ? P.wu : P.wg, DM * FF * 2));
    } else {
      const unsigned q = xl * 8u + (unsigned)w + 256u * (unsigned)(i - 1);
      dma16(L_DUMMY + 2048u + (unsigned)w * 1024u, (unsigned)lane * 16u + (q < 1888u ? 0u : 0x40000000u), mk_rsrc(q < 1376u ? P.wd : P.wo, q < 1376u ? FF * DM * 2 : DM * DM * 2), (q < 1376u ? q : q - 1376u) * 1024u);
    }
  };
  auto op_at = [&](auto tc, auto kc) {
    constexpr int o = SC.op[decltype(tc)::value][decltype(kc)::value];
    if constexpr (o >= OP_ATT) attn_issue(rs_ws, asrc, w, lane, o - OP_ATT, early);
    else if constexpr (o >= OP_PFW) warm(o - OP_PFW);
    else if constexpr (o >= 0) piece(o >> 3, o & 7);
  };
  bf16x8 sel;
  { const unsigned v = (lane & 15) == ((lane >> 4) & 1) ? ONES2 : 0u; sel = __builtin_bit_cast(bf16x8, u32x4{v, v, v, v}); }

  bf16x8 xf[32];
  constexpr int NB1 = NJ ? 1 : 2;
  f32x16 acc[3];
  f32x4 cs[3];
#pragma unroll
  for (int b = 0; b < 3; ++b) { cs[b] = f32x4{0.f, 0.f, 0.f, 0.f};
#pragma unroll
    for (int i = 0; i < 16; ++i) acc[b][i] = 0.f; }
  bf16x8 wf[3][NB1], wg[2][3];
  auto ldx = [&](int t) { xf[t] = lds_b128((unsigned)((t >> 2) % 5) * SLOT1 + xa[t & 3]); };
  auto ld1a = [&](int t, int b) {
    const unsigned a = (unsigned)((t >> 2) % 5) * SLOT1 + wa + ro[t & 3] + (NJ ? 2 : b) * 64u; wf[t % 3][b] = cat(lds_tr(a), lds_tr(a + 768u));
  };
  auto ld1b = [&](int t, int b) {
    const int u = NJ * 3 + b;
    const unsigned a = (unsigned)((8 + (t >> 2)) % 5) * SLOT1 + ((u & 1) ? wbO : wbE) + ro[t & 3] + u * 128u; wg[t & 1][b] = cat(lds_tr(a), lds_tr(a + 768u));
  };
  auto cs_put = [&](int nb, int b0) {
#pragma unroll
    for (int b = 0; b < 3; ++b) if (b < nb && lane < 16) { *(LAS float*)(lds + L_CSP + (((b0 + b) * 32 + lane) * 4 + mi) * 4) = cs[b][0]; *(LAS float*)(lds + L_CSP + (((b0 + b) * 32 + 16 + lane) * 4 + mi) * 4) = cs[b][1]; }
  };
  auto cs_get = [&](int nb, int b0) {
#pragma unroll
    for (int b = 0; b < 3; ++b) if (b < nb) { const f32x4 c4 = lds_f4(L_CSP + ((b0 + b) * 32 + r) * 16);
      *(LAS float*)(lds + L_CSW + w * 384 + b * 128 + r * 4) = (c4[0] + c4[1]) + (c4[2] + c4[3]); }
  };
  wait_vm<(L::NPRO - 1) * 4 + 1>();
  bar_raw();
  STAMP(2); CSTAMP(23);
  ldx(0); ldx(1);
#pragma unroll
  for (int b = 0; b < NB1; ++b) { ld1a(0, b); ld1a(1, b); }
  u32x4 sv;
  float rmean = 0.f, rrstd = 0.f;
  const unsigned sv_off = WS_RS + (unsigned)(128 * rb + 32 * mi) * 16u;
  static_for<0, 32>([&](auto tc) {
    constexpr int t = decltype(tc)::value, s = t >> 2, p = t & 3;
    if constexpr (p == 2) {
      if (s == 7) cs_put(NB1, NJ ? 2 : 0);
      if constexpr (s == 1) {
        wait_vm_tie<(SC.wait_stage(t, 2) < SC.before[t] - L::PRO ? SC.wait_stage(t, 2) : SC.before[t] - L::PRO)>(xrow);
        if (w == 0) { tie(ev); word_set(2, ev[0]); word_set(3, ev[1]); }
      } else wait_vm<SC.wait_stage(t, s + 1)>();
      if (s == 7 || s == 1) bar(); else bar_raw();
      if constexpr (s == 1) {
        const u64 e0 = (u64)word_get(2) | ((u64)word_get(3) << 32);
        epoch = e0 + 1ull;
        tg.a = (unsigned)epoch; tg.b = ((unsigned)(e0 >> 32) ^ 0x5bd1e995u) + tg.a;
      }
      STAMP2(24 + s);
      if (s == 7) cs_get(NB1, NJ ? 2 : 0);
      if (s == 1) CSTAMP(60); if (s == 4) CSTAMP(61); if (s == 7) CSTAMP(62);
    }
    if (NJ) op_at(tc, IC<0>{});
    static_for<0, NB1>([&](auto bc) {
      constexpr int b = decltype(bc)::value;
      acc[b] = mfma(wf[t % 3][b], xf[t], acc[b]);
      if (t < 30) { if (b == 0) ldx(t + 2); ld1a(t + 2, b); }
      if (t == 31) { ld1b(0, b); if (b == NB1 - 1) { for (int bb = NB1; bb < 3; ++bb) ld1b(0, bb); } }
      if (p == (NJ ? 1 : b)) cs[b] = mfma16(sel, wf[t % 3][b], cs[b]);
      if (b == 0) {
        if (t == 6 + NJ) {
          float e[8];
#pragma unroll
          for (int i = 0; i < 4; ++i) { e[2 * i] = bf_lo(xrow[i]); e[2 * i + 1] = bf_hi(xrow[i]); }
          rmean = wave_sum(((e[0] + e[1]) + (e[2] + e[3])) + ((e[4] + e[5]) + (e[6] + e[7]))) * (1.f / 512.f);
          asm volatile("" : "+v"(rmean));
        }
        if (t == 8) {
          float q = 0.f;
#pragma unroll
          for (int i = 0; i < 4; ++i) { const float d0 = bf_lo(xrow[i]) - rmean, d1 = bf_hi(xrow[i]) - rmean; q = fmaf(d0, d0, q); q = fmaf(d1, d1, q); }
          rrstd = __builtin_amdgcn_rsqf(wave_sum(q) * (1.f / 512.f) + 1e-5f);
          asm volatile("" : "+v"(rrstd));
        }
      }
      SB0();
      if (b == 0) op_at(tc, IC<NJ ? 1 : 0>{});
    });
    if (!NJ) op_at(tc, IC<1>{});
    if (t == 24) sv = ld16c_asm(rs_ws, (unsigned)r * 16u, sv_off);
    if (t == 9 && lane == 0) st16c(u32x4{__float_as_uint(rmean), tg.a, __float_as_uint(rrstd), tg.b}, rs_ws, 0u, WS_RS + myrow * 16u);
    SB0();
  });
  STAMP(3); CSTAMP(22);
  { constexpr int after = SC.before[32] - L::NST - SC.before[25]; wait_vm_tie<after>(sv); }
  NOTE(53, __all(sv[1] == tg.a && sv[3] == tg.b) ? 1 : 2);
  for (unsigned spins = 0; !__all(sv[1] == tg.a && sv[3] == tg.b) && spins < SPIN_CAP; ++spins) {
    __builtin_amdgcn_s_sleep(1);
    sv = ld16c_asm(rs_ws, (unsigned)r * 16u, sv_off); wait_vm_tie<0>(sv);
  }
  const float mean = __uint_as_float(sv[0]), rstd = __uint_as_float(sv[2]);
  const int sb = rb * 4 + mi;
#pragma unroll
  for (int b = 0; b < NB1; ++b) {
    const int blk = 3 * g + (NJ ? 2 : b), which = blk >> 4, hp = blk & 15;
    const float sc = which == 0 ? rstd * (0.25f * 1.4426950408889634f) : rstd;
    float v[16];
#pragma unroll
    for (int i = 0; i < 4; ++i) { const f32x4 c4 = lds_f4(L_CSW + w * 384 + b * 128 + (8 * i + 4 * h) * 4);
#pragma unroll
      for (int e = 0; e < 4; ++e) v[4 * i + e] = (acc[b][4 * i + e] - mean * c4[e]) * sc; }
    const unsigned vo = which == 2 ? (unsigned)(h * 512 + ((r ^ (4 * h)) * 16)) : (unsigned)(lane * 16);
#pragma unroll
    for (int hd = 0; hd < 2; ++hd) {
      const u32x4 o = {pk(v[8 * hd], v[8 * hd + 1]), pk(v[8 * hd + 2], v[8 * hd + 3]), pk(v[8 * hd + 4], v[8 * hd + 5]), pk(v[8 * hd + 6], v[8 * hd + 7])};
      st16c(o, rs_ws, vo, WS_QKV + (unsigned)((which * 32 + 2 * hp + hd) * 64 + sb) * 1024u);
    }
  }
  STAMP(4); CSTAMP(18);
#pragma unroll
  for (int b = 0; b < 3; ++b) { cs[b] = f32x4{0.f, 0.f, 0.f, 0.f};
#pragma unroll
    for (int i = 0; i < 16; ++i) acc[b][i] = 0.f; }
  bool act;
  const unsigned qv = qkv_flag_voff(rb2, hp2, lane, act);
  u32x4 fq;
  SB0();
  static_for<32, 64>([&](auto tc) {
    constexpr int Tg = decltype(tc)::value, t = Tg - 32, s = Tg >> 2, p = t & 3;
    if constexpr (p == 3) {
      if (s == 15) cs_put(3, NJ * 3);
      if constexpr (s == 9) wait_vm<(SC.before[Tg] - SC.before[32] < SC.wait_stage(Tg, 10) ? SC.before[Tg] - SC.before[32] : SC.wait_stage(Tg, 10))>();
      else if constexpr (s == ATT_B) { wait_vm<0>(); if (w == 0) { tie(fq); word_set(0, flags_ok(fq, act, tg) ? 1u : 0u); } }
      else if constexpr (s <= 14) wait_vm<SC.wait_stage(Tg, s + 1)>();
      if (s == ATT_B || s == 15) bar(); else bar_raw();
      STAMP2(24 + s);
      if (s == 8) CSTAMP(56); if (s == 11) CSTAMP(57); if (s == 13) CSTAMP(58); if (s == 15) CSTAMP(59);
      if (s == ATT_B) early = word_get(0) != 0u;
      if (s == 15) cs_get(3, NJ * 3);
    }
    if (NJ) op_at(tc, IC<0>{});
    static_for<0, 3>([&](auto bc) {
      constexpr int b = decltype(bc)::value;
      acc[b] = mfma(wg[t & 1][b], xf[t], acc[b]);
      if (t < 31) ld1b(t + 1, b);
      if (p == ((b + 2 * NJ) & 3) || (s == 15 && p == 2 && ((b + 2 * NJ) & 3) == 3)) { if (!(s == 15 && p == 3)) cs[b] = mfma16(sel, wg[t & 1][b], cs[b]); }
      SB0();
    });
    if (!NJ) op_at(tc, IC<0>{});
    op_at(tc, IC<1>{});
    if (Tg == 39) flag_store(rs_ws, WS_FQKV + (unsigned)((rb * 16 + g) * 64 + w * 8), tg, lane);
    if (Tg == 4 * ATT_B - 1 && w == 0) fq = ld16c_asm(rs_ws, qv, 0u);
    SB0();
  });
  STAMP(5); CSTAMP(19);
  const float r2 = rstd * rstd;
  f32x4 cq[3][4];
#pragma unroll
  for (int b = 0; b < 3; ++b)
#pragma unroll
    for (int i = 0; i < 4; ++i) cq[b][i] = lds_f4(L_CSW + w * 384 + b * 128 + (8 * i + 4 * h) * 4);
#pragma unroll
  for (int b = 0; b < 3; ++b) {
    float v[16];
#pragma unroll
    for (int i = 0; i < 4; ++i)
#pragma unroll
      for (int e = 0; e < 4; ++e) v[4 * i + e] = acc[b][4 * i + e] - mean * cq[b][i][e];
    float hv[8];
#pragma unroll
    for (int i = 0; i < 8; ++i) hv[i] = fmaxf(v[i], 0.f) * v[8 + i] * r2;
    const u32x4 o = {pk(hv[0], hv[1]), pk(hv[2], hv[3]), pk(hv[4], hv[5]), pk(hv[6], hv[7])};
    st16c(o, rs_ws, (unsigned)lane * 16u + (NJ * 3 + b < nu ? 0u : 0x40000000u), WS_H + (unsigned)(sb * NU + u0 + NJ * 3 + b) * 1024u);
  }
  STAMP(6);
  wait_vm<3>();
  return early;
}

template <bool FULL, class F1, class F3, class F2>
DI void attn_pair(int j0, int qb, int lane, F1 side1, F3 side2, F2 head_done, u32x4 (&o)[2]) {
  const int q = lane & 31, hh = lane >> 5, tq = q - 4 * hh;
  const float NEG = -__builtin_inff();
  bf16x8 qf[2];
#pragma unroll
  for (int hd = 0; hd < 2; ++hd) qf[hd] = lds_b128(LA_Q + (unsigned)(hd * 4 + qb) * 1024u + lane * 16u);
  unsigned kb = LA_K + (unsigned)qb * 1024u + lane * 16u;
  auto ldk = [&](int hd, int j) { return lds_b128(kb + (unsigned)(hd * 12 + j) * 1024u); };
  auto pin = [&]() { asm volatile("" : "+v"(kb)); };
  auto on = [&](int j) { return FULL || j >= j0; };
  f32x16 c[2], s[2];
  bf16x8 kf[2];
#pragma unroll
  for (int i = 0; i < 16; ++i) c[0][i] = 0.f;
  float m0[2], m1[2];
  {
    f32x16 s8[2], s0[2];
#pragma unroll
    for (int hd = 0; hd < 2; ++hd) { s8[hd] = mfma(ldk(hd, 8), qf[hd], c[0]); if (FULL) s0[hd] = mfma(ldk(hd, 0), qf[hd], c[0]); }
    if (on(1)) { kf[0] = ldk(0, 1); kf[1] = ldk(1, 1); }
    SB0();
#pragma unroll
    for (int hd = 0; hd < 2; ++hd) {
      m0[hd] = NEG; m1[hd] = NEG;
#pragma unroll
      for (int i = 0; i < 16; i += 2) {
        const int c0 = (i & 3) + 8 * (i >> 2);
        const float a = c0 <= tq ? s8[hd][i] : (FULL ? s0[hd][i] : NEG), b = c0 + 1 <= tq ? s8[hd][i + 1] : (FULL ? s0[hd][i + 1] : NEG);
        if (i & 2) m1[hd] = fmaxf(fmaxf(m1[hd], a), b); else m0[hd] = fmaxf(fmaxf(m0[hd], a), b);
      }
      asm volatile("" : "+v"(m0[hd]), "+v"(m1[hd]));
      if (hd == 0) { if (on(1)) s[0] = mfma(kf[0], qf[0], c[0]); if (on(2)) kf[0] = ldk(0, 2); }
      SB0();
    }
  }
  static_for<0, 14>([&](auto nc) {
    constexpr int n = decltype(nc)::value, hd = n & 1, j = 1 + (n >> 1), hn = (n + 1) & 1, jn = 1 + ((n + 1) >> 1);
    pin();
    if (n + 1 < 14) { if (on(jn)) s[hn] = mfma(kf[hn], qf[hn], c[0]); if (jn < 7 && on(jn + 1)) kf[hn] = ldk(hn, jn + 1); }
    SB0();
    if (on(j)) {
#pragma unroll
      for (int i = 0; i < 8; i += 2) { m0[hd] = fmaxf(fmaxf(m0[hd], s[hd][i]), s[hd][i + 1]); m1[hd] = fmaxf(fmaxf(m1[hd], s[hd][8 + i]), s[hd][9 + i]); }
      asm volatile("" : "+v"(m0[hd]), "+v"(m1[hd]));
    }
    side1(n);
    SB0();
  });
#pragma unroll
  for (int hd = 0; hd < 2; ++hd) { const float m = xhalf_max(fmaxf(m0[hd], m1[hd]));
#pragma unroll
    for (int i = 0; i < 16; ++i) c[hd][i] = -m; }
  const int grp = (lane >> 4) & 1, q4 = (lane & 15) >> 2, p4 = lane & 3;
  const unsigned vl = (unsigned)((p4 & 1) * 512 + (((4 * hh + q4) ^ (4 * (p4 & 1))) * 16) + (p4 >> 1) * 8);
  unsigned vb[2];
#pragma unroll
  for (int hd = 0; hd < 2; ++hd) vb[hd] = grp ? L_ONES + 128u : LA_V + (unsigned)(hd * 12 + qb) * 1024u + vl;
  bf16x8 vf[2][2];
  auto ldv = [&](int hd, int j) {
#pragma unroll
    for (int u = 0; u < 2; ++u) vf[hd][u] = cat(lds_tr(vb[hd] + j * 1024u + u * 256u), lds_tr(vb[hd] + j * 1024u + u * 256u + 128u));
  };
  f32x16 acc[2];
#pragma unroll
  for (int hd = 0; hd < 2; ++hd)
#pragma unroll
    for (int i = 0; i < 16; ++i) acc[hd][i] = 0.f;
  auto pv = [&](int hd, const unsigned (&pp)[8]) {
#pragma unroll
    for (int u = 0; u < 2; ++u) acc[hd] = mfma(vf[hd][u], __builtin_bit_cast(bf16x8, u32x4{pp[4 * u], pp[4 * u + 1], pp[4 * u + 2], pp[4 * u + 3]}), acc[hd]);
  };
  if (on(1)) { kf[0] = ldk(0, 1); kf[1] = ldk(1, 1); s[0] = mfma(kf[0], qf[0], c[0]); ldv(0, 1); }
  if (on(2)) kf[0] = ldk(0, 2);
  static_for<0, 14>([&](auto nc) {
    constexpr int n = decltype(nc)::value, hd = n & 1, j = 1 + (n >> 1), hn = (n + 1) & 1, jn = 1 + ((n + 1) >> 1);
    pin();
    if (n + 1 < 14) { if (on(jn)) { s[hn] = mfma(kf[hn], qf[hn], c[hn]); ldv(hn, jn); } if (jn < 7 && on(jn + 1)) kf[hn] = ldk(hn, jn + 1); }
    SB0();
    if (on(j)) {
      unsigned pp[8];
#pragma unroll
      for (int i = 0; i < 8; ++i) pp[i] = pk(__builtin_amdgcn_exp2f(s[hd][2 * i]), __builtin_amdgcn_exp2f(s[hd][2 * i + 1]));
      pv(hd, pp);
    }
    side2(n);
    SB0();
  });
  pin();
  f32x16 s8[2], s0[2];
#pragma unroll
  for (int hd = 0; hd < 2; ++hd) { s8[hd] = mfma(ldk(hd, 8), qf[hd], c[hd]); if (FULL) s0[hd] = mfma(ldk(hd, 0), qf[hd], c[hd]); }
#pragma unroll
  for (int hd = 0; hd < 2; ++hd) {
    ldv(hd, 8);
    unsigned p8[8], p0[8];
#pragma unroll
    for (int i = 0; i < 8; ++i) {
      const int c0 = ((2 * i) & 3) + 8 * (i >> 1);
      const bool va = c0 <= tq, vb_ = c0 + 1 <= tq;
      const float a = va ? s8[hd][2 * i] : (FULL ? s0[hd][2 * i] : NEG), b = vb_ ? s8[hd][2 * i + 1] : (FULL ? s0[hd][2 * i + 1] : NEG);
      const unsigned pp = pk(__builtin_amdgcn_exp2f(a), __builtin_amdgcn_exp2f(b));
      const unsigned mk = (va ? 0xffffu : 0u) | (vb_ ? 0xffff0000u : 0u);
      p8[i] = pp & mk; p0[i] = pp & ~mk;
    }
    pv(hd, p8);
    if (FULL) { ldv(hd, 0); pv(hd, p0); }
    const float inv = __builtin_amdgcn_rcpf(acc[hd][8]);
    o[hd] = u32x4{pk(acc[hd][0] * inv, acc[hd][1] * inv), pk(acc[hd][2] * inv, acc[hd][3] * inv), pk(acc[hd][4] * inv, acc[hd][5] * inv), pk(acc[hd][6] * inv, acc[hd][7] * inv)};
    head_done(hd);
  }
}

constexpr int DW = 10, DA = 6, NR = 12, NK3 = 30, NDN = 22, OCHK = NDN - (DA - 1), OPOLL = OCHK - 6, XLD = 18;
struct Sched3 {
  int park[NR] = {}, before[NK3 + 1] = {}, lastW[NK3] = {}, lastA[NK3] = {};
  constexpr Sched3() {
    int c = 0;
    for (int i = 0; i < DW - 1; ++i) { c += 2; lastW[i] = c; }
    c += 1;
    for (int i = 0; i < NR; ++i) { park[i] = c - lastW[i]; c += 2; lastW[i + DW - 1] = c; }
    c += 2; lastW[NR + DW - 1] = c;
    for (int i = 0; i < DA - 1; ++i) { c += 2; lastA[i] = c; }
    for (int i = 0; i < NK3; ++i) {
      before[i] = c;
      if (i > NR && i + DW - 1 < NK3) { c += 2; lastW[i + DW - 1] = c; }
      if (i + DA - 1 < NK3) { c += 2; lastA[i + DA - 1] = c; }
      if (i == XLD) c += 4;
    }
    before[NK3] = c;
  }
  constexpr int wait(int i) const { const int w = i + 1 < NK3 ? lastW[i + 1] : 0; return before[i] - (lastA[i] > w ? lastA[i] : w); }
};
DI unsigned l3w(int kq) { return kq < 3 ? 86016u + (unsigned)kq * (DW * 2048u) : 0u; }

DI void phase23(const Ptrs& P, bool early, int rb1, int g1, u64 epoch, int rb, int hp, int rb64, int cb, int rot, int w, int lane, Tag tg, Stamps& T) {
  const rsrc_t rs_ws = mk_rsrc(P.ws, WS_BYTES);
  for (unsigned i = threadIdx.x; i < ONES_BYTES / 16; i += 512) *(LAS u32x4*)(lds + L_ONES + i * 16) = u32x4{ONES2, ONES2, ONES2, ONES2};
  NOTE(63, early ? 1 : 2);
  if (!early) {
    bool act; const unsigned qv = qkv_flag_voff(rb, hp, lane, act);
    if (w == 0) flags_spin(rs_ws, qv, act, tg);
    bar();
    const AttnSrc asrc = attn_src(rb, hp, w);
#pragma unroll
    for (int i = 0; i < 7; ++i) attn_issue(rs_ws, asrc, w, lane, i, true);
    wait_vm<0>();
  }
  bar();
  STAMP(9);
  const unsigned fh_off = WS_FH + (unsigned)((rb1 * 16 + g1) * 64 + w * 8);
  const int h = lane >> 5, nsub = (lane >> 4) & 1, q4 = (lane & 15) >> 2, p4 = lane & 3;
  f32x16 acc[4];
  u32x2 xr[4];
  const int kq = w - 4;
  const bool keeper = blockIdx.x == 0 && w == 4;
  u32x4 fe[4];
  const int ulo = kq < 3 ? 22 * kq : 65, nun = kq < 2 ? 22 : 21;
  const unsigned ring = __builtin_amdgcn_readfirstlane(l3w(kq));
  if (w < 4) {
    __builtin_amdgcn_s_setprio(3);
    const int sb = 4 * rb + w;
    const unsigned o_off = WS_O + (unsigned)(sb * 32 + 2 * hp) * 1024u, fo_off = WS_FO + (unsigned)((rb * 16 + hp) * 64 + w * 8);
    auto side1 = [&](int n) { if (n == 13) STAMP(10); };
    auto side_h = [&](int n) { if (n == 2) { wait_vm<0>(); flag_store(rs_ws, fh_off, tg, lane); } };
    u32x4 o[2];
    auto store_o = [&](int hd) { st16c(o[hd], rs_ws, (unsigned)lane * 16u, o_off + (unsigned)hd * 1024u); };
    if (sb >= 8) attn_pair<true>(0, w, lane, side1, side_h, store_o, o); else attn_pair<false>(8 - sb, w, lane, side1, side_h, store_o, o);
    STAMP(11);
    wait_vm<0>();
    flag_store(rs_ws, fo_off, tg, lane); flag_store(rs_ws, fo_off + 32u, tg, lane);
    STAMP(12);
  } else {
    const rsrc_t rs_d = mk_rsrc(P.wd, FF * DM * 2), rs_o = mk_rsrc(P.wo, DM * DM * 2);
    unsigned vw[2];
#pragma unroll
    for (int j = 0; j < 2; ++j) vw[j] = (unsigned)(8 * j + (lane >> 3)) * 1024u + (unsigned)(((lane & 7) ^ (((lane >> 4) & 1) << 2)) * 16);
    const unsigned vo = (unsigned)lane * 16u;
    auto unit = [&](int i) { const int r = i + rot; return i < NDN ? (i < nun ? ulo + (r >= nun ? r - nun : r) : -1) : 8 * kq + ((i - NDN + rot) & 7); };
    auto reqW = [&](int i) {
      const int u = unit(i);
#pragma unroll
      for (int j = 0; j < 2; ++j) dma16(ring + (unsigned)(i % DW) * 2048u + j * 1024u, u < 0 ? 0x40000000u : vw[j], i < NDN ? rs_d : rs_o, (unsigned)cb * 128u + (unsigned)(u < 0 ? 0 : u) * 16384u);
    };
    const unsigned hv = WS_FH + (unsigned)((rb64 >> 1) * 1024 + lane * 16), ov = WS_FO + (unsigned)((rb64 >> 1) * 1024 + lane * 16);
    const int pg = lane >> 2;
    const bool pair_ok = ((lane & 3) & 1) == (rb64 & 1);
    const bool h_act = pair_ok && (((pg + 1) * NU) >> 4) > ulo && ((pg * NU) >> 4) < ulo + nun, o_act = pair_ok && (pg >> 2) == kq;
    unsigned wa[2];
#pragma unroll
    for (int nb = 0; nb < 2; ++nb) wa[nb] = ring + (unsigned)h * 512u + q4 * 128u + (unsigned)((nb ^ (q4 >> 1)) * 64) + nsub * 32u + p4 * 8u;
    auto rdw = [&](int i, int nb) { const unsigned a = wa[nb] + (unsigned)(i % DW) * 2048u; return cat(lds_tr(a), lds_tr(a + 1024u)); };
    static constexpr Sched3 S3{};
#pragma unroll
    for (int i = 0; i < DW - 1; ++i) reqW(i);
    wait_vm<2 * (DW - 1)>(); flag_store(rs_ws, fh_off, tg, lane);
    bf16x8 wr[NR][2];
    static_for<0, NR>([&](auto ic) {
      constexpr int i = decltype(ic)::value;
      wait_vm<S3.park[i]>();
      wr[i][0] = rdw(i, 0); wr[i][1] = rdw(i, 1);
      reqW(i + DW - 1);
      asm volatile("s_waitcnt lgkmcnt(0)" : "+v"(wr[i][0]), "+v"(wr[i][1]) :: "memory");
    });
    reqW(NR + DW - 1);
    flags_spin(rs_ws, hv, h_act, tg);
    STAMP(13);
    u32x4 A[DA][2];
    auto reqA = [&](int i) {
      const int u = unit(i);
#pragma unroll
      for (int mb = 0; mb < 2; ++mb)
        A[i % DA][mb] = ld16c_asm(rs_ws, u < 0 ? 0x40000000u : vo, i < NDN ? WS_H + (unsigned)((2 * rb64 + mb) * NU + (u < 0 ? 0 : u)) * 1024u : WS_O + (unsigned)((2 * rb64 + mb) * 32 + u) * 1024u);
    };
#pragma unroll
    for (int i = 0; i < DA - 1; ++i) reqA(i);
#pragma unroll
    for (int b = 0; b < 4; ++b)
#pragma unroll
      for (int i = 0; i < 16; ++i) acc[b][i] = 0.f;
    const int row = 64 * rb64 + 32 * (kq >> 1) + (lane & 31), col0 = 64 * cb + 32 * (kq & 1) + 4 * h;
    const unsigned short* xp = (const unsigned short*)P.x + (size_t)row * DM + col0;
    u32x4 fo;
    bf16x8 wf[2][2];
    static_for<0, NK3>([&](auto ic) {
      constexpr int i = decltype(ic)::value;
      wait_vm<S3.wait(i)>();
      tie(A[i % DA][0]); tie(A[i % DA][1]);
      if (i == 0) STAMP(14);
      if constexpr (i == OCHK) {
        wait_vm_tie<S3.before[i] - S3.before[OPOLL + 1]>(fo);
        NOTE(54, flags_ok(fo, o_act, tg) ? 1 : 2);
        if (!flags_ok(fo, o_act, tg)) flags_spin(rs_ws, ov, o_act, tg);
      }
      const bf16x8 w0 = i < NR ? wr[i < NR ? i : 0][0] : wf[i & 1][0], w1 = i < NR ? wr[i < NR ? i : 0][1] : wf[i & 1][1];
      SB0();
      acc[0] = mfma(w0, __builtin_bit_cast(bf16x8, A[i % DA][0]), acc[0]);
      if constexpr (i + 1 >= NR && i + 1 < NK3) { wf[(i + 1) & 1][0] = rdw(i + 1, 0); wf[(i + 1) & 1][1] = rdw(i + 1, 1); }
      SB0();
      acc[1] = mfma(w1, __builtin_bit_cast(bf16x8, A[i % DA][0]), acc[1]);
      SB0();
      if constexpr (i > NR && i + DW - 1 < NK3) reqW(i + DW - 1);
      acc[2] = mfma(w0, __builtin_bit_cast(bf16x8, A[i % DA][1]), acc[2]);
      SB0();
      acc[3] = mfma(w1, __builtin_bit_cast(bf16x8, A[i % DA][1]), acc[3]);
      SB0();
      if constexpr (i + DA - 1 < NK3) reqA(i + DA - 1);
      if (i == OPOLL) fo = ld16c_asm(rs_ws, ov, 0u);
      if (i == XLD) {
#pragma unroll
        for (int q = 0; q < 4; ++q) xr[q] = *(const GAS u32x2*)(xp + 8 * q);
      }
      if (i == 20 && keeper) {
#pragma unroll
        for (int q = 0; q < 4; ++q) fe[q] = ld16c_asm(rs_ws, WS_FQKV + (unsigned)(q * 64 + lane) * 64u, 0u);
      }
      SB0();
    });
    STAMP(15);
  }
  if (w >= 4) {
#pragma unroll
    for (int b = 0; b < 4; ++b) {
      if (b != kq) {
        const unsigned base = ring + (unsigned)((b - kq - 1 + 4) % 4) * 4096u + lane * 16u;
#pragma unroll
        for (int q = 0; q < 4; ++q) *(LAS f32x4*)(lds + base + q * 1024u) = f32x4{acc[b][4 * q], acc[b][4 * q + 1], acc[b][4 * q + 2], acc[b][4 * q + 3]};
      }
    }
  }
  bar();
  if (w >= 4) {
    f32x16 r = kq == 0 ? acc[0] : (kq == 1 ? acc[1] : (kq == 2 ? acc[2] : acc[3]));
#pragma unroll
    for (int s = 1; s < 4; ++s) {
      const int src = (kq + s) & 3;
      const unsigned base = l3w(src) + (unsigned)((kq - src - 1 + 4) % 4) * 4096u + lane * 16u;
#pragma unroll
      for (int q = 0; q < 4; ++q) { const f32x4 v = lds_f4(base + q * 1024u);
#pragma unroll
        for (int e = 0; e < 4; ++e) r[4 * q + e] += v[e]; }
    }
    const int row = 64 * rb64 + 32 * (kq >> 1) + (lane & 31), col0 = 64 * cb + 32 * (kq & 1);
    unsigned o[8];
#pragma unroll
    for (int q = 0; q < 4; ++q) { o[2 * q] = pk(r[4 * q] + bf_lo(xr[q][0]), r[4 * q + 1] + bf_hi(xr[q][0])); o[2 * q + 1] = pk(r[4 * q + 2] + bf_lo(xr[q][1]), r[4 * q + 3] + bf_hi(xr[q][1])); }
    unsigned short* op = (unsigned short*)P.out + (size_t)row * DM + col0 + 8 * h;
#pragma unroll
    for (int q = 0; q < 4; q += 2) {
      const auto t0 = __builtin_amdgcn_permlane32_swap(o[2 * q], o[2 * q + 2], false, false), t1 = __builtin_amdgcn_permlane32_swap(o[2 * q + 1], o[2 * q + 3], false, false);
      *(GAS u32x4*)(op + 8 * q) = u32x4{t0[0], t1[0], t0[1], t1[1]};
    }
    if (keeper) {
#pragma unroll
      for (int q = 0; q < 4; ++q) { tie(fe[q]); if (!flags_ok(fe[q], true, tg)) flags_spin(rs_ws, WS_FQKV + (unsigned)(q * 64 + lane) * 64u, true, tg); }
      if (lane == 0) __hip_atomic_store((GAS u64*)(P.ws + WS_EPOCH), epoch, __ATOMIC_RELAXED, __HIP_MEMORY_SCOPE_AGENT);
    }
  }
}

__global__ __launch_bounds__(512, 2) void mk_kernel(const void* x, const void* wqkv, const void* wo, const void* wg, const void* wu, const void* wd, void* out, char* ws) {
  u64 pc0 = 0;
  asm volatile("mk_code_begin:\n\ts_getpc_b64 %0" : "=s"(pc0));
  {
    unsigned len;
    asm volatile("s_mov_b32 %0, mk_code_end-mk_code_begin" : "=s"(len));
    const unsigned wv = __builtin_amdgcn_readfirstlane(threadIdx.x >> 6);
    const unsigned per = ((len >> 5) + 1023u) & ~1023u;
    const unsigned o = (blockIdx.x >> 3) * per + wv * 1024u;
    dma16(L_DUMMY + wv * 1024u, (threadIdx.x & 63u) * 16u, mk_rsrc((const void*)(pc0 & ~255ull), len & ~15u), wv * 1024u < per ? o : 0x40000000u);
  }
  const Ptrs P{x, wqkv, wo, wg, wu, wd, out, ws, pc0};
  Stamps T;
  STAMP(0); CSTAMP(20);
  __builtin_amdgcn_s_setprio(1);
  const int b = blockIdx.x, xcd = b & 7, idx = b >> 3;
  const int w = __builtin_amdgcn_readfirstlane(threadIdx.x >> 6), lane = threadIdx.x & 63;
  Tag tg; u64 epoch;
  const int rb = 2 * (idx >> 2) + (xcd >> 2), g = (xcd & 3) * 4 + (idx & 3), rot1 = ((idx >> 2) + 2 * (idx & 3)) & 7;
  bool early;
  Pro pro;
  prologue(P, rb, g, rot1, w, lane, pro);
  if (w < 4) early = phase1<0>(P, pro, rb, g, rot1, w, lane, rb, g, tg, epoch, T); else early = phase1<1>(P, pro, rb, g, rot1, w - 4, lane, rb, g, tg, epoch, T);
  const int rot3 = ((idx & 7) * 2 + 5 * (idx >> 3)) % 21;
  phase23(P, early, rb, g, epoch, rb, g, xcd * 4 + (idx >> 3), idx & 7, rot3, w, lane, tg, T);
  asm volatile("mk_code_end:");
}

}

extern "C" void kernel_launch(void* const* d_in, const int* in_sizes, int n_in, void* d_out, int out_size, void* d_ws, size_t ws_size, hipStream_t stream) {
  const void *x = d_in[0], *wqkv = d_in[1], *wo = d_in[2], *wg = d_in[4], *wu = d_in[5], *wd = d_in[6];
  mk_kernel<<<256, 512, 0, stream>>>(x, wqkv, wo, wg, wu, wd, d_out, (char*)d_ws);
}
```

```cpp
#include <hip/hip_runtime.h>
#include <hip/hip_bf16.h>
#include <stdint.h>

namespace {

typedef short bf16x8 __attribute__((ext_vector_type(8)));
typedef short s16x4 __attribute__((ext_vector_type(4)));
typedef float f32x16 __attribute__((ext_vector_type(16)));
typedef float f32x4 __attribute__((ext_vector_type(4)));
typedef float f32x2 __attribute__((ext_vector_type(2)));
typedef unsigned u32x4 __attribute__((ext_vector_type(4)));
typedef unsigned u32x2 __attribute__((ext_vector_type(2)));
typedef __bf16 bf16x2_t __attribute__((ext_vector_type(2)));
typedef short v4i16_t __attribute__((ext_vector_type(4)));
typedef __amdgpu_buffer_rsrc_t rsrc_t;
#define LAS __attribute__((address_space(3)))
#define GAS __attribute__((address_space(1)))
#define DI __device__ __forceinline__
#define SB0() __builtin_amdgcn_sched_barrier(0)
typedef unsigned long long u64;

constexpr int SEQ = 2048, DM = 512, FF = 1376, NU = 86;
constexpr unsigned WS_EPOCH = 0;
constexpr unsigned WS_FQKV = 16384, WS_FH = 32768, WS_FO = 49152;
constexpr unsigned WS_RS = 65536;
constexpr unsigned WS_QKV = 1u << 20;
constexpr unsigned WS_H = 7u << 20;
constexpr unsigned WS_O = 13u << 20;
constexpr unsigned WS_BYTES = 16u << 20;
constexpr int NPRO = 3;
constexpr unsigned SLOT1 = 28672;
constexpr unsigned L_CSP = 143360;
constexpr unsigned L_CSW = 146432;
constexpr unsigned L_ONES = 152576;
constexpr unsigned ONES_BYTES = 9984;
constexpr unsigned L_DUMMY = 152576;
constexpr unsigned L_WORD = 163584;
constexpr unsigned LDS_BYTES = 163840;
constexpr unsigned LA_Q = 28672, LA_K = LA_Q + 8192, LA_V = LA_K + 24576;

__shared__ __attribute__((aligned(1024))) char lds[LDS_BYTES];

DI unsigned pk(float a, float b) { f32x2 v = {a, b}; bf16x2_t r = __builtin_convertvector(v, bf16x2_t); return __builtin_bit_cast(unsigned, r); }
DI rsrc_t mk_rsrc(const void* p, unsigned bytes) { return __builtin_amdgcn_make_buffer_rsrc((void*)p, 0, bytes, 0x00020000); }
DI void dma16(unsigned ldsaddr, unsigned voff, rsrc_t rs, unsigned soff) {
  asm volatile("s_mov_b32 m0, %0\n\ts_nop 0\n\tbuffer_load_dwordx4 %1, %2, %3 offen lds" ::"s"(ldsaddr), "v"(voff), "s"(rs), "s"(soff) : "memory");
}
DI void dma16c(unsigned ldsaddr, unsigned voff, rsrc_t rs, unsigned soff) {
  asm volatile("s_mov_b32 m0, %0\n\ts_nop 0\n\tbuffer_load_dwordx4 %1, %2, %3 offen sc1 lds" ::"s"(ldsaddr), "v"(voff), "s"(rs), "s"(soff) : "memory");
}
template <int N> DI void wait_vm() { asm volatile("s_waitcnt vmcnt(%0)" ::"n"(N) : "memory"); }
template <int N> DI void wait_vm_tie(u32x4& v) { asm volatile("s_waitcnt vmcnt(%1)" : "+v"(v) : "n"(N) : "memory"); }
template <int V> struct IC { static constexpr int value = V; };
template <int I, int N, class F> DI void static_for(F&& f) { if constexpr (I < N) { f(IC<I>{}); static_for<I + 1, N>(f); } }
DI void bar_raw() { asm volatile("s_barrier" ::: "memory"); }
DI void tie(u32x4& v) { asm volatile("" : "+v"(v) :: "memory"); }
DI void bar() { asm volatile("s_waitcnt lgkmcnt(0)\n\ts_barrier" ::: "memory"); }
DI f32x16 mfma(bf16x8 a, bf16x8 b, f32x16 c) { return __builtin_amdgcn_mfma_f32_32x32x16_bf16(a, b, c, 0, 0, 0); }
DI bf16x8 lds_b128(unsigned a) { return *(const LAS bf16x8*)(lds + a); }
DI s16x4 lds_tr(unsigned a) { return __builtin_amdgcn_ds_read_tr16_b64_v4i16((LAS v4i16_t*)(lds + a)); }
DI f32x4 mfma16(bf16x8 a, bf16x8 b, f32x4 c) { return __builtin_amdgcn_mfma_f32_16x16x32_bf16(a, b, c, 0, 0, 0); }
DI f32x4 lds_f4(unsigned a) { return *(const LAS f32x4*)(lds + a); }
DI bf16x8 cat(s16x4 lo, s16x4 hi) { return __builtin_shufflevector(lo, hi, 0, 1, 2, 3, 4, 5, 6, 7); }
DI float xhalf_sum(float v) { auto r = __builtin_amdgcn_permlane32_swap(__float_as_uint(v), __float_as_uint(v), false, false); return __uint_as_float(r[0]) + __uint_as_float(r[1]); }
DI float xhalf_max(float v) { auto r = __builtin_amdgcn_permlane32_swap(__float_as_uint(v), __float_as_uint(v), false, false); return fmaxf(__uint_as_float(r[0]), __uint_as_float(r[1])); }
DI void st16c(u32x4 v, rsrc_t rs, unsigned voff, unsigned soff) { __builtin_amdgcn_raw_buffer_store_b128(v, rs, voff + soff, 0, 16); }
DI void st8c(u32x2 v, rsrc_t rs, unsigned voff, unsigned soff) { __builtin_amdgcn_raw_buffer_store_b64(v, rs, voff, soff, 16); }
constexpr unsigned ONES2 = 0x3F803F80u;
constexpr unsigned SPIN_CAP = 1u << 19;
template <int CTRL> DI float dpp_add(float v) { return v + __int_as_float(__builtin_amdgcn_update_dpp(0, __float_as_int(v), CTRL, 0xf, 0xf, true)); }
DI float wave_sum(float v) {
  v = dpp_add<0xB1>(v); v = dpp_add<0x4E>(v); v = dpp_add<0x141>(v); v = dpp_add<0x140>(v);
  auto a = __builtin_amdgcn_permlane16_swap(__float_as_uint(v), __float_as_uint(v), false, false);
  v = __uint_as_float(a[0]) + __uint_as_float(a[1]);
  return xhalf_sum(v);
}
DI float bf_lo(unsigned u) { return __uint_as_float(u << 16); }
DI float bf_hi(unsigned u) { return __uint_as_float(u & 0xffff0000u); }
DI u32x4 ld16_asm(rsrc_t rs, unsigned voff, unsigned soff) { u32x4 v; asm volatile("buffer_load_dwordx4 %0, %1, %2, %3 offen" : "=v"(v) : "v"(voff), "s"(rs), "s"(soff) : "memory"); return v; }
DI u32x4 ld16c_asm(rsrc_t rs, unsigned voff, unsigned soff) { u32x4 v; asm volatile("buffer_load_dwordx4 %0, %1, %2, %3 offen sc1" : "=v"(v) : "v"(voff), "s"(rs), "s"(soff) : "memory"); return v; }
struct Ptrs { const void *x, *wqkv, *wo, *wg, *wu, *wd; void* out; char* ws; };
struct Tag { unsigned a, b; };

DI void flag_store(rsrc_t rs_ws, unsigned off, Tag tg, int lane) { if (lane == 0) st8c(u32x2{tg.a, tg.b}, rs_ws, 0u, off); }
DI bool flags_ok(u32x4 v, bool act, Tag tg) { return __all(!act || (v[0] == tg.a && v[1] == tg.b && v[2] == tg.a && v[3] == tg.b)); }
DI void flags_spin(rsrc_t rs_ws, unsigned voff, bool act, Tag tg) {
  for (unsigned spins = 0; spins < SPIN_CAP; ++spins) {
    u32x4 v = ld16c_asm(rs_ws, voff, 0u); wait_vm_tie<0>(v);
    if (flags_ok(v, act, tg)) break;
    __builtin_amdgcn_s_sleep(1);
  }
}
DI void word_set(int i, unsigned v) { *(volatile LAS unsigned*)(lds + L_WORD + i * 4) = v; }
DI unsigned word_get(int i) { return __builtin_amdgcn_readfirstlane(*(volatile LAS unsigned*)(lds + L_WORD + i * 4)); }

struct AttnSrc { unsigned q, kv; int sb0; };
DI AttnSrc attn_src(int rb, int hp, int w) {
  AttnSrc a;
  a.q = __builtin_amdgcn_readfirstlane(WS_QKV + (unsigned)((2 * hp + (w >> 2)) * 64 + 4 * rb + (w & 3)) * 1024u);
  a.kv = __builtin_amdgcn_readfirstlane(WS_QKV + (unsigned)(((1 + (w >> 2)) * 32 + 2 * hp + ((w >> 1) & 1)) * 64) * 1024u);
  a.sb0 = __builtin_amdgcn_readfirstlane(4 * rb - 8 + 6 * (w & 1));
  return a;
}
DI void attn_issue(rsrc_t rs_ws, const AttnSrc& a, int w, int lane, int piece, bool go) {
  const unsigned vo = (unsigned)lane * 16u + (go ? 0u : 0x40000000u);
  if (piece == 0) { dma16c(LA_Q + (unsigned)w * 1024u, vo, rs_ws, a.q); return; }
  const int sbk = a.sb0 + piece - 1;
  dma16c(LA_K + (unsigned)((w >> 1) * 12 + 6 * (w & 1) + piece - 1) * 1024u, vo, rs_ws, a.kv + (unsigned)(sbk < 0 ? 0 : sbk) * 1024u);
}
DI unsigned qkv_flag_voff(int rb, int hp, int lane, bool& act) {
  const int c = lane >> 2, kind = c == 0 ? 0 : (c <= 3 ? 1 : 2), d = c == 0 ? 0 : (c - 1) % 3, rr = rb - d;
  act = lane < 28 && rr >= 0;
  return act ? WS_FQKV + (unsigned)((rr * 16 + (kind * 16 + hp) / 3) * 64 + (lane & 3) * 16) : 0u;
}

struct Pro { unsigned vx[2], vq[2]; };
DI void prologue(const Ptrs& P, int rb, int g, int rot, int w, int lane, Pro& o) {
  const rsrc_t rs_x = mk_rsrc(P.x, SEQ * DM * 2), rs_q = mk_rsrc(P.wqkv, DM * 1536 * 2);
  const int nj = w >> 2, mi = w & 3;
#pragma unroll
  for (int j = 0; j < 2; ++j) o.vx[j] = (unsigned)(16 * w + 8 * j + (lane >> 3)) * 1024u + (unsigned)(((lane & 7) ^ (4 * j + (lane >> 4))) * 16);
#pragma unroll
  for (int j = 0; j < 2; ++j) { const int G = (nj ? 2 * mi + j : 8 + mi) * 64 + lane;
    o.vq[j] = (unsigned)(G / 12) * 3072u + (unsigned)(G % 12) * 16u; }
  const unsigned sx0 = (unsigned)rb * 131072u, sq0 = (unsigned)g * 192u;
  const unsigned dq0 = 16384u + (unsigned)(nj ? 2 * mi : 8 + mi) * 1024u;
#pragma unroll
  for (int sg = 0; sg < NPRO; ++sg) {
    const unsigned slot = (unsigned)sg * SLOT1, c = (unsigned)(sg + rot) & 7u;
    dma16(slot + (unsigned)(2 * w) * 1024u, o.vx[0], rs_x, sx0 + c * 128u);
    dma16(slot + (unsigned)(2 * w + 1) * 1024u, o.vx[1], rs_x, sx0 + c * 128u);
    dma16(slot + dq0, o.vq[0], rs_q, sq0 + c * (64u * 3072u));
    dma16(nj ? slot + dq0 + 1024u : L_DUMMY + 4096u, nj ? o.vq[1] : 0x40000000u, rs_q, sq0 + c * (64u * 3072u));
  }
}

constexpr int ATT_B = 14;
constexpr int OP_NONE = -1, OP_ATT = 400;
template <int NJ> struct Sched1 {
  static constexpr int NA = NJ ? 4 : 3, NST = NJ ? 2 : 4;
  static constexpr int n(int sg) { return sg < 8 ? NA : 3; }
  static constexpr int TB(int s) { return s < 8 ? 4 * s + 2 : 4 * s + 3; }
  static constexpr int extras(int T) { return (T == 9 ? 1 : 0)   + (T == 24 ? 1 : 0)   + (T == 39 ? 1 : 0)  ; }
  static constexpr int PRO = 1 + NPRO * 4 + 1;
  static constexpr int HOLD = 12;
  int op[64][2] = {}, before[65] = {}, last[16] = {};
  constexpr Sched1() {
    int sg = NPRO, j = 0, att = 0, c = PRO;
    for (int i = 0; i < NPRO; ++i) last[i] = 1 + 4 * (i + 1);
    for (int T = 0; T < 64; ++T) {
      before[T] = c;
      for (int k = 0; k < 2; ++k) {
        int o = OP_NONE;
        if (k == 0 || T < 14) {
          if (sg <= 15 && (sg < 5 || T >= TB(sg - 4)) && (sg < 8 || T >= 4 * sg - HOLD)) {   o = sg * 8 + j; if (++j == n(sg)) { last[sg] = c + 1; ++sg; j = 0; } }
        }
        if (T >= 4 * ATT_B + 3 && att < 7 && (k == 0 || T >= 61)) o = OP_ATT + att++;
        op[T][k] = o;
        if (o != OP_NONE) ++c;
      }
      c += extras(T);
      if (T == 31) c += NST;
    }
    before[64] = c;
  }
  constexpr int wait_stage(int T, int sg) const { return before[T] - last[sg]; }
};

template <int NJ>
DI bool phase1(const Ptrs& P, const Pro& pro, int rb, int g, int rot, int mi, int lane, int rb2, int hp2, Tag& tg, u64& epoch) {
  typedef Sched1<NJ> L;
  static constexpr Sched1<NJ> SC{};
  const int w = NJ * 4 + mi;
  const rsrc_t rs_x = mk_rsrc(P.x, SEQ * DM * 2), rs_q = mk_rsrc(P.wqkv, DM * 1536 * 2);
  const rsrc_t rs_f = mk_rsrc(NJ ? P.wu : P.wg, DM * FF * 2), rs_ws = mk_rsrc(P.ws, WS_BYTES);
  const unsigned sx0 = (unsigned)rb * 131072u, sq0 = (unsigned)g * 192u;
  constexpr int NQ = NJ ? 2 : 1;
  constexpr int NA = 2 + NQ;
  const unsigned vx[2] = {pro.vx[0], pro.vx[1]}, vq[2] = {pro.vq[0], pro.vq[1]};
  unsigned vf[3];
  const int u0 = (g * NU) >> 4, nu = (((g + 1) * NU) >> 4) - u0;
  const unsigned sf0 = (unsigned)u0 * 32u;
  auto piece = [&](int gs, int j) {
    const unsigned slot = (unsigned)(gs % 5) * SLOT1;
    const unsigned c = (unsigned)(gs + rot) & 7u;
    if (gs < 8) {
      if (j < 2) dma16(slot + (unsigned)(2 * w + j) * 1024u, vx[j], rs_x, sx0 + c * 128u);
      else dma16(slot + 16384u + (unsigned)(NJ ? 2 * mi + j - 2 : 8 + mi) * 1024u, vq[j - 2], rs_q, sq0 + c * (64u * 3072u));
    } else dma16(slot + NJ * 12288u + (unsigned)(3 * mi + j) * 1024u, vf[j], rs_f, sf0 + c * (64u * 2752u));
  };
  const unsigned myrow = (unsigned)(128 * rb + 8 * g + w);
  u32x4 xrow = ld16_asm(rs_x, (unsigned)lane * 16u, myrow * 1024u), ev;
  if (w == 0) ev = ld16c_asm(rs_ws, 0u, WS_EPOCH);
#pragma unroll
  for (int j = 0; j < 3; ++j) { const int G = (3 * mi + j) * 64 + lane, kg = G / 48, rr = G % 48, unit = (rr >> 3) ^ NJ;
    vf[j] = unit < nu ? (unsigned)(4 * kg + ((rr & 7) >> 1)) * 2752u + (unsigned)(unit * 32 + (rr & 1) * 16) : 0x40000000u; }
  const int r = lane & 31, h = lane >> 5, nsub = (lane >> 4) & 1, q4 = (lane & 15) >> 2, p4 = lane & 3;
  unsigned xa[4], ro[4];
#pragma unroll
  for (int p = 0; p < 4; ++p) { const int ks = (p + mi) & 3;
    xa[p] = (unsigned)(32 * mi + r) * 128u + (unsigned)(((2 * ks + h) ^ ((r >> 1) & 7)) * 16);
    ro[p] = (unsigned)ks * 3072u; }
  const unsigned wl = (unsigned)(2 * h) * 768u + q4 * 32u + p4 * 8u;
  const unsigned wa = 16384u + (unsigned)(2 * h) * 768u + q4 * 192u + nsub * 32u + p4 * 8u;
  const unsigned wbE = nsub * 12288u + wl + nsub * 128u;
  const unsigned wbO = nsub * 12288u + wl - nsub * 128u;
  const AttnSrc asrc = attn_src(rb2, hp2, w);
  bool early = false;
  auto op_at = [&](auto tc, auto kc) {
    constexpr int o = SC.op[decltype(tc)::value][decltype(kc)::value];
    if constexpr (o >= OP_ATT) attn_issue(rs_ws, asrc, w, lane, o - OP_ATT, early);
    else if constexpr (o >= 0) piece(o >> 3, o & 7);
  };
  bf16x8 sel;
  { const unsigned v = (lane & 15) == ((lane >> 4) & 1) ? ONES2 : 0u; sel = __builtin_bit_cast(bf16x8, u32x4{v, v, v, v}); }

  bf16x8 xf[32];
  constexpr int NB1 = NJ ? 1 : 2;
  f32x16 acc[3];
  f32x4 cs[3];
#pragma unroll
  for (int b = 0; b < 3; ++b) { cs[b] = f32x4{0.f, 0.f, 0.f, 0.f};
#pragma unroll
    for (int i = 0; i < 16; ++i) acc[b][i] = 0.f; }
  bf16x8 wf[3][NB1], wg[2][3];
  auto ldx = [&](int t) { xf[t] = lds_b128((unsigned)((t >> 2) % 5) * SLOT1 + xa[t & 3]); };
  auto ld1a = [&](int t, int b) {
    const unsigned a = (unsigned)((t >> 2) % 5) * SLOT1 + wa + ro[t & 3] + (NJ ? 2 : b) * 64u; wf[t % 3][b] = cat(lds_tr(a), lds_tr(a + 768u));
  };
  auto ld1b = [&](int t, int b) {
    const int u = NJ * 3 + b;
    const unsigned a = (unsigned)((8 + (t >> 2)) % 5) * SLOT1 + ((u & 1) ? wbO : wbE) + ro[t & 3] + u * 128u; wg[t & 1][b] = cat(lds_tr(a), lds_tr(a + 768u));
  };
  auto cs_put = [&](int nb, int b0) {
#pragma unroll
    for (int b = 0; b < 3; ++b) if (b < nb && lane < 16) { *(LAS float*)(lds + L_CSP + (((b0 + b) * 32 + lane) * 4 + mi) * 4) = cs[b][0]; *(LAS float*)(lds + L_CSP + (((b0 + b) * 32 + 16 + lane) * 4 + mi) * 4) = cs[b][1]; }
  };
  auto cs_get = [&](int nb, int b0) {
#pragma unroll
    for (int b = 0; b < 3; ++b) if (b < nb) { const f32x4 c4 = lds_f4(L_CSP + ((b0 + b) * 32 + r) * 16);
      *(LAS float*)(lds + L_CSW + w * 384 + b * 128 + r * 4) = (c4[0] + c4[1]) + (c4[2] + c4[3]); }
  };
  wait_vm<(NPRO - 1) * 4 + 1>();
  bar_raw();
  ldx(0); ldx(1);
#pragma unroll
  for (int b = 0; b < NB1; ++b) { ld1a(0, b); ld1a(1, b); }
  u32x4 sv;
  float rmean = 0.f, rrstd = 0.f;
  const unsigned sv_off = WS_RS + (unsigned)(128 * rb + 32 * mi) * 16u;
  static_for<0, 32>([&](auto tc) {
    constexpr int t = decltype(tc)::value, s = t >> 2, p = t & 3;
    if constexpr (p == 2) {
      if (s == 7) cs_put(NB1, NJ ? 2 : 0);
      if constexpr (s == 1) {
        wait_vm_tie<(SC.wait_stage(t, 2) < SC.before[t] - L::PRO ? SC.wait_stage(t, 2) : SC.before[t] - L::PRO)>(xrow);
        if (w == 0) { tie(ev); word_set(2, ev[0]); word_set(3, ev[1]); }
      } else wait_vm<SC.wait_stage(t, s + 1)>();
      if (s == 7 || s == 1) bar(); else bar_raw();
      if constexpr (s == 1) {
        const u64 e0 = (u64)word_get(2) | ((u64)word_get(3) << 32);
        epoch = e0 + 1ull;
        tg.a = (unsigned)epoch; tg.b = ((unsigned)(e0 >> 32) ^ 0x5bd1e995u) + tg.a;
      }
      if (s == 7) cs_get(NB1, NJ ? 2 : 0);
    }
    if (NJ) op_at(tc, IC<0>{});
    static_for<0, NB1>([&](auto bc) {
      constexpr int b = decltype(bc)::value;
      acc[b] = mfma(wf[t % 3][b], xf[t], acc[b]);
      if (t < 30) { if (b == 0) ldx(t + 2); ld1a(t + 2, b); }
      if (t == 31) { ld1b(0, b); if (b == NB1 - 1) { for (int bb = NB1; bb < 3; ++bb) ld1b(0, bb); } }
      if (p == (NJ ? 1 : b)) cs[b] = mfma16(sel, wf[t % 3][b], cs[b]);
      if (b == 0) {
        if (t == 6 + NJ) {
          float e[8];
#pragma unroll
          for (int i = 0; i < 4; ++i) { e[2 * i] = bf_lo(xrow[i]); e[2 * i + 1] = bf_hi(xrow[i]); }
          rmean = wave_sum(((e[0] + e[1]) + (e[2] + e[3])) + ((e[4] + e[5]) + (e[6] + e[7]))) * (1.f / 512.f);
          asm volatile("" : "+v"(rmean));
        }
        if (t == 8) {
          float q = 0.f;
#pragma unroll
          for (int i = 0; i < 4; ++i) { const float d0 = bf_lo(xrow[i]) - rmean, d1 = bf_hi(xrow[i]) - rmean; q = fmaf(d0, d0, q); q = fmaf(d1, d1, q); }
          rrstd = __builtin_amdgcn_rsqf(wave_sum(q) * (1.f / 512.f) + 1e-5f);
          asm volatile("" : "+v"(rrstd));
        }
      }
      SB0();
      if (b == 0) op_at(tc, IC<NJ ? 1 : 0>{});
    });
    if (!NJ) op_at(tc, IC<1>{});
    if (t == 24) sv = ld16c_asm(rs_ws, (unsigned)r * 16u, sv_off);
    if (t == 9 && lane == 0) st16c(u32x4{__float_as_uint(rmean), tg.a, __float_as_uint(rrstd), tg.b}, rs_ws, 0u, WS_RS + myrow * 16u);
    SB0();
  });
  { constexpr int after = SC.before[32] - L::NST - SC.before[25]; wait_vm_tie<after>(sv); }
  for (unsigned spins = 0; !__all(sv[1] == tg.a && sv[3] == tg.b) && spins < SPIN_CAP; ++spins) {
    __builtin_amdgcn_s_sleep(1);
    sv = ld16c_asm(rs_ws, (unsigned)r * 16u, sv_off); wait_vm_tie<0>(sv);
  }
  const float mean = __uint_as_float(sv[0]), rstd = __uint_as_float(sv[2]);
  const int sb = rb * 4 + mi;
#pragma unroll
  for (int b = 0; b < NB1; ++b) {
    const int blk = 3 * g + (NJ ? 2 : b), which = blk >> 4, hp = blk & 15;
    const float sc = which == 0 ? rstd * (0.25f * 1.4426950408889634f) : rstd;
    float v[16];
#pragma unroll
    for (int i = 0; i < 4; ++i) { const f32x4 c4 = lds_f4(L_CSW + w * 384 + b * 128 + (8 * i + 4 * h) * 4);
#pragma unroll
      for (int e = 0; e < 4; ++e) v[4 * i + e] = (acc[b][4 * i + e] - mean * c4[e]) * sc; }
    const unsigned vo = which == 2 ? (unsigned)(h * 512 + ((r ^ (4 * h)) * 16)) : (unsigned)(lane * 16);
#pragma unroll
    for (int hd = 0; hd < 2; ++hd) {
      const u32x4 o = {pk(v[8 * hd], v[8 * hd + 1]), pk(v[8 * hd + 2], v[8 * hd + 3]), pk(v[8 * hd + 4], v[8 * hd + 5]), pk(v[8 * hd + 6], v[8 * hd + 7])};
      st16c(o, rs_ws, vo, WS_QKV + (unsigned)((which * 32 + 2 * hp + hd) * 64 + sb) * 1024u);
    }
  }
#pragma unroll
  for (int b = 0; b < 3; ++b) { cs[b] = f32x4{0.f, 0.f, 0.f, 0.f};
#pragma unroll
    for (int i = 0; i < 16; ++i) acc[b][i] = 0.f; }
  bool act;
  const unsigned qv = qkv_flag_voff(rb2, hp2, lane, act);
  u32x4 fq;
  SB0();
  static_for<32, 64>([&](auto tc) {
    constexpr int Tg = decltype(tc)::value, t = Tg - 32, s = Tg >> 2, p = t & 3;
    if constexpr (p == 3) {
      if (s == 15) cs_put(3, NJ * 3);
      if constexpr (s == 9) wait_vm<(SC.before[Tg] - SC.before[32] < SC.wait_stage(Tg, 10) ? SC.before[Tg] - SC.before[32] : SC.wait_stage(Tg, 10))>();
      else if constexpr (s == ATT_B) { wait_vm<0>(); if (w == 0) { tie(fq); word_set(0, flags_ok(fq, act, tg) ? 1u : 0u); } }
      else if constexpr (s <= 14) wait_vm<SC.wait_stage(Tg, s + 1)>();
      if (s == ATT_B || s == 15) bar(); else bar_raw();
      if (s == ATT_B) early = word_get(0) != 0u;
      if (s == 15) cs_get(3, NJ * 3);
    }
    if (NJ) op_at(tc, IC<0>{});
    static_for<0, 3>([&](auto bc) {
      constexpr int b = decltype(bc)::value;
      acc[b] = mfma(wg[t & 1][b], xf[t], acc[b]);
      if (t < 31) ld1b(t + 1, b);
      if (p == ((b + 2 * NJ) & 3) || (s == 15 && p == 2 && ((b + 2 * NJ) & 3) == 3)) { if (!(s == 15 && p == 3)) cs[b] = mfma16(sel, wg[t & 1][b], cs[b]); }
      SB0();
    });
    if (!NJ) op_at(tc, IC<0>{});
    op_at(tc, IC<1>{});
    if (Tg == 39) flag_store(rs_ws, WS_FQKV + (unsigned)((rb * 16 + g) * 64 + w * 8), tg, lane);
    if (Tg == 4 * ATT_B - 1 && w == 0) fq = ld16c_asm(rs_ws, qv, 0u);
    SB0();
  });
  const float r2 = rstd * rstd;
  f32x4 cq[3][4];
#pragma unroll
  for (int b = 0; b < 3; ++b)
#pragma unroll
    for (int i = 0; i < 4; ++i) cq[b][i] = lds_f4(L_CSW + w * 384 + b * 128 + (8 * i + 4 * h) * 4);
#pragma unroll
  for (int b = 0; b < 3; ++b) {
    float v[16];
#pragma unroll
    for (int i = 0; i < 4; ++i)
#pragma unroll
      for (int e = 0; e < 4; ++e) v[4 * i + e] = acc[b][4 * i + e] - mean * cq[b][i][e];
    float hv[8];
#pragma unroll
    for (int i = 0; i < 8; ++i) hv[i] = fmaxf(v[i], 0.f) * v[8 + i] * r2;
    const u32x4 o = {pk(hv[0], hv[1]), pk(hv[2], hv[3]), pk(hv[4], hv[5]), pk(hv[6], hv[7])};
    st16c(o, rs_ws, (unsigned)lane * 16u + (NJ * 3 + b < nu ? 0u : 0x40000000u), WS_H + (unsigned)(sb * NU + u0 + NJ * 3 + b) * 1024u);
  }
  wait_vm<3>();
  return early;
}

template <bool FULL, class F1, class F3, class F2>
DI void attn_pair(int j0, int qb, int lane, F1 side1, F3 side2, F2 head_done, u32x4 (&o)[2]) {
  const int q = lane & 31, hh = lane >> 5, tq = q - 4 * hh;
  const float NEG = -__builtin_inff();
  bf16x8 qf[2];
#pragma unroll
  for (int hd = 0; hd < 2; ++hd) qf[hd] = lds_b128(LA_Q + (unsigned)(hd * 4 + qb) * 1024u + lane * 16u);
  unsigned kb = LA_K + (unsigned)qb * 1024u + lane * 16u;
  auto ldk = [&](int hd, int j) { return lds_b128(kb + (unsigned)(hd * 12 + j) * 1024u); };
  auto pin = [&]() { asm volatile("" : "+v"(kb)); };
  auto on = [&](int j) { return FULL || j >= j0; };
  f32x16 c[2], s[2];
  bf16x8 kf[2];
#pragma unroll
  for (int i = 0; i < 16; ++i) c[0][i] = 0.f;
  float m0[2], m1[2];
  {
    f32x16 s8[2], s0[2];
#pragma unroll
    for (int hd = 0; hd < 2; ++hd) { s8[hd] = mfma(ldk(hd, 8), qf[hd], c[0]); if (FULL) s0[hd] = mfma(ldk(hd, 0), qf[hd], c[0]); }
    if (on(1)) { kf[0] = ldk(0, 1); kf[1] = ldk(1, 1); }
    SB0();
#pragma unroll
    for (int hd = 0; hd < 2; ++hd) {
      m0[hd] = NEG; m1[hd] = NEG;
#pragma unroll
      for (int i = 0; i < 16; i += 2) {
        const int c0 = (i & 3) + 8 * (i >> 2);
        const float a = c0 <= tq ? s8[hd][i] : (FULL ? s0[hd][i] : NEG), b = c0 + 1 <= tq ? s8[hd][i + 1] : (FULL ? s0[hd][i + 1] : NEG);
        if (i & 2) m1[hd] = fmaxf(fmaxf(m1[hd], a), b); else m0[hd] = fmaxf(fmaxf(m0[hd], a), b);
      }
      asm volatile("" : "+v"(m0[hd]), "+v"(m1[hd]));
      if (hd == 0) { if (on(1)) s[0] = mfma(kf[0], qf[0], c[0]); if (on(2)) kf[0] = ldk(0, 2); }
      SB0();
    }
  }
  static_for<0, 14>([&](auto nc) {
    constexpr int n = decltype(nc)::value, hd = n & 1, j = 1 + (n >> 1), hn = (n + 1) & 1, jn = 1 + ((n + 1) >> 1);
    pin();
    if (n + 1 < 14) { if (on(jn)) s[hn] = mfma(kf[hn], qf[hn], c[0]); if (jn < 7 && on(jn + 1)) kf[hn] = ldk(hn, jn + 1); }
    SB0();
    if (on(j)) {
#pragma unroll
      for (int i = 0; i < 8; i += 2) { m0[hd] = fmaxf(fmaxf(m0[hd], s[hd][i]), s[hd][i + 1]); m1[hd] = fmaxf(fmaxf(m1[hd], s[hd][8 + i]), s[hd][9 + i]); }
      asm volatile("" : "+v"(m0[hd]), "+v"(m1[hd]));
    }
    side1(n);
    SB0();
  });
#pragma unroll
  for (int hd = 0; hd < 2; ++hd) { const float m = xhalf_max(fmaxf(m0[hd], m1[hd]));
#pragma unroll
    for (int i = 0; i < 16; ++i) c[hd][i] = -m; }
  const int grp = (lane >> 4) & 1, q4 = (lane & 15) >> 2, p4 = lane & 3;
  const unsigned vl = (unsigned)((p4 & 1) * 512 + (((4 * hh + q4) ^ (4 * (p4 & 1))) * 16) + (p4 >> 1) * 8);
  unsigned vb[2];
#pragma unroll
  for (int hd = 0; hd < 2; ++hd) vb[hd] = grp ? L_ONES + 128u : LA_V + (unsigned)(hd * 12 + qb) * 1024u + vl;
  bf16x8 vf[2][2];
  auto ldv = [&](int hd, int j) {
#pragma unroll
    for (int u = 0; u < 2; ++u) vf[hd][u] = cat(lds_tr(vb[hd] + j * 1024u + u * 256u), lds_tr(vb[hd] + j * 1024u + u * 256u + 128u));
  };
  f32x16 acc[2];
#pragma unroll
  for (int hd = 0; hd < 2; ++hd)
#pragma unroll
    for (int i = 0; i < 16; ++i) acc[hd][i] = 0.f;
  auto pv = [&](int hd, const unsigned (&pp)[8]) {
#pragma unroll
    for (int u = 0; u < 2; ++u) acc[hd] = mfma(vf[hd][u], __builtin_bit_cast(bf16x8, u32x4{pp[4 * u], pp[4 * u + 1], pp[4 * u + 2], pp[4 * u + 3]}), acc[hd]);
  };
  if (on(1)) { kf[0] = ldk(0, 1); kf[1] = ldk(1, 1); s[0] = mfma(kf[0], qf[0], c[0]); ldv(0, 1); }
  if (on(2)) kf[0] = ldk(0, 2);
  static_for<0, 14>([&](auto nc) {
    constexpr int n = decltype(nc)::value, hd = n & 1, j = 1 + (n >> 1), hn = (n + 1) & 1, jn = 1 + ((n + 1) >> 1);
    pin();
    if (n + 1 < 14) { if (on(jn)) { s[hn] = mfma(kf[hn], qf[hn], c[hn]); ldv(hn, jn); } if (jn < 7 && on(jn + 1)) kf[hn] = ldk(hn, jn + 1); }
    SB0();
    if (on(j)) {
      unsigned pp[8];
#pragma unroll
      for (int i = 0; i < 8; ++i) pp[i] = pk(__builtin_amdgcn_exp2f(s[hd][2 * i]), __builtin_amdgcn_exp2f(s[hd][2 * i + 1]));
      pv(hd, pp);
    }
    side2(n);
    SB0();
  });
  pin();
  f32x16 s8[2], s0[2];
#pragma unroll
  for (int hd = 0; hd < 2; ++hd) { s8[hd] = mfma(ldk(hd, 8), qf[hd], c[hd]); if (FULL) s0[hd] = mfma(ldk(hd, 0), qf[hd], c[hd]); }
#pragma unroll
  for (int hd = 0; hd < 2; ++hd) {
    ldv(hd, 8);
    unsigned p8[8], p0[8];
#pragma unroll
    for (int i = 0; i < 8; ++i) {
      const int c0 = ((2 * i) & 3) + 8 * (i >> 1);
      const bool va = c0 <= tq, vb_ = c0 + 1 <= tq;
      const float a = va ? s8[hd][2 * i] : (FULL ? s0[hd][2 * i] : NEG), b = vb_ ? s8[hd][2 * i + 1] : (FULL ? s0[hd][2 * i + 1] : NEG);
      const unsigned pp = pk(__builtin_amdgcn_exp2f(a), __builtin_amdgcn_exp2f(b));
      const unsigned mk = (va ? 0xffffu : 0u) | (vb_ ? 0xffff0000u : 0u);
      p8[i] = pp & mk; p0[i] = pp & ~mk;
    }
    pv(hd, p8);
    if (FULL) { ldv(hd, 0); pv(hd, p0); }
    const float inv = __builtin_amdgcn_rcpf(acc[hd][8]);
    o[hd] = u32x4{pk(acc[hd][0] * inv, acc[hd][1] * inv), pk(acc[hd][2] * inv, acc[hd][3] * inv), pk(acc[hd][4] * inv, acc[hd][5] * inv), pk(acc[hd][6] * inv, acc[hd][7] * inv)};
    head_done(hd);
  }
}

constexpr int DW = 10, DA = 6, NR = 12, NK3 = 30, NDN = 22, OCHK = NDN - (DA - 1), OPOLL = OCHK - 6, XLD = 18;
struct Sched3 {
  int park[NR] = {}, before[NK3 + 1] = {}, lastW[NK3] = {}, lastA[NK3] = {};
  constexpr Sched3() {
    int c = 0;
    for (int i = 0; i < DW - 1; ++i) { c += 2; lastW[i] = c; }
    c += 1;
    for (int i = 0; i < NR; ++i) { park[i] = c - lastW[i]; c += 2; lastW[i + DW - 1] = c; }
    c += 2; lastW[NR + DW - 1] = c;
    for (int i = 0; i < DA - 1; ++i) { c += 2; lastA[i] = c; }
    for (int i = 0; i < NK3; ++i) {
      before[i] = c;
      if (i > NR && i + DW - 1 < NK3) { c += 2; lastW[i + DW - 1] = c; }
      if (i + DA - 1 < NK3) { c += 2; lastA[i + DA - 1] = c; }
      if (i == XLD) c += 4;
    }
    before[NK3] = c;
  }
  constexpr int wait(int i) const { const int w = i + 1 < NK3 ? lastW[i + 1] : 0; return before[i] - (lastA[i] > w ? lastA[i] : w); }
};
DI unsigned l3w(int kq) { return kq < 3 ? 86016u + (unsigned)kq * (DW * 2048u) : 0u; }

DI void phase23(const Ptrs& P, bool early, int rb1, int g1, u64 epoch, int rb, int hp, int rb64, int cb, int rot, int w, int lane, Tag tg) {
  const rsrc_t rs_ws = mk_rsrc(P.ws, WS_BYTES);
  for (unsigned i = threadIdx.x; i < ONES_BYTES / 16; i += 512) *(LAS u32x4*)(lds + L_ONES + i * 16) = u32x4{ONES2, ONES2, ONES2, ONES2};
  if (!early) {
    bool act; const unsigned qv = qkv_flag_voff(rb, hp, lane, act);
    if (w == 0) flags_spin(rs_ws, qv, act, tg);
    bar();
    const AttnSrc asrc = attn_src(rb, hp, w);
#pragma unroll
    for (int i = 0; i < 7; ++i) attn_issue(rs_ws, asrc, w, lane, i, true);
    wait_vm<0>();
  }
  bar();
  const unsigned fh_off = WS_FH + (unsigned)((rb1 * 16 + g1) * 64 + w * 8);
  const int h = lane >> 5, nsub = (lane >> 4) & 1, q4 = (lane & 15) >> 2, p4 = lane & 3;
  f32x16 acc[4];
  u32x2 xr[4];
  const int kq = w - 4;
  const bool keeper = blockIdx.x == 0 && w == 4;
  u32x4 fe[4];
  const int ulo = kq < 3 ? 22 * kq : 65, nun = kq < 2 ? 22 : 21;
  const unsigned ring = __builtin_amdgcn_readfirstlane(l3w(kq));
  if (w < 4) {
    __builtin_amdgcn_s_setprio(3);
    const int sb = 4 * rb + w;
    const unsigned o_off = WS_O + (unsigned)(sb * 32 + 2 * hp) * 1024u, fo_off = WS_FO + (unsigned)((rb * 16 + hp) * 64 + w * 8);
    auto side1 = [&](int) {};
    auto side_h = [&](int n) { if (n == 2) { wait_vm<0>(); flag_store(rs_ws, fh_off, tg, lane); } };
    u32x4 o[2];
    auto store_o = [&](int hd) { st16c(o[hd], rs_ws, (unsigned)lane * 16u, o_off + (unsigned)hd * 1024u); };
    if (sb >= 8) attn_pair<true>(0, w, lane, side1, side_h, store_o, o); else attn_pair<false>(8 - sb, w, lane, side1, side_h, store_o, o);
    wait_vm<0>();
    flag_store(rs_ws, fo_off, tg, lane); flag_store(rs_ws, fo_off + 32u, tg, lane);
  } else {
    const rsrc_t rs_d = mk_rsrc(P.wd, FF * DM * 2), rs_o = mk_rsrc(P.wo, DM * DM * 2);
    unsigned vw[2];
#pragma unroll
    for (int j = 0; j < 2; ++j) vw[j] = (unsigned)(8 * j + (lane >> 3)) * 1024u + (unsigned)(((lane & 7) ^ (((lane >> 4) & 1) << 2)) * 16);
    const unsigned vo = (unsigned)lane * 16u;
    auto unit = [&](int i) { const int r = i + rot; return i < NDN ? (i < nun ? ulo + (r >= nun ? r - nun : r) : -1) : 8 * kq + ((i - NDN + rot) & 7); };
    auto reqW = [&](int i) {
      const int u = unit(i);
#pragma unroll
      for (int j = 0; j < 2; ++j) dma16(ring + (unsigned)(i % DW) * 2048u + j * 1024u, u < 0 ? 0x40000000u : vw[j], i < NDN ? rs_d : rs_o, (unsigned)cb * 128u + (unsigned)(u < 0 ? 0 : u) * 16384u);
    };
    const unsigned hv = WS_FH + (unsigned)((rb64 >> 1) * 1024 + lane * 16), ov = WS_FO + (unsigned)((rb64 >> 1) * 1024 + lane * 16);
    const int pg = lane >> 2;
    const bool pair_ok = ((lane & 3) & 1) == (rb64 & 1);
    const bool h_act = pair_ok && (((pg + 1) * NU) >> 4) > ulo && ((pg * NU) >> 4) < ulo + nun, o_act = pair_ok && (pg >> 2) == kq;
    unsigned wa[2];
#pragma unroll
    for (int nb = 0; nb < 2; ++nb) wa[nb] = ring + (unsigned)h * 512u + q4 * 128u + (unsigned)((nb ^ (q4 >> 1)) * 64) + nsub * 32u + p4 * 8u;
    auto rdw = [&](int i, int nb) { const unsigned a = wa[nb] + (unsigned)(i % DW) * 2048u; return cat(lds_tr(a), lds_tr(a + 1024u)); };
    static constexpr Sched3 S3{};
#pragma unroll
    for (int i = 0; i < DW - 1; ++i) reqW(i);
    wait_vm<2 * (DW - 1)>(); flag_store(rs_ws, fh_off, tg, lane);
    bf16x8 wr[NR][2];
    static_for<0, NR>([&](auto ic) {
      constexpr int i = decltype(ic)::value;
      wait_vm<S3.park[i]>();
      wr[i][0] = rdw(i, 0); wr[i][1] = rdw(i, 1);
      reqW(i + DW - 1);
      asm volatile("s_waitcnt lgkmcnt(0)" : "+v"(wr[i][0]), "+v"(wr[i][1]) :: "memory");
    });
    reqW(NR + DW - 1);
    flags_spin(rs_ws, hv, h_act, tg);
    u32x4 A[DA][2];
    auto reqA = [&](int i) {
      const int u = unit(i);
#pragma unroll
      for (int mb = 0; mb < 2; ++mb)
        A[i % DA][mb] = ld16c_asm(rs_ws, u < 0 ? 0x40000000u : vo, i < NDN ? WS_H + (unsigned)((2 * rb64 + mb) * NU + (u < 0 ? 0 : u)) * 1024u : WS_O + (unsigned)((2 * rb64 + mb) * 32 + u) * 1024u);
    };
#pragma unroll
    for (int i = 0; i < DA - 1; ++i) reqA(i);
#pragma unroll
    for (int b = 0; b < 4; ++b)
#pragma unroll
      for (int i = 0; i < 16; ++i) acc[b][i] = 0.f;
    const int row = 64 * rb64 + 32 * (kq >> 1) + (lane & 31), col0 = 64 * cb + 32 * (kq & 1) + 4 * h;
    const unsigned short* xp = (const unsigned short*)P.x + (size_t)row * DM + col0;
    u32x4 fo;
    bf16x8 wf[2][2];
    static_for<0, NK3>([&](auto ic) {
      constexpr int i = decltype(ic)::value;
      wait_vm<S3.wait(i)>();
      tie(A[i % DA][0]); tie(A[i % DA][1]);

      if constexpr (i == OCHK) {
        wait_vm_tie<S3.before[i] - S3.before[OPOLL + 1]>(fo);
        if (!flags_ok(fo, o_act, tg)) flags_spin(rs_ws, ov, o_act, tg);
      }
      const bf16x8 w0 = i < NR ? wr[i < NR ? i : 0][0] : wf[i & 1][0], w1 = i < NR ? wr[i < NR ? i : 0][1] : wf[i & 1][1];
      SB0();
      acc[0] = mfma(w0, __builtin_bit_cast(bf16x8, A[i % DA][0]), acc[0]);
      if constexpr (i + 1 >= NR && i + 1 < NK3) { wf[(i + 1) & 1][0] = rdw(i + 1, 0); wf[(i + 1) & 1][1] = rdw(i + 1, 1); }
      SB0();
      acc[1] = mfma(w1, __builtin_bit_cast(bf16x8, A[i % DA][0]), acc[1]);
      SB0();
      if constexpr (i > NR && i + DW - 1 < NK3) reqW(i + DW - 1);
      acc[2] = mfma(w0, __builtin_bit_cast(bf16x8, A[i % DA][1]), acc[2]);
      SB0();
      acc[3] = mfma(w1, __builtin_bit_cast(bf16x8, A[i % DA][1]), acc[3]);
      SB0();
      if constexpr (i + DA - 1 < NK3) reqA(i + DA - 1);
      if (i == OPOLL) fo = ld16c_asm(rs_ws, ov, 0u);
      if (i == XLD) {
#pragma unroll
        for (int q = 0; q < 4; ++q) xr[q] = *(const GAS u32x2*)(xp + 8 * q);
      }
      if (i == 20 && keeper) {
#pragma unroll
        for (int q = 0; q < 4; ++q) fe[q] = ld16c_asm(rs_ws, WS_FQKV + (unsigned)(q * 64 + lane) * 64u, 0u);
      }
      SB0();
    });
  }
  if (w >= 4) {
#pragma unroll
    for (int b = 0; b < 4; ++b) {
      if (b != kq) {
        const unsigned base = ring + (unsigned)((b - kq - 1 + 4) % 4) * 4096u + lane * 16u;
#pragma unroll
        for (int q = 0; q < 4; ++q) *(LAS f32x4*)(lds + base + q * 1024u) = f32x4{acc[b][4 * q], acc[b][4 * q + 1], acc[b][4 * q + 2], acc[b][4 * q + 3]};
      }
    }
  }
  bar();
  if (w >= 4) {
    f32x16 r = kq == 0 ? acc[0] : (kq == 1 ? acc[1] : (kq == 2 ? acc[2] : acc[3]));
#pragma unroll
    for (int s = 1; s < 4; ++s) {
      const int src = (kq + s) & 3;
      const unsigned base = l3w(src) + (unsigned)((kq - src - 1 + 4) % 4) * 4096u + lane * 16u;
#pragma unroll
      for (int q = 0; q < 4; ++q) { const f32x4 v = lds_f4(base + q * 1024u);
#pragma unroll
        for (int e = 0; e < 4; ++e) r[4 * q + e] += v[e]; }
    }
    const int row = 64 * rb64 + 32 * (kq >> 1) + (lane & 31), col0 = 64 * cb + 32 * (kq & 1);
    unsigned o[8];
#pragma unroll
    for (int q = 0; q < 4; ++q) { o[2 * q] = pk(r[4 * q] + bf_lo(xr[q][0]), r[4 * q + 1] + bf_hi(xr[q][0])); o[2 * q + 1] = pk(r[4 * q + 2] + bf_lo(xr[q][1]), r[4 * q + 3] + bf_hi(xr[q][1])); }
    unsigned short* op = (unsigned short*)P.out + (size_t)row * DM + col0 + 8 * h;
#pragma unroll
    for (int q = 0; q < 4; q += 2) {
      const auto t0 = __builtin_amdgcn_permlane32_swap(o[2 * q], o[2 * q + 2], false, false), t1 = __builtin_amdgcn_permlane32_swap(o[2 * q + 1], o[2 * q + 3], false, false);
      *(GAS u32x4*)(op + 8 * q) = u32x4{t0[0], t1[0], t0[1], t1[1]};
    }
    if (keeper) {
#pragma unroll
      for (int q = 0; q < 4; ++q) { tie(fe[q]); if (!flags_ok(fe[q], true, tg)) flags_spin(rs_ws, WS_FQKV + (unsigned)(q * 64 + lane) * 64u, true, tg); }
      if (lane == 0) __hip_atomic_store((GAS u64*)(P.ws + WS_EPOCH), epoch, __ATOMIC_RELAXED, __HIP_MEMORY_SCOPE_AGENT);
    }
  }
}

__global__ __launch_bounds__(512, 2) void mk_kernel(const void* x, const void* wqkv, const void* wo, const void* wg, const void* wu, const void* wd, void* out, char* ws) {
  u64 pc0 = 0;
  asm volatile("mk_code_begin:\n\ts_getpc_b64 %0" : "=s"(pc0));
  {
    unsigned len;
    asm volatile("s_mov_b32 %0, mk_code_end-mk_code_begin" : "=s"(len));
    const unsigned wv = __builtin_amdgcn_readfirstlane(threadIdx.x >> 6);
    const unsigned per = ((len >> 5) + 1023u) & ~1023u;
    const unsigned o = (blockIdx.x >> 3) * per + wv * 1024u;
    dma16(L_DUMMY + wv * 1024u, (threadIdx.x & 63u) * 16u, mk_rsrc((const void*)(pc0 & ~255ull), len & ~15u), wv * 1024u < per ? o : 0x40000000u);
  }
  const Ptrs P{x, wqkv, wo, wg, wu, wd, out, ws};
  __builtin_amdgcn_s_setprio(1);
  const int b = blockIdx.x, xcd = b & 7, idx = b >> 3;
  const int w = __builtin_amdgcn_readfirstlane(threadIdx.x >> 6), lane = threadIdx.x & 63;
  Tag tg; u64 epoch;
  const int rb = 2 * (idx >> 2) + (xcd >> 2), g = (xcd & 3) * 4 + (idx & 3), rot1 = ((idx >> 2) + 2 * (idx & 3)) & 7;
  bool early;
  Pro pro;
  prologue(P, rb, g, rot1, w, lane, pro);
  if (w < 4) early = phase1<0>(P, pro, rb, g, rot1, w, lane, rb, g, tg, epoch); else early = phase1<1>(P, pro, rb, g, rot1, w - 4, lane, rb, g, tg, epoch);
  const int rot3 = ((idx & 7) * 2 + 5 * (idx >> 3)) % 21;
  phase23(P, early, rb, g, epoch, rb, g, xcd * 4 + (idx >> 3), idx & 7, rot3, w, lane, tg);
  asm volatile("mk_code_end:");
}

}

extern "C" void kernel_launch(void* const* d_in, const int* in_sizes, int n_in, void* d_out, int out_size, void* d_ws, size_t ws_size, hipStream_t stream) {
  const void *x = d_in[0], *wqkv = d_in[1], *wo = d_in[2], *wg = d_in[4], *wu = d_in[5], *wd = d_in[6];
  mk_kernel<<<256, 512, 0, stream>>>(x, wqkv, wo, wg, wu, wd, d_out, (char*)d_ws);
}
```

```cpp
#define PH_N 0
#include <hip/hip_runtime.h>
#include <hip/hip_bf16.h>
#include <stdint.h>

#define P1_NPRO 3
namespace {

typedef short bf16x8 __attribute__((ext_vector_type(8)));
typedef short s16x4 __attribute__((ext_vector_type(4)));
typedef float f32x16 __attribute__((ext_vector_type(16)));
typedef float f32x4 __attribute__((ext_vector_type(4)));
typedef float f32x2 __attribute__((ext_vector_type(2)));
typedef unsigned u32x4 __attribute__((ext_vector_type(4)));
typedef unsigned u32x2 __attribute__((ext_vector_type(2)));
typedef __bf16 bf16x2_t __attribute__((ext_vector_type(2)));
typedef short v4i16_t __attribute__((ext_vector_type(4)));
typedef __amdgpu_buffer_rsrc_t rsrc_t;
#define LAS __attribute__((address_space(3)))
#define GAS __attribute__((address_space(1)))
#define DI __device__ __forceinline__
#define SB0() __builtin_amdgcn_sched_barrier(0)
typedef unsigned long long u64;

constexpr int SEQ = 2048, DM = 512, FF = 1376, NU = 86;
constexpr unsigned WS_EPOCH = 0;
constexpr unsigned WS_FQKV = 16384, WS_FH = 32768, WS_FO = 49152;
constexpr unsigned WS_RS = 65536;
constexpr unsigned WS_QKV = 1u << 20;
constexpr unsigned WS_H = 7u << 20;
constexpr unsigned WS_O = 13u << 20;
constexpr unsigned WS_BYTES = 16u << 20;
constexpr unsigned SLOT1 = 28672;
constexpr unsigned L_CSP = 143360;
constexpr unsigned L_CSW = 146432;
constexpr unsigned L_ONES = 152576;
constexpr unsigned ONES_BYTES = 9984;
constexpr unsigned L_DUMMY = 152576;
constexpr unsigned L_WORD = 163584;
constexpr unsigned LDS_BYTES = 163840;
constexpr unsigned LA_Q = 28672, LA_K = LA_Q + 8192, LA_V = LA_K + 24576;

__shared__ __attribute__((aligned(1024))) char lds[LDS_BYTES];

DI unsigned pk(float a, float b) { f32x2 v = {a, b}; bf16x2_t r = __builtin_convertvector(v, bf16x2_t); return __builtin_bit_cast(unsigned, r); }
DI float dot2(unsigned a, unsigned b, float c) { return __builtin_amdgcn_fdot2_f32_bf16(__builtin_bit_cast(bf16x2_t, a), __builtin_bit_cast(bf16x2_t, b), c, false); }
DI rsrc_t mk_rsrc(const void* p, unsigned bytes) { return __builtin_amdgcn_make_buffer_rsrc((void*)p, 0, bytes, 0x00020000); }
#define LD_POL ""
#define ST_AUX 16
DI void dma16(unsigned ldsaddr, unsigned voff, rsrc_t rs, unsigned soff) {
  asm volatile("s_mov_b32 m0, %0\n\ts_nop 0\n\tbuffer_load_dwordx4 %1, %2, %3 offen" LD_POL " lds" ::"s"(ldsaddr), "v"(voff), "s"(rs), "s"(soff) : "memory");
}
DI void dma16c(unsigned ldsaddr, unsigned voff, rsrc_t rs, unsigned soff) {
  asm volatile("s_mov_b32 m0, %0\n\ts_nop 0\n\tbuffer_load_dwordx4 %1, %2, %3 offen sc1 lds" ::"s"(ldsaddr), "v"(voff), "s"(rs), "s"(soff) : "memory");
}
DI void pf_lines(unsigned ldsaddr, unsigned voff, rsrc_t rs) {
  asm volatile("s_mov_b32 m0, %0\n\ts_nop 0\n\tbuffer_load_dword %1, %2, 0 offen lds" ::"s"(ldsaddr), "v"(voff), "s"(rs) : "memory");
}
template <int N> DI void wait_vm() { asm volatile("s_waitcnt vmcnt(%0)" ::"n"(N) : "memory"); }
template <int N> DI void wait_vm_tie(u32x4& v) { asm volatile("s_waitcnt vmcnt(%1)" : "+v"(v) : "n"(N) : "memory"); }
template <int V> struct IC { static constexpr int value = V; };
template <int I, int N, class F> DI void static_for(F&& f) { if constexpr (I < N) { f(IC<I>{}); static_for<I + 1, N>(f); } }
DI void bar_raw() { asm volatile("s_barrier" ::: "memory"); }
DI void tie(u32x4& v) { asm volatile("" : "+v"(v) :: "memory"); }
DI void bar() { asm volatile("s_waitcnt lgkmcnt(0)\n\ts_barrier" ::: "memory"); }
DI f32x16 mfma(bf16x8 a, bf16x8 b, f32x16 c) { return __builtin_amdgcn_mfma_f32_32x32x16_bf16(a, b, c, 0, 0, 0); }
DI bf16x8 lds_b128(unsigned a) { return *(const LAS bf16x8*)(lds + a); }
#define MK_REAL 1
DI s16x4 lds_tr(unsigned a) { return __builtin_amdgcn_ds_read_tr16_b64_v4i16((LAS v4i16_t*)(lds + a)); }
DI f32x4 mfma16(bf16x8 a, bf16x8 b, f32x4 c) { return __builtin_amdgcn_mfma_f32_16x16x32_bf16(a, b, c, 0, 0, 0); }
DI f32x4 lds_f4(unsigned a) { return *(const LAS f32x4*)(lds + a); }
DI bf16x8 cat(s16x4 lo, s16x4 hi) { return __builtin_shufflevector(lo, hi, 0, 1, 2, 3, 4, 5, 6, 7); }
DI float xhalf_sum(float v) { auto r = __builtin_amdgcn_permlane32_swap(__float_as_uint(v), __float_as_uint(v), false, false); return __uint_as_float(r[0]) + __uint_as_float(r[1]); }
DI float xhalf_max(float v) { auto r = __builtin_amdgcn_permlane32_swap(__float_as_uint(v), __float_as_uint(v), false, false); return fmaxf(__uint_as_float(r[0]), __uint_as_float(r[1])); }
DI void st16c(u32x4 v, rsrc_t rs, unsigned voff, unsigned soff) { __builtin_amdgcn_raw_buffer_store_b128(v, rs, voff + soff, 0, ST_AUX); }
DI void st8c(u32x2 v, rsrc_t rs, unsigned voff, unsigned soff) { __builtin_amdgcn_raw_buffer_store_b64(v, rs, voff, soff, 16); }
constexpr unsigned ONES2 = 0x3F803F80u;
constexpr unsigned SPIN_CAP = 1u << 19;
template <int CTRL> DI float dpp_add(float v) { return v + __int_as_float(__builtin_amdgcn_update_dpp(0, __float_as_int(v), CTRL, 0xf, 0xf, true)); }
DI float wave_sum(float v) {
  v = dpp_add<0xB1>(v); v = dpp_add<0x4E>(v); v = dpp_add<0x141>(v); v = dpp_add<0x140>(v);
  auto a = __builtin_amdgcn_permlane16_swap(__float_as_uint(v), __float_as_uint(v), false, false);
  v = __uint_as_float(a[0]) + __uint_as_float(a[1]);
  return xhalf_sum(v);
}
DI float bf_lo(unsigned u) { return __uint_as_float(u << 16); }
DI float bf_hi(unsigned u) { return __uint_as_float(u & 0xffff0000u); }
DI u32x4 ld16_asm(rsrc_t rs, unsigned voff, unsigned soff) { u32x4 v; asm volatile("buffer_load_dwordx4 %0, %1, %2, %3 offen" : "=v"(v) : "v"(voff), "s"(rs), "s"(soff) : "memory"); return v; }
DI u32x4 ld16c_asm(rsrc_t rs, unsigned voff, unsigned soff) { u32x4 v; asm volatile("buffer_load_dwordx4 %0, %1, %2, %3 offen sc1" : "=v"(v) : "v"(voff), "s"(rs), "s"(soff) : "memory"); return v; }
#define STAMP2(i) do {} while (0)
struct Stamps {};
#define STAMP(i) do {} while (0)
#define CSTAMP(i) do {} while (0)
#define NOTE(i, v) do {} while (0)

struct Ptrs { const void *x, *wqkv, *wo, *wg, *wu, *wd; void* out; char* ws; u64 pc; };
struct Tag { unsigned a, b; };

DI void flag_store(rsrc_t rs_ws, unsigned off, Tag tg, int lane) { if (lane == 0) st8c(u32x2{tg.a, tg.b}, rs_ws, 0u, off); }
DI bool flags_ok(u32x4 v, bool act, Tag tg) { return __all(!act || (v[0] == tg.a && v[1] == tg.b && v[2] == tg.a && v[3] == tg.b)); }
DI void flags_spin(rsrc_t rs_ws, unsigned voff, bool act, Tag tg) {
  for (unsigned spins = 0; spins < SPIN_CAP; ++spins) {
    u32x4 v = ld16c_asm(rs_ws, voff, 0u); wait_vm_tie<0>(v);
    if (flags_ok(v, act, tg)) break;
    __builtin_amdgcn_s_sleep(1);
  }
}
DI void word_set(int i, unsigned v) { *(volatile LAS unsigned*)(lds + L_WORD + i * 4) = v; }
DI unsigned word_get(int i) { return __builtin_amdgcn_readfirstlane(*(volatile LAS unsigned*)(lds + L_WORD + i * 4)); }

struct AttnSrc { unsigned q, kv; int sb0; };
DI AttnSrc attn_src(int rb, int hp, int w) {
  AttnSrc a;
  a.q = __builtin_amdgcn_readfirstlane(WS_QKV + (unsigned)((2 * hp + (w >> 2)) * 64 + 4 * rb + (w & 3)) * 1024u);
  a.kv = __builtin_amdgcn_readfirstlane(WS_QKV + (unsigned)(((1 + (w >> 2)) * 32 + 2 * hp + ((w >> 1) & 1)) * 64) * 1024u);
  a.sb0 = __builtin_amdgcn_readfirstlane(4 * rb - 8 + 6 * (w & 1));
  return a;
}
DI void attn_issue(rsrc_t rs_ws, const AttnSrc& a, int w, int lane, int piece, bool go) {
  const unsigned vo = (unsigned)lane * 16u + (go ? 0u : 0x40000000u);
  if (piece == 0) { dma16c(LA_Q + (unsigned)w * 1024u, vo, rs_ws, a.q); return; }
  const int sbk = a.sb0 + piece - 1;
  dma16c(LA_K + (unsigned)((w >> 1) * 12 + 6 * (w & 1) + piece - 1) * 1024u, vo, rs_ws, a.kv + (unsigned)(sbk < 0 ? 0 : sbk) * 1024u);
}
DI unsigned qkv_flag_voff(int rb, int hp, int lane, bool& act) {
  const int c = lane >> 2, kind = c == 0 ? 0 : (c <= 3 ? 1 : 2), d = c == 0 ? 0 : (c - 1) % 3, rr = rb - d;
  act = lane < 28 && rr >= 0;
  return act ? WS_FQKV + (unsigned)((rr * 16 + (kind * 16 + hp) / 3) * 64 + (lane & 3) * 16) : 0u;
}

struct Pro { unsigned vx[2], vq[2]; };
DI void prologue(const Ptrs& P, int rb, int g, int rot, int w, int lane, Pro& o) {
  const rsrc_t rs_x = mk_rsrc(P.x, SEQ * DM * 2), rs_q = mk_rsrc(P.wqkv, DM * 1536 * 2);
  const int nj = w >> 2, mi = w & 3;
#pragma unroll
  for (int j = 0; j < 2; ++j) o.vx[j] = (unsigned)(16 * w + 8 * j + (lane >> 3)) * 1024u + (unsigned)(((lane & 7) ^ (4 * j + (lane >> 4))) * 16);
#pragma unroll
  for (int j = 0; j < 2; ++j) { const int G = (nj ? 2 * mi + j : 8 + mi) * 64 + lane;
    o.vq[j] = (unsigned)(G / 12) * 3072u + (unsigned)(G % 12) * 16u; }
  const unsigned sx0 = (unsigned)rb * 131072u, sq0 = (unsigned)g * 192u;
  const unsigned dq0 = 16384u + (unsigned)(nj ? 2 * mi : 8 + mi) * 1024u;
#pragma unroll
  for (int sg = 0; sg < P1_NPRO; ++sg) {
    const unsigned slot = (unsigned)sg * SLOT1, c = (unsigned)(sg + rot) & 7u;
    dma16(slot + (unsigned)(2 * w) * 1024u, o.vx[0], rs_x, sx0 + c * 128u);
    dma16(slot + (unsigned)(2 * w + 1) * 1024u, o.vx[1], rs_x, sx0 + c * 128u);
    dma16(slot + dq0, o.vq[0], rs_q, sq0 + c * (64u * 3072u));
    dma16(nj ? slot + dq0 + 1024u : L_DUMMY + 4096u, nj ? o.vq[1] : 0x40000000u, rs_q, sq0 + c * (64u * 3072u));
  }
}

#define TLBW_N 0
#define PFX_N 0
#define PF3_N 0
#define PF3_T 33
#define P0_T 99
#define ATT_B 14
#define P1_HOLD 12
#define PQ_N 0
constexpr int OP_NONE = -1, OP_PFW = 200, OP_PRE = 300, OP_ATT = 400;
template <int NJ> struct Sched1 {
  static constexpr int NA = NJ ? 4 : 3, NST = NJ ? 2 : 4;
  static constexpr int n(int sg) { return sg < 8 ? NA : 3; }
  static constexpr int TB(int s) { return s < 8 ? 4 * s + 2 : 4 * s + 3; }
  static constexpr int extras(int T) { return (T == 9 ? 1 : 0)   + (T == 24 ? 1 : 0)   + (T == 39 ? 1 : 0)  ; }
  static constexpr int NPRO = P1_NPRO;
  static constexpr int NTW = TLBW_N;
  static constexpr int NPX = PFX_N;
  static constexpr int PRO = 1 + NPX + NPRO * 4 + 1 + NTW;
  static constexpr int HOLD = P1_HOLD;
  int op[64][2] = {}, before[65] = {}, last[16] = {};
  constexpr Sched1() {
    int sg = NPRO, j = 0, pf = 0, att = 0, pq = 0, ph = 0, c = PRO;
    for (int i = 0; i < NPRO; ++i) last[i] = 1 + 4 * (i + 1);
    for (int T = 0; T < 64; ++T) {
      before[T] = c;
      for (int k = 0; k < 2; ++k) {
        int o = OP_NONE;
        if (k == 0 || T < 14) {
          if (sg <= 15 && (sg < 5 || T >= TB(sg - 4)) && (sg < 8 || T >= 4 * sg - HOLD)) {   o = sg * 8 + j; if (++j == n(sg)) { last[sg] = c + 1; ++sg; j = 0; } }
          else if (pf == 0 && T >= P0_T) o = OP_PFW + pf++;
          else if (pf >= 1 && pf < 1 + PF3_N && T >= PF3_T) o = OP_PFW + pf++;
          else if (pq < PQ_N && T >= 8) o = OP_PRE + pq++;
          else if (ph < PH_N && T >= 33) o = OP_PRE + 4 + ph++;
        }
        if (T >= 4 * ATT_B + 3 && att < 7 && (k == 0 || T >= 61)) o = OP_ATT + att++;
        op[T][k] = o;
        if (o != OP_NONE) ++c;
      }
      c += extras(T);
      if (T == 31) c += NST;
    }
    before[64] = c;
  }
  constexpr int wait_stage(int T, int sg) const { return before[T] - last[sg]; }
};

template <int NJ>
DI bool phase1(const Ptrs& P, const Pro& pro, int rb, int g, int rot, int mi, int lane, int rb2, int hp2, Tag& tg, u64& epoch, Stamps& T_) {
  Stamps& T = T_;
  typedef Sched1<NJ> L;
  static constexpr Sched1<NJ> SC{};
  const int w = NJ * 4 + mi;
  const rsrc_t rs_x = mk_rsrc(P.x, SEQ * DM * 2), rs_q = mk_rsrc(P.wqkv, DM * 1536 * 2);
  const rsrc_t rs_f = mk_rsrc(NJ ? P.wu : P.wg, DM * FF * 2), rs_ws = mk_rsrc(P.ws, WS_BYTES);
  const unsigned sx0 = (unsigned)rb * 131072u, sq0 = (unsigned)g * 192u;
  constexpr int NQ = NJ ? 2 : 1;
  constexpr int NA = 2 + NQ;
  const unsigned vx[2] = {pro.vx[0], pro.vx[1]}, vq[2] = {pro.vq[0], pro.vq[1]};
  unsigned vf[3];
  const int u0 = (g * NU) >> 4, nu = (((g + 1) * NU) >> 4) - u0;
  const unsigned sf0 = (unsigned)u0 * 32u;
  auto piece = [&](int gs, int j) {
    const unsigned slot = (unsigned)(gs % 5) * SLOT1;
    const unsigned c = (unsigned)(gs + rot) & 7u;
    if (gs < 8) {
      if (j < 2) dma16(slot + (unsigned)(2 * w + j) * 1024u, vx[j], rs_x, sx0 + c * 128u);
      else dma16(slot + 16384u + (unsigned)(NJ ? 2 * mi + j - 2 : 8 + mi) * 1024u, vq[j - 2], rs_q, sq0 + c * (64u * 3072u));
    } else dma16(slot + NJ * 12288u + (unsigned)(3 * mi + j) * 1024u, vf[j], rs_f, sf0 + c * (64u * 2752u));
  };
  const unsigned myrow = (unsigned)(128 * rb + 8 * g + w);
  u32x4 xrow = ld16_asm(rs_x, (unsigned)lane * 16u, myrow * 1024u), ev;
  if (w == 0) ev = ld16c_asm(rs_ws, 0u, WS_EPOCH);
  STAMP(1);
#pragma unroll
  for (int j = 0; j < 3; ++j) { const int G = (3 * mi + j) * 64 + lane, kg = G / 48, rr = G % 48, unit = (rr >> 3) ^ NJ;
    vf[j] = unit < nu ? (unsigned)(4 * kg + ((rr & 7) >> 1)) * 2752u + (unsigned)(unit * 32 + (rr & 1) * 16) : 0x40000000u; }
  const int r = lane & 31, h = lane >> 5, nsub = (lane >> 4) & 1, q4 = (lane & 15) >> 2, p4 = lane & 3;
  unsigned xa[4], ro[4];
#pragma unroll
  for (int p = 0; p < 4; ++p) { const int ks = (p + mi) & 3;
    xa[p] = (unsigned)(32 * mi + r) * 128u + (unsigned)(((2 * ks + h) ^ ((r >> 1) & 7)) * 16);
    ro[p] = (unsigned)ks * 3072u; }
  const unsigned wl = (unsigned)(2 * h) * 768u + q4 * 32u + p4 * 8u;
  const unsigned wa = 16384u + (unsigned)(2 * h) * 768u + q4 * 192u + nsub * 32u + p4 * 8u;
  const unsigned wbE = nsub * 12288u + wl + nsub * 128u;
  const unsigned wbO = nsub * 12288u + wl - nsub * 128u;
  const AttnSrc asrc = attn_src(rb2, hp2, w);
  bool early = false;
  auto warm = [&](int i) {
    const unsigned xl = blockIdx.x >> 3, dst = L_DUMMY + (unsigned)w * 256u;
    if (i == 0) {
      const unsigned q = (unsigned)(w & 1) * 64u + (unsigned)lane, rr = q / 7u, ln = q - rr * 7u;
      const unsigned ub = (unsigned)((((blockIdx.x & 3) * 4 * NU) >> 4) * 32) & ~127u;
      pf_lines(dst, (w < 4 && q < 112u) ? (16u * xl + rr) * 2752u + ub + ln * 128u : 0x40000000u, mk_rsrc((w & 2) ? P.wu : P.wg, DM * FF * 2));
    } else {
      const unsigned q = xl * 8u + (unsigned)w + 256u * (unsigned)(i - 1);
      dma16(L_DUMMY + 2048u + (unsigned)w * 1024u, (unsigned)lane * 16u + (q < 1888u ? 0u : 0x40000000u), mk_rsrc(q < 1376u ? P.wd : P.wo, q < 1376u ? FF * DM * 2 : DM * DM * 2), (q < 1376u ? q : q - 1376u) * 1024u);
    }
  };
  auto pretouch = [&](int k) {
    const int sbw = rb * 4 + mi;
    unsigned off;
    if (k < 4) { const int blk = 3 * g + (NJ ? 2 : (k >> 1)), which = blk >> 4, hp = blk & 15; off = (NJ && k >= 2) ? 0x40000000u : WS_QKV + (unsigned)((which * 32 + 2 * hp + (k & 1)) * 64 + sbw) * 1024u; }
    else off = NJ * 3 + k - 4 < nu ? WS_H + (unsigned)(sbw * NU + u0 + NJ * 3 + k - 4) * 1024u : 0x40000000u;
    dma16(L_DUMMY + 4096u, (unsigned)lane * 16u + (off >= 0x40000000u ? off : 0u), rs_ws, off >= 0x40000000u ? 0u : off);
  };
  auto op_at = [&](auto tc, auto kc) {
    constexpr int o = SC.op[decltype(tc)::value][decltype(kc)::value];
    if constexpr (o >= OP_ATT) attn_issue(rs_ws, asrc, w, lane, o - OP_ATT, early);
    else if constexpr (o >= OP_PRE) pretouch(o - OP_PRE);
    else if constexpr (o >= OP_PFW) warm(o - OP_PFW);
    else if constexpr (o >= 0) piece(o >> 3, o & 7);
  };
  bf16x8 sel;
  { const unsigned v = (lane & 15) == ((lane >> 4) & 1) ? ONES2 : 0u; sel = __builtin_bit_cast(bf16x8, u32x4{v, v, v, v}); }

  bf16x8 xf[32];
  constexpr int NB1 = NJ ? 1 : 2;
  f32x16 acc[3];
  f32x4 cs[3];
#pragma unroll
  for (int b = 0; b < 3; ++b) { cs[b] = f32x4{0.f, 0.f, 0.f, 0.f};
#pragma unroll
    for (int i = 0; i < 16; ++i) acc[b][i] = 0.f; }
  bf16x8 wf[3][NB1], wg[2][3];
  auto ldx = [&](int t) { xf[t] = lds_b128((unsigned)((t >> 2) % 5) * SLOT1 + xa[t & 3]); };
  auto ld1a = [&](int t, int b) {
    const unsigned a = (unsigned)((t >> 2) % 5) * SLOT1 + wa + ro[t & 3] + (NJ ? 2 : b) * 64u; wf[t % 3][b] = cat(lds_tr(a), lds_tr(a + 768u));
  };
  auto ld1b = [&](int t, int b) {
    const int u = NJ * 3 + b;
    const unsigned a = (unsigned)((8 + (t >> 2)) % 5) * SLOT1 + ((u & 1) ? wbO : wbE) + ro[t & 3] + u * 128u; wg[t & 1][b] = cat(lds_tr(a), lds_tr(a + 768u));
  };
  auto cs_put = [&](int nb, int b0) {
#pragma unroll
    for (int b = 0; b < 3; ++b) if (b < nb && lane < 16) { *(LAS float*)(lds + L_CSP + (((b0 + b) * 32 + lane) * 4 + mi) * 4) = cs[b][0]; *(LAS float*)(lds + L_CSP + (((b0 + b) * 32 + 16 + lane) * 4 + mi) * 4) = cs[b][1]; }
  };
  auto cs_get = [&](int nb, int b0) {
#pragma unroll
    for (int b = 0; b < 3; ++b) if (b < nb) { const f32x4 c4 = lds_f4(L_CSP + ((b0 + b) * 32 + r) * 16);
      *(LAS float*)(lds + L_CSW + w * 384 + b * 128 + r * 4) = (c4[0] + c4[1]) + (c4[2] + c4[3]); }
  };
  wait_vm<(L::NPRO - 1) * 4 + 1>();
  bar_raw();
  STAMP(2); CSTAMP(23);
  ldx(0); ldx(1);
#pragma unroll
  for (int b = 0; b < NB1; ++b) { ld1a(0, b); ld1a(1, b); }
  u32x4 sv;
  float rmean = 0.f, rrstd = 0.f;
  const unsigned sv_off = WS_RS + (unsigned)(128 * rb + 32 * mi) * 16u;
  static_for<0, 32>([&](auto tc) {
    constexpr int t = decltype(tc)::value, s = t >> 2, p = t & 3;
    if constexpr (p == 2) {
      if (s == 7) cs_put(NB1, NJ ? 2 : 0);
      if constexpr (s == 1) {
        wait_vm_tie<(SC.wait_stage(t, 2) < SC.before[t] - L::PRO ? SC.wait_stage(t, 2) : SC.before[t] - L::PRO)>(xrow);
        if (w == 0) { tie(ev); word_set(2, ev[0]); word_set(3, ev[1]); }
      } else wait_vm<SC.wait_stage(t, s + 1)>();
      if (s == 7 || s == 1) bar(); else bar_raw();
      if constexpr (s == 1) {
        const u64 e0 = (u64)word_get(2) | ((u64)word_get(3) << 32);
        epoch = e0 + 1ull;
        tg.a = (unsigned)epoch; tg.b = ((unsigned)(e0 >> 32) ^ 0x5bd1e995u) + tg.a;
      }
      STAMP2(24 + s);
      if (s == 7) cs_get(NB1, NJ ? 2 : 0);
      if (s == 1) CSTAMP(60); if (s == 4) CSTAMP(61); if (s == 7) CSTAMP(62);
    }
    if (NJ) op_at(tc, IC<0>{});
    static_for<0, NB1>([&](auto bc) {
      constexpr int b = decltype(bc)::value;
      acc[b] = mfma(wf[t % 3][b], xf[t], acc[b]);
      if (t < 30) { if (b == 0) ldx(t + 2); ld1a(t + 2, b); }
      if (t == 31) { ld1b(0, b); if (b == NB1 - 1) { for (int bb = NB1; bb < 3; ++bb) ld1b(0, bb); } }
      if (p == (NJ ? 1 : b)) cs[b] = mfma16(sel, wf[t % 3][b], cs[b]);
      if (b == 0) {
        if (t == 6 + NJ) {
          float e[8];
#pragma unroll
          for (int i = 0; i < 4; ++i) { e[2 * i] = bf_lo(xrow[i]); e[2 * i + 1] = bf_hi(xrow[i]); }
          rmean = wave_sum(((e[0] + e[1]) + (e[2] + e[3])) + ((e[4] + e[5]) + (e[6] + e[7]))) * (1.f / 512.f);
          asm volatile("" : "+v"(rmean));
        }
        if (t == 8) {
          float q = 0.f;
#pragma unroll
          for (int i = 0; i < 4; ++i) { const float d0 = bf_lo(xrow[i]) - rmean, d1 = bf_hi(xrow[i]) - rmean; q = fmaf(d0, d0, q); q = fmaf(d1, d1, q); }
          rrstd = __builtin_amdgcn_rsqf(wave_sum(q) * (1.f / 512.f) + 1e-5f);
          asm volatile("" : "+v"(rrstd));
        }
      }
      SB0();
      if (b == 0) op_at(tc, IC<NJ ? 1 : 0>{});
    });
    if (!NJ) op_at(tc, IC<1>{});
    if (t == 24) sv = ld16c_asm(rs_ws, (unsigned)r * 16u, sv_off);
    if (t == 9 && lane == 0) st16c(u32x4{__float_as_uint(rmean), tg.a, __float_as_uint(rrstd), tg.b}, rs_ws, 0u, WS_RS + myrow * 16u);
    SB0();
  });
  STAMP(3); CSTAMP(22);
  { constexpr int after = SC.before[32] - L::NST - SC.before[25]; wait_vm_tie<after>(sv); }
  NOTE(53, __all(sv[1] == tg.a && sv[3] == tg.b) ? 1 : 2);
  for (unsigned spins = 0; !__all(sv[1] == tg.a && sv[3] == tg.b) && spins < SPIN_CAP; ++spins) {
    __builtin_amdgcn_s_sleep(1);
    sv = ld16c_asm(rs_ws, (unsigned)r * 16u, sv_off); wait_vm_tie<0>(sv);
  }
  const float mean = __uint_as_float(sv[0]), rstd = __uint_as_float(sv[2]);
  const int sb = rb * 4 + mi;
#pragma unroll
  for (int b = 0; b < NB1; ++b) {
    const int blk = 3 * g + (NJ ? 2 : b), which = blk >> 4, hp = blk & 15;
    const float sc = which == 0 ? rstd * (0.25f * 1.4426950408889634f) : rstd;
    float v[16];
#pragma unroll
    for (int i = 0; i < 4; ++i) { const f32x4 c4 = lds_f4(L_CSW + w * 384 + b * 128 + (8 * i + 4 * h) * 4);
#pragma unroll
      for (int e = 0; e < 4; ++e) v[4 * i + e] = (acc[b][4 * i + e] - mean * c4[e]) * sc; }
    const unsigned vo = which == 2 ? (unsigned)(h * 512 + ((r ^ (4 * h)) * 16)) : (unsigned)(lane * 16);
#pragma unroll
    for (int hd = 0; hd < 2; ++hd) {
      const u32x4 o = {pk(v[8 * hd], v[8 * hd + 1]), pk(v[8 * hd + 2], v[8 * hd + 3]), pk(v[8 * hd + 4], v[8 * hd + 5]), pk(v[8 * hd + 6], v[8 * hd + 7])};
      st16c(o, rs_ws, vo, WS_QKV + (unsigned)((which * 32 + 2 * hp + hd) * 64 + sb) * 1024u);
    }
  }
  STAMP(4); CSTAMP(18);
#pragma unroll
  for (int b = 0; b < 3; ++b) { cs[b] = f32x4{0.f, 0.f, 0.f, 0.f};
#pragma unroll
    for (int i = 0; i < 16; ++i) acc[b][i] = 0.f; }
  bool act;
  const unsigned qv = qkv_flag_voff(rb2, hp2, lane, act);
  u32x4 fq;
  SB0();
  static_for<32, 64>([&](auto tc) {
    constexpr int Tg = decltype(tc)::value, t = Tg - 32, s = Tg >> 2, p = t & 3;
    if constexpr (p == 3) {
      if (s == 15) cs_put(3, NJ * 3);
      if constexpr (s == 9) wait_vm<(SC.before[Tg] - SC.before[32] < SC.wait_stage(Tg, 10) ? SC.before[Tg] - SC.before[32] : SC.wait_stage(Tg, 10))>();
      else if constexpr (s == ATT_B) { wait_vm<0>(); if (w == 0) { tie(fq); word_set(0, flags_ok(fq, act, tg) ? 1u : 0u); } }
      else if constexpr (s <= 14) wait_vm<SC.wait_stage(Tg, s + 1)>();
      if (s == ATT_B || s == 15) bar(); else bar_raw();
      STAMP2(24 + s);
      if (s == 8) CSTAMP(56); if (s == 11) CSTAMP(57); if (s == 13) CSTAMP(58); if (s == 15) CSTAMP(59);
      if (s == ATT_B) early = word_get(0) != 0u;
      if (s == 15) cs_get(3, NJ * 3);
    }
    if (NJ) op_at(tc, IC<0>{});
    static_for<0, 3>([&](auto bc) {
      constexpr int b = decltype(bc)::value;
      acc[b] = mfma(wg[t & 1][b], xf[t], acc[b]);
      if (t < 31) ld1b(t + 1, b);
      if (p == ((b + 2 * NJ) & 3) || (s == 15 && p == 2 && ((b + 2 * NJ) & 3) == 3)) { if (!(s == 15 && p == 3)) cs[b] = mfma16(sel, wg[t & 1][b], cs[b]); }
      SB0();
    });
    if (!NJ) op_at(tc, IC<0>{});
    op_at(tc, IC<1>{});
    if (Tg == 39) flag_store(rs_ws, WS_FQKV + (unsigned)((rb * 16 + g) * 64 + w * 8), tg, lane);
    if (Tg == 4 * ATT_B - 1 && w == 0) fq = ld16c_asm(rs_ws, qv, 0u);
    SB0();
  });
  STAMP(5); CSTAMP(19);
  const float r2 = rstd * rstd;
  f32x4 cq[3][4];
#pragma unroll
  for (int b = 0; b < 3; ++b)
#pragma unroll
    for (int i = 0; i < 4; ++i) cq[b][i] = lds_f4(L_CSW + w * 384 + b * 128 + (8 * i + 4 * h) * 4);
#pragma unroll
  for (int b = 0; b < 3; ++b) {
    float v[16];
#pragma unroll
    for (int i = 0; i < 4; ++i)
#pragma unroll
      for (int e = 0; e < 4; ++e) v[4 * i + e] = acc[b][4 * i + e] - mean * cq[b][i][e];
    float hv[8];
#pragma unroll
    for (int i = 0; i < 8; ++i) hv[i] = fmaxf(v[i], 0.f) * v[8 + i] * r2;
    const u32x4 o = {pk(hv[0], hv[1]), pk(hv[2], hv[3]), pk(hv[4], hv[5]), pk(hv[6], hv[7])};
    st16c(o, rs_ws, (unsigned)lane * 16u + (NJ * 3 + b < nu ? 0u : 0x40000000u), WS_H + (unsigned)(sb * NU + u0 + NJ * 3 + b) * 1024u);
  }
  STAMP(6);
  wait_vm<3>();
  return early;
}

template <bool FULL, class F1, class F3, class F2>
DI void attn_pair(int j0, int qb, int lane, F1 side1, F3 side2, F2 head_done, u32x4 (&o)[2]) {
  const int q = lane & 31, hh = lane >> 5, tq = q - 4 * hh;
  const float NEG = -__builtin_inff();
  bf16x8 qf[2];
#pragma unroll
  for (int hd = 0; hd < 2; ++hd) qf[hd] = lds_b128(LA_Q + (unsigned)(hd * 4 + qb) * 1024u + lane * 16u);
  unsigned kb = LA_K + (unsigned)qb * 1024u + lane * 16u;
  auto ldk = [&](int hd, int j) { return lds_b128(kb + (unsigned)(hd * 12 + j) * 1024u); };
  auto pin = [&]() { asm volatile("" : "+v"(kb)); };
  auto on = [&](int j) { return FULL || j >= j0; };
  f32x16 c[2], s[2];
  bf16x8 kf[2];
#pragma unroll
  for (int i = 0; i < 16; ++i) c[0][i] = 0.f;
  float m0[2], m1[2];
  {
    f32x16 s8[2], s0[2];
#pragma unroll
    for (int hd = 0; hd < 2; ++hd) { s8[hd] = mfma(ldk(hd, 8), qf[hd], c[0]); if (FULL) s0[hd] = mfma(ldk(hd, 0), qf[hd], c[0]); }
    if (on(1)) { kf[0] = ldk(0, 1); kf[1] = ldk(1, 1); }
    SB0();
#pragma unroll
    for (int hd = 0; hd < 2; ++hd) {
      m0[hd] = NEG; m1[hd] = NEG;
#pragma unroll
      for (int i = 0; i < 16; i += 2) {
        const int c0 = (i & 3) + 8 * (i >> 2);
        const float a = c0 <= tq ? s8[hd][i] : (FULL ? s0[hd][i] : NEG), b = c0 + 1 <= tq ? s8[hd][i + 1] : (FULL ? s0[hd][i + 1] : NEG);
        if (i & 2) m1[hd] = fmaxf(fmaxf(m1[hd], a), b); else m0[hd] = fmaxf(fmaxf(m0[hd], a), b);
      }
      asm volatile("" : "+v"(m0[hd]), "+v"(m1[hd]));
      if (hd == 0) { if (on(1)) s[0] = mfma(kf[0], qf[0], c[0]); if (on(2)) kf[0] = ldk(0, 2); }
      SB0();
    }
  }
  static_for<0, 14>([&](auto nc) {
    constexpr int n = decltype(nc)::value, hd = n & 1, j = 1 + (n >> 1), hn = (n + 1) & 1, jn = 1 + ((n + 1) >> 1);
    pin();
    if (n + 1 < 14) { if (on(jn)) s[hn] = mfma(kf[hn], qf[hn], c[0]); if (jn < 7 && on(jn + 1)) kf[hn] = ldk(hn, jn + 1); }
    SB0();
    if (on(j)) {
#pragma unroll
      for (int i = 0; i < 8; i += 2) { m0[hd] = fmaxf(fmaxf(m0[hd], s[hd][i]), s[hd][i + 1]); m1[hd] = fmaxf(fmaxf(m1[hd], s[hd][8 + i]), s[hd][9 + i]); }
      asm volatile("" : "+v"(m0[hd]), "+v"(m1[hd]));
    }
    side1(n);
    SB0();
  });
#pragma unroll
  for (int hd = 0; hd < 2; ++hd) { const float m = xhalf_max(fmaxf(m0[hd], m1[hd]));
#pragma unroll
    for (int i = 0; i < 16; ++i) c[hd][i] = -m; }
  const int grp = (lane >> 4) & 1, q4 = (lane & 15) >> 2, p4 = lane & 3;
  const unsigned vl = (unsigned)((p4 & 1) * 512 + (((4 * hh + q4) ^ (4 * (p4 & 1))) * 16) + (p4 >> 1) * 8);
  unsigned vb[2];
#pragma unroll
  for (int hd = 0; hd < 2; ++hd) vb[hd] = grp ? L_ONES + 128u : LA_V + (unsigned)(hd * 12 + qb) * 1024u + vl;
  bf16x8 vf[2][2];
  auto ldv = [&](int hd, int j) {
#pragma unroll
    for (int u = 0; u < 2; ++u) vf[hd][u] = cat(lds_tr(vb[hd] + j * 1024u + u * 256u), lds_tr(vb[hd] + j * 1024u + u * 256u + 128u));
  };
  f32x16 acc[2];
#pragma unroll
  for (int hd = 0; hd < 2; ++hd)
#pragma unroll
    for (int i = 0; i < 16; ++i) acc[hd][i] = 0.f;
  auto pv = [&](int hd, const unsigned (&pp)[8]) {
#pragma unroll
    for (int u = 0; u < 2; ++u) acc[hd] = mfma(vf[hd][u], __builtin_bit_cast(bf16x8, u32x4{pp[4 * u], pp[4 * u + 1], pp[4 * u + 2], pp[4 * u + 3]}), acc[hd]);
  };
  if (on(1)) { kf[0] = ldk(0, 1); kf[1] = ldk(1, 1); s[0] = mfma(kf[0], qf[0], c[0]); ldv(0, 1); }
  if (on(2)) kf[0] = ldk(0, 2);
  static_for<0, 14>([&](auto nc) {
    constexpr int n = decltype(nc)::value, hd = n & 1, j = 1 + (n >> 1), hn = (n + 1) & 1, jn = 1 + ((n + 1) >> 1);
    pin();
    if (n + 1 < 14) { if (on(jn)) { s[hn] = mfma(kf[hn], qf[hn], c[hn]); ldv(hn, jn); } if (jn < 7 && on(jn + 1)) kf[hn] = ldk(hn, jn + 1); }
    SB0();
    if (on(j)) {
      unsigned pp[8];
#pragma unroll
      for (int i = 0; i < 8; ++i) pp[i] = pk(__builtin_amdgcn_exp2f(s[hd][2 * i]), __builtin_amdgcn_exp2f(s[hd][2 * i + 1]));
      pv(hd, pp);
    }
    side2(n);
    SB0();
  });
  pin();
  f32x16 s8[2], s0[2];
#pragma unroll
  for (int hd = 0; hd < 2; ++hd) { s8[hd] = mfma(ldk(hd, 8), qf[hd], c[hd]); if (FULL) s0[hd] = mfma(ldk(hd, 0), qf[hd], c[hd]); }
#pragma unroll
  for (int hd = 0; hd < 2; ++hd) {
    ldv(hd, 8);
    unsigned p8[8], p0[8];
#pragma unroll
    for (int i = 0; i < 8; ++i) {
      const int c0 = ((2 * i) & 3) + 8 * (i >> 1);
      const bool va = c0 <= tq, vb_ = c0 + 1 <= tq;
      const float a = va ? s8[hd][2 * i] : (FULL ? s0[hd][2 * i] : NEG), b = vb_ ? s8[hd][2 * i + 1] : (FULL ? s0[hd][2 * i + 1] : NEG);
      const unsigned pp = pk(__builtin_amdgcn_exp2f(a), __builtin_amdgcn_exp2f(b));
      const unsigned mk = (va ? 0xffffu : 0u) | (vb_ ? 0xffff0000u : 0u);
      p8[i] = pp & mk; p0[i] = pp & ~mk;
    }
    pv(hd, p8);
    if (FULL) { ldv(hd, 0); pv(hd, p0); }
    const float inv = __builtin_amdgcn_rcpf(acc[hd][8]);
    o[hd] = u32x4{pk(acc[hd][0] * inv, acc[hd][1] * inv), pk(acc[hd][2] * inv, acc[hd][3] * inv), pk(acc[hd][4] * inv, acc[hd][5] * inv), pk(acc[hd][6] * inv, acc[hd][7] * inv)};
    head_done(hd);
  }
}

constexpr int DW = 10, DA = 6, NR = 12, NK3 = 30, NDN = 22, OCHK = NDN - (DA - 1), OPOLL = OCHK - 6, XLD = 18;
struct Sched3 {
  int park[NR] = {}, before[NK3 + 1] = {}, lastW[NK3] = {}, lastA[NK3] = {};
  constexpr Sched3() {
    int c = 0;
    for (int i = 0; i < DW - 1; ++i) { c += 2; lastW[i] = c; }
    c += 1;
    for (int i = 0; i < NR; ++i) { park[i] = c - lastW[i]; c += 2; lastW[i + DW - 1] = c; }
    c += 2; lastW[NR + DW - 1] = c;
    for (int i = 0; i < DA - 1; ++i) { c += 2; lastA[i] = c; }
    for (int i = 0; i < NK3; ++i) {
      before[i] = c;
      if (i > NR && i + DW - 1 < NK3) { c += 2; lastW[i + DW - 1] = c; }
      if (i + DA - 1 < NK3) { c += 2; lastA[i + DA - 1] = c; }
      if (i == XLD) c += 4;
    }
    before[NK3] = c;
  }
  constexpr int wait(int i) const { const int w = i + 1 < NK3 ? lastW[i + 1] : 0; return before[i] - (lastA[i] > w ? lastA[i] : w); }
};
DI unsigned l3w(int kq) { return kq < 3 ? 86016u + (unsigned)kq * (DW * 2048u) : 0u; }

DI void phase23(const Ptrs& P, bool early, int rb1, int g1, u64 epoch, int rb, int hp, int rb64, int cb, int rot, int w, int lane, Tag tg, Stamps& T) {
  const rsrc_t rs_ws = mk_rsrc(P.ws, WS_BYTES);
  for (unsigned i = threadIdx.x; i < ONES_BYTES / 16; i += 512) *(LAS u32x4*)(lds + L_ONES + i * 16) = u32x4{ONES2, ONES2, ONES2, ONES2};
  NOTE(63, early ? 1 : 2);
  if (!early) {
    bool act; const unsigned qv = qkv_flag_voff(rb, hp, lane, act);
    if (w == 0) flags_spin(rs_ws, qv, act, tg);
    bar();
    const AttnSrc asrc = attn_src(rb, hp, w);
#pragma unroll
    for (int i = 0; i < 7; ++i) attn_issue(rs_ws, asrc, w, lane, i, true);
    wait_vm<0>();
  }
  bar();
  STAMP(9);
  const unsigned fh_off = WS_FH + (unsigned)((rb1 * 16 + g1) * 64 + w * 8);
  const int h = lane >> 5, nsub = (lane >> 4) & 1, q4 = (lane & 15) >> 2, p4 = lane & 3;
  f32x16 acc[4];
  u32x2 xr[4];
  const int kq = w - 4;
  const bool keeper = blockIdx.x == 0 && w == 4;
  u32x4 fe[4];
  const int ulo = kq < 3 ? 22 * kq : 65, nun = kq < 2 ? 22 : 21;
  const unsigned ring = __builtin_amdgcn_readfirstlane(l3w(kq));
  if (w < 4) {
    __builtin_amdgcn_s_setprio(3);
    const int sb = 4 * rb + w;
    const unsigned o_off = WS_O + (unsigned)(sb * 32 + 2 * hp) * 1024u, fo_off = WS_FO + (unsigned)((rb * 16 + hp) * 64 + w * 8);
    auto side1 = [&](int n) { if (n == 13) STAMP(10); };
    auto side_h = [&](int n) { if (n == 2) { wait_vm<0>(); flag_store(rs_ws, fh_off, tg, lane); } };
    u32x4 o[2];
    auto store_o = [&](int hd) { st16c(o[hd], rs_ws, (unsigned)lane * 16u, o_off + (unsigned)hd * 1024u); };
    if (sb >= 8) attn_pair<true>(0, w, lane, side1, side_h, store_o, o); else attn_pair<false>(8 - sb, w, lane, side1, side_h, store_o, o);
    STAMP(11);
    wait_vm<0>();
    flag_store(rs_ws, fo_off, tg, lane); flag_store(rs_ws, fo_off + 32u, tg, lane);
    STAMP(12);
  } else {
    const rsrc_t rs_d = mk_rsrc(P.wd, FF * DM * 2), rs_o = mk_rsrc(P.wo, DM * DM * 2);
    unsigned vw[2];
#pragma unroll
    for (int j = 0; j < 2; ++j) vw[j] = (unsigned)(8 * j + (lane >> 3)) * 1024u + (unsigned)(((lane & 7) ^ (((lane >> 4) & 1) << 2)) * 16);
    const unsigned vo = (unsigned)lane * 16u;
    auto unit = [&](int i) { const int r = i + rot; return i < NDN ? (i < nun ? ulo + (r >= nun ? r - nun : r) : -1) : 8 * kq + ((i - NDN + rot) & 7); };
    auto reqW = [&](int i) {
      const int u = unit(i);
#pragma unroll
      for (int j = 0; j < 2; ++j) dma16(ring + (unsigned)(i % DW) * 2048u + j * 1024u, u < 0 ? 0x40000000u : vw[j], i < NDN ? rs_d : rs_o, (unsigned)cb * 128u + (unsigned)(u < 0 ? 0 : u) * 16384u);
    };
    const unsigned hv = WS_FH + (unsigned)((rb64 >> 1) * 1024 + lane * 16), ov = WS_FO + (unsigned)((rb64 >> 1) * 1024 + lane * 16);
    const int pg = lane >> 2;
    const bool pair_ok = ((lane & 3) & 1) == (rb64 & 1);
    const bool h_act = pair_ok && (((pg + 1) * NU) >> 4) > ulo && ((pg * NU) >> 4) < ulo + nun, o_act = pair_ok && (pg >> 2) == kq;
    unsigned wa[2];
#pragma unroll
    for (int nb = 0; nb < 2; ++nb) wa[nb] = ring + (unsigned)h * 512u + q4 * 128u + (unsigned)((nb ^ (q4 >> 1)) * 64) + nsub * 32u + p4 * 8u;
    auto rdw = [&](int i, int nb) { const unsigned a = wa[nb] + (unsigned)(i % DW) * 2048u; return cat(lds_tr(a), lds_tr(a + 1024u)); };
    static constexpr Sched3 S3{};
#pragma unroll
    for (int i = 0; i < DW - 1; ++i) reqW(i);
    wait_vm<2 * (DW - 1)>(); flag_store(rs_ws, fh_off, tg, lane);
    bf16x8 wr[NR][2];
    static_for<0, NR>([&](auto ic) {
      constexpr int i = decltype(ic)::value;
      wait_vm<S3.park[i]>();
      wr[i][0] = rdw(i, 0); wr[i][1] = rdw(i, 1);
      reqW(i + DW - 1);
      asm volatile("s_waitcnt lgkmcnt(0)" : "+v"(wr[i][0]), "+v"(wr[i][1]) :: "memory");
    });
    reqW(NR + DW - 1);
    flags_spin(rs_ws, hv, h_act, tg);
    STAMP(13);
    u32x4 A[DA][2];
    auto reqA = [&](int i) {
      const int u = unit(i);
#pragma unroll
      for (int mb = 0; mb < 2; ++mb)
        A[i % DA][mb] = ld16c_asm(rs_ws, u < 0 ? 0x40000000u : vo, i < NDN ? WS_H + (unsigned)((2 * rb64 + mb) * NU + (u < 0 ? 0 : u)) * 1024u : WS_O + (unsigned)((2 * rb64 + mb) * 32 + u) * 1024u);
    };
#pragma unroll
    for (int i = 0; i < DA - 1; ++i) reqA(i);
#pragma unroll
    for (int b = 0; b < 4; ++b)
#pragma unroll
      for (int i = 0; i < 16; ++i) acc[b][i] = 0.f;
    const int row = 64 * rb64 + 32 * (kq >> 1) + (lane & 31), col0 = 64 * cb + 32 * (kq & 1) + 4 * h;
    const unsigned short* xp = (const unsigned short*)P.x + (size_t)row * DM + col0;
    u32x4 fo;
    bf16x8 wf[2][2];
    static_for<0, NK3>([&](auto ic) {
      constexpr int i = decltype(ic)::value;
      wait_vm<S3.wait(i)>();
      tie(A[i % DA][0]); tie(A[i % DA][1]);
      if (i == 0) STAMP(14);
      if constexpr (i == OCHK) {
        wait_vm_tie<S3.before[i] - S3.before[OPOLL + 1]>(fo);
        NOTE(54, flags_ok(fo, o_act, tg) ? 1 : 2);
        if (!flags_ok(fo, o_act, tg)) flags_spin(rs_ws, ov, o_act, tg);
      }
      const bf16x8 w0 = i < NR ? wr[i < NR ? i : 0][0] : wf[i & 1][0], w1 = i < NR ? wr[i < NR ? i : 0][1] : wf[i & 1][1];
      SB0();
      acc[0] = mfma(w0, __builtin_bit_cast(bf16x8, A[i % DA][0]), acc[0]);
      if constexpr (i + 1 >= NR && i + 1 < NK3) { wf[(i + 1) & 1][0] = rdw(i + 1, 0); wf[(i + 1) & 1][1] = rdw(i + 1, 1); }
      SB0();
      acc[1] = mfma(w1, __builtin_bit_cast(bf16x8, A[i % DA][0]), acc[1]);
      SB0();
      if constexpr (i > NR && i + DW - 1 < NK3) reqW(i + DW - 1);
      acc[2] = mfma(w0, __builtin_bit_cast(bf16x8, A[i % DA][1]), acc[2]);
      SB0();
      acc[3] = mfma(w1, __builtin_bit_cast(bf16x8, A[i % DA][1]), acc[3]);
      SB0();
      if constexpr (i + DA - 1 < NK3) reqA(i + DA - 1);
      if (i == OPOLL) fo = ld16c_asm(rs_ws, ov, 0u);
      if (i == XLD) {
#pragma unroll
        for (int q = 0; q < 4; ++q) xr[q] = *(const GAS u32x2*)(xp + 8 * q);
      }
      if (i == 20 && keeper) {
#pragma unroll
        for (int q = 0; q < 4; ++q) fe[q] = ld16c_asm(rs_ws, WS_FQKV + (unsigned)(q * 64 + lane) * 64u, 0u);
      }
      SB0();
    });
    STAMP(15);
  }
  if (w >= 4) {
#pragma unroll
    for (int b = 0; b < 4; ++b) {
      if (b != kq) {
        const unsigned base = ring + (unsigned)((b - kq - 1 + 4) % 4) * 4096u + lane * 16u;
#pragma unroll
        for (int q = 0; q < 4; ++q) *(LAS f32x4*)(lds + base + q * 1024u) = f32x4{acc[b][4 * q], acc[b][4 * q + 1], acc[b][4 * q + 2], acc[b][4 * q + 3]};
      }
    }
  }
  bar();
  if (w >= 4) {
    f32x16 r = kq == 0 ? acc[0] : (kq == 1 ? acc[1] : (kq == 2 ? acc[2] : acc[3]));
#pragma unroll
    for (int s = 1; s < 4; ++s) {
      const int src = (kq + s) & 3;
      const unsigned base = l3w(src) + (unsigned)((kq - src - 1 + 4) % 4) * 4096u + lane * 16u;
#pragma unroll
      for (int q = 0; q < 4; ++q) { const f32x4 v = lds_f4(base + q * 1024u);
#pragma unroll
        for (int e = 0; e < 4; ++e) r[4 * q + e] += v[e]; }
    }
    const int row = 64 * rb64 + 32 * (kq >> 1) + (lane & 31), col0 = 64 * cb + 32 * (kq & 1);
    unsigned o[8];
#pragma unroll
    for (int q = 0; q < 4; ++q) { o[2 * q] = pk(r[4 * q] + bf_lo(xr[q][0]), r[4 * q + 1] + bf_hi(xr[q][0])); o[2 * q + 1] = pk(r[4 * q + 2] + bf_lo(xr[q][1]), r[4 * q + 3] + bf_hi(xr[q][1])); }
    unsigned short* op = (unsigned short*)P.out + (size_t)row * DM + col0 + 8 * h;
#pragma unroll
    for (int q = 0; q < 4; q += 2) {
      const auto t0 = __builtin_amdgcn_permlane32_swap(o[2 * q], o[2 * q + 2], false, false), t1 = __builtin_amdgcn_permlane32_swap(o[2 * q + 1], o[2 * q + 3], false, false);
      *(GAS u32x4*)(op + 8 * q) = u32x4{t0[0], t1[0], t0[1], t1[1]};
    }
    if (keeper) {
#pragma unroll
      for (int q = 0; q < 4; ++q) { tie(fe[q]); if (!flags_ok(fe[q], true, tg)) flags_spin(rs_ws, WS_FQKV + (unsigned)(q * 64 + lane) * 64u, true, tg); }
      if (lane == 0) __hip_atomic_store((GAS u64*)(P.ws + WS_EPOCH), epoch, __ATOMIC_RELAXED, __HIP_MEMORY_SCOPE_AGENT);
    }
  }
}

__global__ __launch_bounds__(512, 2) void mk_kernel(const void* x, const void* wqkv, const void* wo, const void* wg, const void* wu, const void* wd, void* out, char* ws) {
  u64 pc0 = 0;
  asm volatile("mk_code_begin:\n\ts_getpc_b64 %0" : "=s"(pc0));
  {
    unsigned len;
    asm volatile("s_mov_b32 %0, mk_code_end-mk_code_begin" : "=s"(len));
    const unsigned wv = __builtin_amdgcn_readfirstlane(threadIdx.x >> 6);
    const unsigned per = ((len >> 5) + 1023u) & ~1023u;
    const unsigned o = (blockIdx.x >> 3) * per + wv * 1024u;
    dma16(L_DUMMY + wv * 1024u, (threadIdx.x & 63u) * 16u, mk_rsrc((const void*)(pc0 & ~255ull), len & ~15u), wv * 1024u < per ? o : 0x40000000u);
  }
  const Ptrs P{x, wqkv, wo, wg, wu, wd, out, ws, pc0};
  Stamps T;
  STAMP(0); CSTAMP(20);
  __builtin_amdgcn_s_setprio(1);
  const int b = blockIdx.x, xcd = b & 7, idx = b >> 3;
  const int w = __builtin_amdgcn_readfirstlane(threadIdx.x >> 6), lane = threadIdx.x & 63;
  Tag tg; u64 epoch;
  const int rb = 2 * (idx >> 2) + (xcd >> 2), g = (xcd & 3) * 4 + (idx & 3), rot1 = ((idx >> 2) + 2 * (idx & 3)) & 7;
  bool early;
  Pro pro;
  prologue(P, rb, g, rot1, w, lane, pro);
  if (w < 4) early = phase1<0>(P, pro, rb, g, rot1, w, lane, rb, g, tg, epoch, T); else early = phase1<1>(P, pro, rb, g, rot1, w - 4, lane, rb, g, tg, epoch, T);
  const int rot3 = ((idx & 7) * 2 + 5 * (idx >> 3)) % 21;
  phase23(P, early, rb, g, epoch, rb, g, xcd * 4 + (idx >> 3), idx & 7, rot3, w, lane, tg, T);
  asm volatile("mk_code_end:");
}

}

extern "C" void kernel_launch(void* const* d_in, const int* in_sizes, int n_in, void* d_out, int out_size, void* d_ws, size_t ws_size, hipStream_t stream) {
  const void *x = d_in[0], *wqkv = d_in[1], *wo = d_in[2], *wg = d_in[4], *wu = d_in[5], *wd = d_in[6];
  mk_kernel<<<256, 512, 0, stream>>>(x, wqkv, wo, wg, wu, wd, d_out, (char*)d_ws);
}
```

```cpp
#include <hip/hip_runtime.h>
#include <hip/hip_bf16.h>
#include <stdint.h>

namespace {

typedef short bf16x8 __attribute__((ext_vector_type(8)));
typedef short s16x4 __attribute__((ext_vector_type(4)));
typedef float f32x16 __attribute__((ext_vector_type(16)));
typedef float f32x4 __attribute__((ext_vector_type(4)));
typedef float f32x2 __attribute__((ext_vector_type(2)));
typedef unsigned u32x4 __attribute__((ext_vector_type(4)));
typedef unsigned u32x2 __attribute__((ext_vector_type(2)));
typedef __bf16 bf16x2_t __attribute__((ext_vector_type(2)));
typedef short v4i16_t __attribute__((ext_vector_type(4)));
typedef __amdgpu_buffer_rsrc_t rsrc_t;
#define LAS __attribute__((address_space(3)))
#define GAS __attribute__((address_space(1)))
#define DI __device__ __forceinline__
#define SB0() __builtin_amdgcn_sched_barrier(0)
typedef unsigned long long u64;

constexpr int SEQ = 2048, DM = 512, FF = 1376, NU = 86;
constexpr unsigned WS_EPOCH = 0;
constexpr unsigned WS_FQKV = 16384, WS_FH = 32768, WS_FO = 49152;
constexpr unsigned WS_RS = 65536;
constexpr unsigned WS_QKV = 1u << 20;
constexpr unsigned WS_H = 7u << 20;
constexpr unsigned WS_O = 13u << 20;
constexpr unsigned WS_BYTES = 16u << 20;
constexpr int NPRO = 3;
constexpr unsigned SLOT1 = 28672;
constexpr unsigned L_CSP = 143360;
constexpr unsigned L_CSW = 146432;
constexpr unsigned L_ONES = 152576;
constexpr unsigned ONES_BYTES = 9984;
constexpr unsigned L_DUMMY = 152576;
constexpr unsigned L_WORD = 163584;
constexpr unsigned LDS_BYTES = 163840;
constexpr unsigned LA_Q = 28672, LA_K = LA_Q + 8192, LA_V = LA_K + 24576;

__shared__ __attribute__((aligned(1024))) char lds[LDS_BYTES];

DI unsigned pk(float a, float b) { f32x2 v = {a, b}; bf16x2_t r = __builtin_convertvector(v, bf16x2_t); return __builtin_bit_cast(unsigned, r); }
DI rsrc_t mk_rsrc(const void* p, unsigned bytes) { return __builtin_amdgcn_make_buffer_rsrc((void*)p, 0, bytes, 0x00020000); }
DI void dma16(unsigned ldsaddr, unsigned voff, rsrc_t rs, unsigned soff) {
  asm volatile("s_mov_b32 m0, %0\n\ts_nop 0\n\tbuffer_load_dwordx4 %1, %2, %3 offen lds" ::"s"(ldsaddr), "v"(voff), "s"(rs), "s"(soff) : "memory");
}
DI void dma16c(unsigned ldsaddr, unsigned voff, rsrc_t rs, unsigned soff) {
  asm volatile("s_mov_b32 m0, %0\n\ts_nop 0\n\tbuffer_load_dwordx4 %1, %2, %3 offen sc1 lds" ::"s"(ldsaddr), "v"(voff), "s"(rs), "s"(soff) : "memory");
}
template <int N> DI void wait_vm() { asm volatile("s_waitcnt vmcnt(%0)" ::"n"(N) : "memory"); }
template <int N> DI void wait_vm_tie(u32x4& v) { asm volatile("s_waitcnt vmcnt(%1)" : "+v"(v) : "n"(N) : "memory"); }
template <int V> struct IC { static constexpr int value = V; };
template <int I, int N, class F> DI void static_for(F&& f) { if constexpr (I < N) { f(IC<I>{}); static_for<I + 1, N>(f); } }
DI void bar_raw() { asm volatile("s_barrier" ::: "memory"); }
DI void tie(u32x4& v) { asm volatile("" : "+v"(v) :: "memory"); }
DI void bar() { asm volatile("s_waitcnt lgkmcnt(0)\n\ts_barrier" ::: "memory"); }
DI f32x16 mfma(bf16x8 a, bf16x8 b, f32x16 c) { return __builtin_amdgcn_mfma_f32_32x32x16_bf16(a, b, c, 0, 0, 0); }
DI bf16x8 lds_b128(unsigned a) { return *(const LAS bf16x8*)(lds + a); }
DI s16x4 lds_tr(unsigned a) { return __builtin_amdgcn_ds_read_tr16_b64_v4i16((LAS v4i16_t*)(lds + a)); }
DI f32x4 mfma16(bf16x8 a, bf16x8 b, f32x4 c) { return __builtin_amdgcn_mfma_f32_16x16x32_bf16(a, b, c, 0, 0, 0); }
DI f32x4 lds_f4(unsigned a) { return *(const LAS f32x4*)(lds + a); }
DI bf16x8 cat(s16x4 lo, s16x4 hi) { return __builtin_shufflevector(lo, hi, 0, 1, 2, 3, 4, 5, 6, 7); }
DI float xhalf_sum(float v) { auto r = __builtin_amdgcn_permlane32_swap(__float_as_uint(v), __float_as_uint(v), false, false); return __uint_as_float(r[0]) + __uint_as_float(r[1]); }
DI float xhalf_max(float v) { auto r = __builtin_amdgcn_permlane32_swap(__float_as_uint(v), __float_as_uint(v), false, false); return fmaxf(__uint_as_float(r[0]), __uint_as_float(r[1])); }
DI void st16c(u32x4 v, rsrc_t rs, unsigned voff, unsigned soff) { __builtin_amdgcn_raw_buffer_store_b128(v, rs, voff + soff, 0, 16); }
DI void st8c(u32x2 v, rsrc_t rs, unsigned voff, unsigned soff) { __builtin_amdgcn_raw_buffer_store_b64(v, rs, voff, soff, 16); }
constexpr unsigned ONES2 = 0x3F803F80u;
constexpr unsigned SPIN_CAP = 1u << 19;
template <int CTRL> DI float dpp_add(float v) { return v + __int_as_float(__builtin_amdgcn_update_dpp(0, __float_as_int(v), CTRL, 0xf, 0xf, true)); }
DI float wave_sum(float v) {
  v = dpp_add<0xB1>(v); v = dpp_add<0x4E>(v); v = dpp_add<0x141>(v); v = dpp_add<0x140>(v);
  auto a = __builtin_amdgcn_permlane16_swap(__float_as_uint(v), __float_as_uint(v), false, false);
  v = __uint_as_float(a[0]) + __uint_as_float(a[1]);
  return xhalf_sum(v);
}
DI float bf_lo(unsigned u) { return __uint_as_float(u << 16); }
DI float bf_hi(unsigned u) { return __uint_as_float(u & 0xffff0000u); }
DI u32x4 ld16_asm(rsrc_t rs, unsigned voff, unsigned soff) { u32x4 v; asm volatile("buffer_load_dwordx4 %0, %1, %2, %3 offen" : "=v"(v) : "v"(voff), "s"(rs), "s"(soff) : "memory"); return v; }
DI u32x4 ld16c_asm(rsrc_t rs, unsigned voff, unsigned soff) { u32x4 v; asm volatile("buffer_load_dwordx4 %0, %1, %2, %3 offen sc1" : "=v"(v) : "v"(voff), "s"(rs), "s"(soff) : "memory"); return v; }
struct Ptrs { const void *x, *wqkv, *wo, *wg, *wu, *wd; void* out; char* ws; };
struct Tag { unsigned a, b; };

DI void flag_store(rsrc_t rs_ws, unsigned off, Tag tg, int lane) { if (lane == 0) st8c(u32x2{tg.a, tg.b}, rs_ws, 0u, off); }
DI bool flags_ok(u32x4 v, bool act, Tag tg) { return __all(!act || (v[0] == tg.a && v[1] == tg.b && v[2] == tg.a && v[3] == tg.b)); }
DI void flags_spin(rsrc_t rs_ws, unsigned voff, bool act, Tag tg) {
  for (unsigned spins = 0; spins < SPIN_CAP; ++spins) {
    u32x4 v = ld16c_asm(rs_ws, voff, 0u); wait_vm_tie<0>(v);
    if (flags_ok(v, act, tg)) break;
    __builtin_amdgcn_s_sleep(1);
  }
}
DI void word_set(int i, unsigned v) { *(volatile LAS unsigned*)(lds + L_WORD + i * 4) = v; }
DI unsigned word_get(int i) { return __builtin_amdgcn_readfirstlane(*(volatile LAS unsigned*)(lds + L_WORD + i * 4)); }

struct AttnSrc { unsigned q, kv; int sb0; };
DI AttnSrc attn_src(int rb, int hp, int w) {
  AttnSrc a;
  a.q = __builtin_amdgcn_readfirstlane(WS_QKV + (unsigned)((2 * hp + (w >> 2)) * 64 + 4 * rb + (w & 3)) * 1024u);
  a.kv = __builtin_amdgcn_readfirstlane(WS_QKV + (unsigned)(((1 + (w >> 2)) * 32 + 2 * hp + ((w >> 1) & 1)) * 64) * 1024u);
  a.sb0 = __builtin_amdgcn_readfirstlane(4 * rb - 8 + 6 * (w & 1));
  return a;
}
DI void attn_issue(rsrc_t rs_ws, const AttnSrc& a, int w, int lane, int piece, bool go) {
  const unsigned vo = (unsigned)lane * 16u + (go ? 0u : 0x40000000u);
  if (piece == 0) { dma16c(LA_Q + (unsigned)w * 1024u, vo, rs_ws, a.q); return; }
  const int sbk = a.sb0 + piece - 1;
  dma16c(LA_K + (unsigned)((w >> 1) * 12 + 6 * (w & 1) + piece - 1) * 1024u, vo, rs_ws, a.kv + (unsigned)(sbk < 0 ? 0 : sbk) * 1024u);
}
DI unsigned qkv_flag_voff(int rb, int hp, int lane, bool& act) {
  const int c = lane >> 2, kind = c == 0 ? 0 : (c <= 3 ? 1 : 2), d = c == 0 ? 0 : (c - 1) % 3, rr = rb - d;
  act = lane < 28 && rr >= 0;
  return act ? WS_FQKV + (unsigned)((rr * 16 + (kind * 16 + hp) / 3) * 64 + (lane & 3) * 16) : 0u;
}

struct Pro { unsigned vx[2], vq[2]; };
DI void prologue(const Ptrs& P, int rb, int g, int rot, int w, int lane, Pro& o) {
  const rsrc_t rs_x = mk_rsrc(P.x, SEQ * DM * 2), rs_q = mk_rsrc(P.wqkv, DM * 1536 * 2);
  const int nj = w >> 2, mi = w & 3;
#pragma unroll
  for (int j = 0; j < 2; ++j) o.vx[j] = (unsigned)(16 * w + 8 * j + (lane >> 3)) * 1024u + (unsigned)(((lane & 7) ^ (4 * j + (lane >> 4))) * 16);
#pragma unroll
  for (int j = 0; j < 2; ++j) { const int G = (nj ? 2 * mi + j : 8 + mi) * 64 + lane;
    o.vq[j] = (unsigned)(G / 12) * 3072u + (unsigned)(G % 12) * 16u; }
  const unsigned sx0 = (unsigned)rb * 131072u, sq0 = (unsigned)g * 192u;
  const unsigned dq0 = 16384u + (unsigned)(nj ? 2 * mi : 8 + mi) * 1024u;
#pragma unroll
  for (int sg = 0; sg < NPRO; ++sg) {
    const unsigned slot = (unsigned)sg * SLOT1, c = (unsigned)(sg + rot) & 7u;
    dma16(slot + (unsigned)(2 * w) * 1024u, o.vx[0], rs_x, sx0 + c * 128u);
    dma16(slot + (unsigned)(2 * w + 1) * 1024u, o.vx[1], rs_x, sx0 + c * 128u);
    dma16(slot + dq0, o.vq[0], rs_q, sq0 + c * (64u * 3072u));
    dma16(nj ? slot + dq0 + 1024u : L_DUMMY + 4096u, nj ? o.vq[1] : 0x40000000u, rs_q, sq0 + c * (64u * 3072u));
  }
}

constexpr int ATT_B = 14;
constexpr int OP_NONE = -1, OP_ATT = 400;
template <int NJ> struct Sched1 {
  static constexpr int NA = NJ ? 4 : 3, NST = NJ ? 2 : 4;
  static constexpr int n(int sg) { return sg < 8 ? NA : 3; }
  static constexpr int TB(int s) { return s < 8 ? 4 * s + 2 : 4 * s + 3; }
  static constexpr int extras(int T) { return (T == 9 ? 1 : 0)   + (T == 27 ? 1 : 0)   + (T == 39 ? 1 : 0)  ; }
  static constexpr int PRO = 1 + NPRO * 4 + 1;
  static constexpr int HOLD = 12;
  int op[64][2] = {}, before[65] = {}, last[16] = {};
  constexpr Sched1() {
    int sg = NPRO, j = 0, att = 0, c = PRO;
    for (int i = 0; i < NPRO; ++i) last[i] = 1 + 4 * (i + 1);
    for (int T = 0; T < 64; ++T) {
      before[T] = c;
      for (int k = 0; k < 2; ++k) {
        int o = OP_NONE;
        if (k == 0 || T < 14) {
          if (sg <= 15 && (sg < 5 || T >= TB(sg - 4)) && (sg < 8 || T >= 4 * sg - HOLD)) {   o = sg * 8 + j; if (++j == n(sg)) { last[sg] = c + 1; ++sg; j = 0; } }
        }
        if (T >= 4 * ATT_B + 3 && att < 7 && (k == 0 || T >= 61)) o = OP_ATT + att++;
        op[T][k] = o;
        if (o != OP_NONE) ++c;
      }
      c += extras(T);
      if (T == 31) c += NST;
    }
    before[64] = c;
  }
  constexpr int wait_stage(int T, int sg) const { return before[T] - last[sg]; }
};

template <int NJ>
DI bool phase1(const Ptrs& P, const Pro& pro, int rb, int g, int rot, int mi, int lane, int rb2, int hp2, Tag& tg, u64& epoch) {
  typedef Sched1<NJ> L;
  static constexpr Sched1<NJ> SC{};
  const int w = NJ * 4 + mi;
  const rsrc_t rs_x = mk_rsrc(P.x, SEQ * DM * 2), rs_q = mk_rsrc(P.wqkv, DM * 1536 * 2);
  const rsrc_t rs_f = mk_rsrc(NJ ? P.wu : P.wg, DM * FF * 2), rs_ws = mk_rsrc(P.ws, WS_BYTES);
  const unsigned sx0 = (unsigned)rb * 131072u, sq0 = (unsigned)g * 192u;
  constexpr int NQ = NJ ? 2 : 1;
  constexpr int NA = 2 + NQ;
  const unsigned vx[2] = {pro.vx[0], pro.vx[1]}, vq[2] = {pro.vq[0], pro.vq[1]};
  unsigned vf[3];
  const int u0 = (g * NU) >> 4, nu = (((g + 1) * NU) >> 4) - u0;
  const unsigned sf0 = (unsigned)u0 * 32u;
  auto piece = [&](int gs, int j) {
    const unsigned slot = (unsigned)(gs % 5) * SLOT1;
    const unsigned c = (unsigned)(gs + rot) & 7u;
    if (gs < 8) {
      if (j < 2) dma16(slot + (unsigned)(2 * w + j) * 1024u, vx[j], rs_x, sx0 + c * 128u);
      else dma16(slot + 16384u + (unsigned)(NJ ? 2 * mi + j - 2 : 8 + mi) * 1024u, vq[j - 2], rs_q, sq0 + c * (64u * 3072u));
    } else dma16(slot + NJ * 12288u + (unsigned)(3 * mi + j) * 1024u, vf[j], rs_f, sf0 + c * (64u * 2752u));
  };
  const unsigned myrow = (unsigned)(128 * rb + 8 * g + w);
  u32x4 xrow = ld16_asm(rs_x, (unsigned)lane * 16u, myrow * 1024u), ev;
  if (w == 0) ev = ld16c_asm(rs_ws, 0u, WS_EPOCH);
#pragma unroll
  for (int j = 0; j < 3; ++j) { const int G = (3 * mi + j) * 64 + lane, kg = G / 48, rr = G % 48, unit = (rr >> 3) ^ NJ;
    vf[j] = unit < nu ? (unsigned)(4 * kg + ((rr & 7) >> 1)) * 2752u + (unsigned)(unit * 32 + (rr & 1) * 16) : 0x40000000u; }
  const int r = lane & 31, h = lane >> 5, nsub = (lane >> 4) & 1, q4 = (lane & 15) >> 2, p4 = lane & 3;
  unsigned xa[4], ro[4];
#pragma unroll
  for (int p = 0; p < 4; ++p) { const int ks = (p + mi) & 3;
    xa[p] = (unsigned)(32 * mi + r) * 128u + (unsigned)(((2 * ks + h) ^ ((r >> 1) & 7)) * 16);
    ro[p] = (unsigned)ks * 3072u; }
  const unsigned wl = (unsigned)(2 * h) * 768u + q4 * 32u + p4 * 8u;
  const unsigned wa = 16384u + (unsigned)(2 * h) * 768u + q4 * 192u + nsub * 32u + p4 * 8u;
  const unsigned wbE = nsub * 12288u + wl + nsub * 128u;
  const unsigned wbO = nsub * 12288u + wl - nsub * 128u;
  const AttnSrc asrc = attn_src(rb2, hp2, w);
  bool early = false;
  auto op_at = [&](auto tc, auto kc) {
    constexpr int o = SC.op[decltype(tc)::value][decltype(kc)::value];
    if constexpr (o >= OP_ATT) attn_issue(rs_ws, asrc, w, lane, o - OP_ATT, early);
    else if constexpr (o >= 0) piece(o >> 3, o & 7);
  };
  bf16x8 sel;
  { const unsigned v = (lane & 15) == ((lane >> 4) & 1) ? ONES2 : 0u; sel = __builtin_bit_cast(bf16x8, u32x4{v, v, v, v}); }

  bf16x8 xf[32];
  constexpr int NB1 = NJ ? 1 : 2;
  f32x16 acc[3];
  f32x4 cs[3];
#pragma unroll
  for (int b = 0; b < 3; ++b) { cs[b] = f32x4{0.f, 0.f, 0.f, 0.f};
#pragma unroll
    for (int i = 0; i < 16; ++i) acc[b][i] = 0.f; }
  bf16x8 wf[3][NB1], wg[2][3];
  auto ldx = [&](int t) { xf[t] = lds_b128((unsigned)((t >> 2) % 5) * SLOT1 + xa[t & 3]); };
  auto ld1a = [&](int t, int b) {
    const unsigned a = (unsigned)((t >> 2) % 5) * SLOT1 + wa + ro[t & 3] + (NJ ? 2 : b) * 64u; wf[t % 3][b] = cat(lds_tr(a), lds_tr(a + 768u));
  };
  auto ld1b = [&](int t, int b) {
    const int u = NJ * 3 + b;
    const unsigned a = (unsigned)((8 + (t >> 2)) % 5) * SLOT1 + ((u & 1) ? wbO : wbE) + ro[t & 3] + u * 128u; wg[t & 1][b] = cat(lds_tr(a), lds_tr(a + 768u));
  };
  auto cs_put = [&](int nb, int b0) {
#pragma unroll
    for (int b = 0; b < 3; ++b) if (b < nb && lane < 16) { *(LAS float*)(lds + L_CSP + (((b0 + b) * 32 + lane) * 4 + mi) * 4) = cs[b][0]; *(LAS float*)(lds + L_CSP + (((b0 + b) * 32 + 16 + lane) * 4 + mi) * 4) = cs[b][1]; }
  };
  auto cs_get = [&](int nb, int b0) {
#pragma unroll
    for (int b = 0; b < 3; ++b) if (b < nb) { const f32x4 c4 = lds_f4(L_CSP + ((b0 + b) * 32 + r) * 16);
      *(LAS float*)(lds + L_CSW + w * 384 + b * 128 + r * 4) = (c4[0] + c4[1]) + (c4[2] + c4[3]); }
  };
  wait_vm<(NPRO - 1) * 4 + 1>();
  bar_raw();
  ldx(0); ldx(1);
#pragma unroll
  for (int b = 0; b < NB1; ++b) { ld1a(0, b); ld1a(1, b); }
  u32x4 sv;
  float rmean = 0.f, rrstd = 0.f;
  const unsigned sv_off = WS_RS + (unsigned)(128 * rb + 32 * mi) * 16u;
  static_for<0, 32>([&](auto tc) {
    constexpr int t = decltype(tc)::value, s = t >> 2, p = t & 3;
    if constexpr (p == 2) {
      if (s == 7) cs_put(NB1, NJ ? 2 : 0);
      if constexpr (s == 1) {
        wait_vm_tie<(SC.wait_stage(t, 2) < SC.before[t] - L::PRO ? SC.wait_stage(t, 2) : SC.before[t] - L::PRO)>(xrow);
        if (w == 0) { tie(ev); word_set(2, ev[0]); word_set(3, ev[1]); }
      } else wait_vm<SC.wait_stage(t, s + 1)>();
      if (s == 7 || s == 1) bar(); else bar_raw();
      if constexpr (s == 1) {
        const u64 e0 = (u64)word_get(2) | ((u64)word_get(3) << 32);
        epoch = e0 + 1ull;
        tg.a = (unsigned)epoch; tg.b = ((unsigned)(e0 >> 32) ^ 0x5bd1e995u) + tg.a;
      }
      if (s == 7) cs_get(NB1, NJ ? 2 : 0);
    }
    if (NJ) op_at(tc, IC<0>{});
    static_for<0, NB1>([&](auto bc) {
      constexpr int b = decltype(bc)::value;
      acc[b] = mfma(wf[t % 3][b], xf[t], acc[b]);
      if (t < 30) { if (b == 0) ldx(t + 2); ld1a(t + 2, b); }
      if (t == 31) { ld1b(0, b); if (b == NB1 - 1) { for (int bb = NB1; bb < 3; ++bb) ld1b(0, bb); } }
      if (p == (NJ ? 1 : b)) cs[b] = mfma16(sel, wf[t % 3][b], cs[b]);
      if (b == 0) {
        if (t == 6 + NJ) {
          float e[8];
#pragma unroll
          for (int i = 0; i < 4; ++i) { e[2 * i] = bf_lo(xrow[i]); e[2 * i + 1] = bf_hi(xrow[i]); }
          rmean = wave_sum(((e[0] + e[1]) + (e[2] + e[3])) + ((e[4] + e[5]) + (e[6] + e[7]))) * (1.f / 512.f);
          asm volatile("" : "+v"(rmean));
        }
        if (t == 8) {
          float q = 0.f;
#pragma unroll
          for (int i = 0; i < 4; ++i) { const float d0 = bf_lo(xrow[i]) - rmean, d1 = bf_hi(xrow[i]) - rmean; q = fmaf(d0, d0, q); q = fmaf(d1, d1, q); }
          rrstd = __builtin_amdgcn_rsqf(wave_sum(q) * (1.f / 512.f) + 1e-5f);
          asm volatile("" : "+v"(rrstd));
        }
      }
      SB0();
      if (b == 0) op_at(tc, IC<NJ ? 1 : 0>{});
    });
    if (!NJ) op_at(tc, IC<1>{});
    if (t == 27) sv = ld16c_asm(rs_ws, (unsigned)r * 16u, sv_off);
    if (t == 9 && lane == 0) st16c(u32x4{__float_as_uint(rmean), tg.a, __float_as_uint(rrstd), tg.b}, rs_ws, 0u, WS_RS + myrow * 16u);
    SB0();
  });
  { constexpr int after = SC.before[32] - L::NST - SC.before[28]; wait_vm_tie<after>(sv); }
  for (unsigned spins = 0; !__all(sv[1] == tg.a && sv[3] == tg.b) && spins < SPIN_CAP; ++spins) {
    __builtin_amdgcn_s_sleep(1);
    sv = ld16c_asm(rs_ws, (unsigned)r * 16u, sv_off); wait_vm_tie<0>(sv);
  }
  const float mean = __uint_as_float(sv[0]), rstd = __uint_as_float(sv[2]);
  const int sb = rb * 4 + mi;
#pragma unroll
  for (int b = 0; b < NB1; ++b) {
    const int blk = 3 * g + (NJ ? 2 : b), which = blk >> 4, hp = blk & 15;
    const float sc = which == 0 ? rstd * (0.25f * 1.4426950408889634f) : rstd;
    float v[16];
#pragma unroll
    for (int i = 0; i < 4; ++i) { const f32x4 c4 = lds_f4(L_CSW + w * 384 + b * 128 + (8 * i + 4 * h) * 4);
#pragma unroll
      for (int e = 0; e < 4; ++e) v[4 * i + e] = (acc[b][4 * i + e] - mean * c4[e]) * sc; }
    const unsigned vo = which == 2 ? (unsigned)(h * 512 + ((r ^ (4 * h)) * 16)) : (unsigned)(lane * 16);
#pragma unroll
    for (int hd = 0; hd < 2; ++hd) {
      const u32x4 o = {pk(v[8 * hd], v[8 * hd + 1]), pk(v[8 * hd + 2], v[8 * hd + 3]), pk(v[8 * hd + 4], v[8 * hd + 5]), pk(v[8 * hd + 6], v[8 * hd + 7])};
      st16c(o, rs_ws, vo, WS_QKV + (unsigned)((which * 32 + 2 * hp + hd) * 64 + sb) * 1024u);
    }
  }
#pragma unroll
  for (int b = 0; b < 3; ++b) { cs[b] = f32x4{0.f, 0.f, 0.f, 0.f};
#pragma unroll
    for (int i = 0; i < 16; ++i) acc[b][i] = 0.f; }
  bool act;
  const unsigned qv = qkv_flag_voff(rb2, hp2, lane, act);
  u32x4 fq;
  SB0();
  static_for<32, 64>([&](auto tc) {
    constexpr int Tg = decltype(tc)::value, t = Tg - 32, s = Tg >> 2, p = t & 3;
    if constexpr (p == 3) {
      if (s == 15) cs_put(3, NJ * 3);
      if constexpr (s == 9) wait_vm<(SC.before[Tg] - SC.before[32] < SC.wait_stage(Tg, 10) ? SC.before[Tg] - SC.before[32] : SC.wait_stage(Tg, 10))>();
      else if constexpr (s == ATT_B) { wait_vm<0>(); if (w == 0) { tie(fq); word_set(0, flags_ok(fq, act, tg) ? 1u : 0u); } }
      else if constexpr (s <= 14) wait_vm<SC.wait_stage(Tg, s + 1)>();
      if (s == ATT_B || s == 15) bar(); else bar_raw();
      if (s == ATT_B) early = word_get(0) != 0u;
      if (s == 15) cs_get(3, NJ * 3);
    }
    if (NJ) op_at(tc, IC<0>{});
    static_for<0, 3>([&](auto bc) {
      constexpr int b = decltype(bc)::value;
      acc[b] = mfma(wg[t & 1][b], xf[t], acc[b]);
      if (t < 31) ld1b(t + 1, b);
      if (p == ((b + 2 * NJ) & 3) || (s == 15 && p == 2 && ((b + 2 * NJ) & 3) == 3)) { if (!(s == 15 && p == 3)) cs[b] = mfma16(sel, wg[t & 1][b], cs[b]); }
      SB0();
    });
    if (!NJ) op_at(tc, IC<0>{});
    op_at(tc, IC<1>{});
    if (Tg == 39) flag_store(rs_ws, WS_FQKV + (unsigned)((rb * 16 + g) * 64 + w * 8), tg, lane);
    if (Tg == 4 * ATT_B - 1 && w == 0) fq = ld16c_asm(rs_ws, qv, 0u);
    SB0();
  });
  const float r2 = rstd * rstd;
  f32x4 cq[3][4];
#pragma unroll
  for (int b = 0; b < 3; ++b)
#pragma unroll
    for (int i = 0; i < 4; ++i) cq[b][i] = lds_f4(L_CSW + w * 384 + b * 128 + (8 * i + 4 * h) * 4);
#pragma unroll
  for (int b = 0; b < 3; ++b) {
    float v[16];
#pragma unroll
    for (int i = 0; i < 4; ++i)
#pragma unroll
      for (int e = 0; e < 4; ++e) v[4 * i + e] = acc[b][4 * i + e] - mean * cq[b][i][e];
    float hv[8];
#pragma unroll
    for (int i = 0; i < 8; ++i) hv[i] = fmaxf(v[i], 0.f) * v[8 + i] * r2;
    const u32x4 o = {pk(hv[0], hv[1]), pk(hv[2], hv[3]), pk(hv[4], hv[5]), pk(hv[6], hv[7])};
    st16c(o, rs_ws, (unsigned)lane * 16u + (NJ * 3 + b < nu ? 0u : 0x40000000u), WS_H + (unsigned)(sb * NU + u0 + NJ * 3 + b) * 1024u);
  }
  wait_vm<3>();
  return early;
}

template <bool FULL, class F1, class F3, class F2>
DI void attn_pair(int j0, int qb, int lane, F1 side1, F3 side2, F2 head_done, u32x4 (&o)[2]) {
  const int q = lane & 31, hh = lane >> 5, tq = q - 4 * hh;
  const float NEG = -__builtin_inff();
  bf16x8 qf[2];
#pragma unroll
  for (int hd = 0; hd < 2; ++hd) qf[hd] = lds_b128(LA_Q + (unsigned)(hd * 4 + qb) * 1024u + lane * 16u);
  unsigned kb = LA_K + (unsigned)qb * 1024u + lane * 16u;
  auto ldk = [&](int hd, int j) { return lds_b128(kb + (unsigned)(hd * 12 + j) * 1024u); };
  auto pin = [&]() { asm volatile("" : "+v"(kb)); };
  auto on = [&](int j) { return FULL || j >= j0; };
  f32x16 c[2], s[2];
  bf16x8 kf[2];
#pragma unroll
  for (int i = 0; i < 16; ++i) c[0][i] = 0.f;
  float m0[2], m1[2];
  {
    f32x16 s8[2], s0[2];
#pragma unroll
    for (int hd = 0; hd < 2; ++hd) { s8[hd] = mfma(ldk(hd, 8), qf[hd], c[0]); if (FULL) s0[hd] = mfma(ldk(hd, 0), qf[hd], c[0]); }
    if (on(1)) { kf[0] = ldk(0, 1); kf[1] = ldk(1, 1); }
    SB0();
#pragma unroll
    for (int hd = 0; hd < 2; ++hd) {
      m0[hd] = NEG; m1[hd] = NEG;
#pragma unroll
      for (int i = 0; i < 16; i += 2) {
        const int c0 = (i & 3) + 8 * (i >> 2);
        const float a = c0 <= tq ? s8[hd][i] : (FULL ? s0[hd][i] : NEG), b = c0 + 1 <= tq ? s8[hd][i + 1] : (FULL ? s0[hd][i + 1] : NEG);
        if (i & 2) m1[hd] = fmaxf(fmaxf(m1[hd], a), b); else m0[hd] = fmaxf(fmaxf(m0[hd], a), b);
      }
      asm volatile("" : "+v"(m0[hd]), "+v"(m1[hd]));
      if (hd == 0) { if (on(1)) s[0] = mfma(kf[0], qf[0], c[0]); if (on(2)) kf[0] = ldk(0, 2); }
      SB0();
    }
  }
  static_for<0, 14>([&](auto nc) {
    constexpr int n = decltype(nc)::value, hd = n & 1, j = 1 + (n >> 1), hn = (n + 1) & 1, jn = 1 + ((n + 1) >> 1);
    pin();
    if (n + 1 < 14) { if (on(jn)) s[hn] = mfma(kf[hn], qf[hn], c[0]); if (jn < 7 && on(jn + 1)) kf[hn] = ldk(hn, jn + 1); }
    SB0();
    if (on(j)) {
#pragma unroll
      for (int i = 0; i < 8; i += 2) { m0[hd] = fmaxf(fmaxf(m0[hd], s[hd][i]), s[hd][i + 1]); m1[hd] = fmaxf(fmaxf(m1[hd], s[hd][8 + i]), s[hd][9 + i]); }
      asm volatile("" : "+v"(m0[hd]), "+v"(m1[hd]));
    }
    side1(n);
    SB0();
  });
#pragma unroll
  for (int hd = 0; hd < 2; ++hd) { const float m = xhalf_max(fmaxf(m0[hd], m1[hd]));
#pragma unroll
    for (int i = 0; i < 16; ++i) c[hd][i] = -m; }
  const int grp = (lane >> 4) & 1, q4 = (lane & 15) >> 2, p4 = lane & 3;
  const unsigned vl = (unsigned)((p4 & 1) * 512 + (((4 * hh + q4) ^ (4 * (p4 & 1))) * 16) + (p4 >> 1) * 8);
  unsigned vb[2];
#pragma unroll
  for (int hd = 0; hd < 2; ++hd) vb[hd] = grp ? L_ONES + 128u : LA_V + (unsigned)(hd * 12 + qb) * 1024u + vl;
  bf16x8 vf[2][2];
  auto ldv = [&](int hd, int j) {
#pragma unroll
    for (int u = 0; u < 2; ++u) vf[hd][u] = cat(lds_tr(vb[hd] + j * 1024u + u * 256u), lds_tr(vb[hd] + j * 1024u + u * 256u + 128u));
  };
  f32x16 acc[2];
#pragma unroll
  for (int hd = 0; hd < 2; ++hd)
#pragma unroll
    for (int i = 0; i < 16; ++i) acc[hd][i] = 0.f;
  auto pv = [&](int hd, const unsigned (&pp)[8]) {
#pragma unroll
    for (int u = 0; u < 2; ++u) acc[hd] = mfma(vf[hd][u], __builtin_bit_cast(bf16x8, u32x4{pp[4 * u], pp[4 * u + 1], pp[4 * u + 2], pp[4 * u + 3]}), acc[hd]);
  };
  if (on(1)) { kf[0] = ldk(0, 1); kf[1] = ldk(1, 1); s[0] = mfma(kf[0], qf[0], c[0]); ldv(0, 1); }
  if (on(2)) kf[0] = ldk(0, 2);
  static_for<0, 14>([&](auto nc) {
    constexpr int n = decltype(nc)::value, hd = n & 1, j = 1 + (n >> 1), hn = (n + 1) & 1, jn = 1 + ((n + 1) >> 1);
    pin();
    if (n + 1 < 14) { if (on(jn)) { s[hn] = mfma(kf[hn], qf[hn], c[hn]); ldv(hn, jn); } if (jn < 7 && on(jn + 1)) kf[hn] = ldk(hn, jn + 1); }
    SB0();
    if (on(j)) {
      unsigned pp[8];
#pragma unroll
      for (int i = 0; i < 8; ++i) pp[i] = pk(__builtin_amdgcn_exp2f(s[hd][2 * i]), __builtin_amdgcn_exp2f(s[hd][2 * i + 1]));
      pv(hd, pp);
    }
    side2(n);
    SB0();
  });
  pin();
  f32x16 s8[2], s0[2];
#pragma unroll
  for (int hd = 0; hd < 2; ++hd) { s8[hd] = mfma(ldk(hd, 8), qf[hd], c[hd]); if (FULL) s0[hd] = mfma(ldk(hd, 0), qf[hd], c[hd]); }
#pragma unroll
  for (int hd = 0; hd < 2; ++hd) {
    ldv(hd, 8);
    unsigned p8[8], p0[8];
#pragma unroll
    for (int i = 0; i < 8; ++i) {
      const int c0 = ((2 * i) & 3) + 8 * (i >> 1);
      const bool va = c0 <= tq, vb_ = c0 + 1 <= tq;
      const float a = va ? s8[hd][2 * i] : (FULL ? s0[hd][2 * i] : NEG), b = vb_ ? s8[hd][2 * i + 1] : (FULL ? s0[hd][2 * i + 1] : NEG);
      const unsigned pp = pk(__builtin_amdgcn_exp2f(a), __builtin_amdgcn_exp2f(b));
      const unsigned mk = (va ? 0xffffu : 0u) | (vb_ ? 0xffff0000u : 0u);
      p8[i] = pp & mk; p0[i] = pp & ~mk;
    }
    pv(hd, p8);
    if (FULL) { ldv(hd, 0); pv(hd, p0); }
    const float inv = __builtin_amdgcn_rcpf(acc[hd][8]);
    o[hd] = u32x4{pk(acc[hd][0] * inv, acc[hd][1] * inv), pk(acc[hd][2] * inv, acc[hd][3] * inv), pk(acc[hd][4] * inv, acc[hd][5] * inv), pk(acc[hd][6] * inv, acc[hd][7] * inv)};
    head_done(hd);
  }
}

constexpr int DW = 10, DA = 6, NR = 12, NK3 = 30, NDN = 22, OCHK = NDN - (DA - 1), OPOLL = OCHK - 6, XLD = 18;
struct Sched3 {
  int park[NR] = {}, before[NK3 + 1] = {}, lastW[NK3] = {}, lastA[NK3] = {};
  constexpr Sched3() {
    int c = 0;
    for (int i = 0; i < DW - 1; ++i) { c += 2; lastW[i] = c; }
    c += 1;
    for (int i = 0; i < NR; ++i) { park[i] = c - lastW[i]; c += 2; lastW[i + DW - 1] = c; }
    c += 2; lastW[NR + DW - 1] = c;
    for (int i = 0; i < DA - 1; ++i) { c += 2; lastA[i] = c; }
    for (int i = 0; i < NK3; ++i) {
      before[i] = c;
      if (i > NR && i + DW - 1 < NK3) { c += 2; lastW[i + DW - 1] = c; }
      if (i + DA - 1 < NK3) { c += 2; lastA[i + DA - 1] = c; }
      if (i == XLD) c += 4;
    }
    before[NK3] = c;
  }
  constexpr int wait(int i) const { const int w = i + 1 < NK3 ? lastW[i + 1] : 0; return before[i] - (lastA[i] > w ? lastA[i] : w); }
};
DI unsigned l3w(int kq) { return kq < 3 ? 86016u + (unsigned)kq * (DW * 2048u) : 0u; }

DI void phase23(const Ptrs& P, bool early, int rb1, int g1, u64 epoch, int rb, int hp, int rb64, int cb, int rot, int w, int lane, Tag tg) {
  const rsrc_t rs_ws = mk_rsrc(P.ws, WS_BYTES);
  for (unsigned i = threadIdx.x; i < ONES_BYTES / 16; i += 512) *(LAS u32x4*)(lds + L_ONES + i * 16) = u32x4{ONES2, ONES2, ONES2, ONES2};
  if (!early) {
    bool act; const unsigned qv = qkv_flag_voff(rb, hp, lane, act);
    if (w == 0) flags_spin(rs_ws, qv, act, tg);
    bar();
    const AttnSrc asrc = attn_src(rb, hp, w);
#pragma unroll
    for (int i = 0; i < 7; ++i) attn_issue(rs_ws, asrc, w, lane, i, true);
    wait_vm<0>();
  }
  bar();
  const unsigned fh_off = WS_FH + (unsigned)((rb1 * 16 + g1) * 64 + w * 8);
  const int h = lane >> 5, nsub = (lane >> 4) & 1, q4 = (lane & 15) >> 2, p4 = lane & 3;
  f32x16 acc[4];
  u32x2 xr[4];
  const int kq = w - 4;
  const bool keeper = blockIdx.x == 0 && w == 4;
  u32x4 fe[4];
  const int ulo = kq < 3 ? 22 * kq : 65, nun = kq < 2 ? 22 : 21;
  const unsigned ring = __builtin_amdgcn_readfirstlane(l3w(kq));
  if (w < 4) {
    __builtin_amdgcn_s_setprio(3);
    const int sb = 4 * rb + w;
    const unsigned o_off = WS_O + (unsigned)(sb * 32 + 2 * hp) * 1024u, fo_off = WS_FO + (unsigned)((rb * 16 + hp) * 64 + w * 8);
    auto side1 = [&](int) {};
    auto side_h = [&](int n) { if (n == 2) { wait_vm<0>(); flag_store(rs_ws, fh_off, tg, lane); } };
    u32x4 o[2];
    auto store_o = [&](int hd) { st16c(o[hd], rs_ws, (unsigned)lane * 16u, o_off + (unsigned)hd * 1024u); };
    if (sb >= 8) attn_pair<true>(0, w, lane, side1, side_h, store_o, o); else attn_pair<false>(8 - sb, w, lane, side1, side_h, store_o, o);
    wait_vm<0>();
    flag_store(rs_ws, fo_off, tg, lane); flag_store(rs_ws, fo_off + 32u, tg, lane);
  } else {
    const rsrc_t rs_d = mk_rsrc(P.wd, FF * DM * 2), rs_o = mk_rsrc(P.wo, DM * DM * 2);
    unsigned vw[2];
#pragma unroll
    for (int j = 0; j < 2; ++j) vw[j] = (unsigned)(8 * j + (lane >> 3)) * 1024u + (unsigned)(((lane & 7) ^ (((lane >> 4) & 1) << 2)) * 16);
    const unsigned vo = (unsigned)lane * 16u;
    auto unit = [&](int i) { const int r = i + rot; return i < NDN ? (i < nun ? ulo + (r >= nun ? r - nun : r) : -1) : 8 * kq + ((i - NDN + rot) & 7); };
    auto reqW = [&](int i) {
      const int u = unit(i);
#pragma unroll
      for (int j = 0; j < 2; ++j) dma16(ring + (unsigned)(i % DW) * 2048u + j * 1024u, u < 0 ? 0x40000000u : vw[j], i < NDN ? rs_d : rs_o, (unsigned)cb * 128u + (unsigned)(u < 0 ? 0 : u) * 16384u);
    };
    const unsigned hv = WS_FH + (unsigned)((rb64 >> 1) * 1024 + lane * 16), ov = WS_FO + (unsigned)((rb64 >> 1) * 1024 + lane * 16);
    const int pg = lane >> 2;
    const bool pair_ok = ((lane & 3) & 1) == (rb64 & 1);
    const bool h_act = pair_ok && (((pg + 1) * NU) >> 4) > ulo && ((pg * NU) >> 4) < ulo + nun, o_act = pair_ok && (pg >> 2) == kq;
    unsigned wa[2];
#pragma unroll
    for (int nb = 0; nb < 2; ++nb) wa[nb] = ring + (unsigned)h * 512u + q4 * 128u + (unsigned)((nb ^ (q4 >> 1)) * 64) + nsub * 32u + p4 * 8u;
    auto rdw = [&](int i, int nb) { const unsigned a = wa[nb] + (unsigned)(i % DW) * 2048u; return cat(lds_tr(a), lds_tr(a + 1024u)); };
    static constexpr Sched3 S3{};
#pragma unroll
    for (int i = 0; i < DW - 1; ++i) reqW(i);
    wait_vm<2 * (DW - 1)>(); flag_store(rs_ws, fh_off, tg, lane);
    bf16x8 wr[NR][2];
    static_for<0, NR>([&](auto ic) {
      constexpr int i = decltype(ic)::value;
      wait_vm<S3.park[i]>();
      wr[i][0] = rdw(i, 0); wr[i][1] = rdw(i, 1);
      reqW(i + DW - 1);
      asm volatile("s_waitcnt lgkmcnt(0)" : "+v"(wr[i][0]), "+v"(wr[i][1]) :: "memory");
    });
    reqW(NR + DW - 1);
    flags_spin(rs_ws, hv, h_act, tg);
    u32x4 A[DA][2];
    auto reqA = [&](int i) {
      const int u = unit(i);
#pragma unroll
      for (int mb = 0; mb < 2; ++mb)
        A[i % DA][mb] = ld16c_asm(rs_ws, u < 0 ? 0x40000000u : vo, i < NDN ? WS_H + (unsigned)((2 * rb64 + mb) * NU + (u < 0 ? 0 : u)) * 1024u : WS_O + (unsigned)((2 * rb64 + mb) * 32 + u) * 1024u);
    };
#pragma unroll
    for (int i = 0; i < DA - 1; ++i) reqA(i);
#pragma unroll
    for (int b = 0; b < 4; ++b)
#pragma unroll
      for (int i = 0; i < 16; ++i) acc[b][i] = 0.f;
    const int row = 64 * rb64 + 32 * (kq >> 1) + (lane & 31), col0 = 64 * cb + 32 * (kq & 1) + 4 * h;
    const unsigned short* xp = (const unsigned short*)P.x + (size_t)row * DM + col0;
    u32x4 fo;
    bf16x8 wf[2][2];
    static_for<0, NK3>([&](auto ic) {
      constexpr int i = decltype(ic)::value;
      wait_vm<S3.wait(i)>();
      tie(A[i % DA][0]); tie(A[i % DA][1]);
      if constexpr (i == OCHK) {
        wait_vm_tie<S3.before[i] - S3.before[OPOLL + 1]>(fo);
        if (!flags_ok(fo, o_act, tg)) flags_spin(rs_ws, ov, o_act, tg);
      }
      const bf16x8 w0 = i < NR ? wr[i < NR ? i : 0][0] : wf[i & 1][0], w1 = i < NR ? wr[i < NR ? i : 0][1] : wf[i & 1][1];
      SB0();
      acc[0] = mfma(w0, __builtin_bit_cast(bf16x8, A[i % DA][0]), acc[0]);
      if constexpr (i + 1 >= NR && i + 1 < NK3) { wf[(i + 1) & 1][0] = rdw(i + 1, 0); wf[(i + 1) & 1][1] = rdw(i + 1, 1); }
      SB0();
      acc[1] = mfma(w1, __builtin_bit_cast(bf16x8, A[i % DA][0]), acc[1]);
      SB0();
      if constexpr (i > NR && i + DW - 1 < NK3) reqW(i + DW - 1);
      acc[2] = mfma(w0, __builtin_bit_cast(bf16x8, A[i % DA][1]), acc[2]);
      SB0();
      acc[3] = mfma(w1, __builtin_bit_cast(bf16x8, A[i % DA][1]), acc[3]);
      SB0();
      if constexpr (i + DA - 1 < NK3) reqA(i + DA - 1);
      if (i == OPOLL) fo = ld16c_asm(rs_ws, ov, 0u);
      if (i == XLD) {
#pragma unroll
        for (int q = 0; q < 4; ++q) xr[q] = *(const GAS u32x2*)(xp + 8 * q);
      }
      if (i == 20 && keeper) {
#pragma unroll
        for (int q = 0; q < 4; ++q) fe[q] = ld16c_asm(rs_ws, WS_FQKV + (unsigned)(q * 64 + lane) * 64u, 0u);
      }
      SB0();
    });
  }
  if (w >= 4) {
#pragma unroll
    for (int b = 0; b < 4; ++b) {
      if (b != kq) {
        const unsigned base = ring + (unsigned)((b - kq - 1 + 4) % 4) * 4096u + lane * 16u;
#pragma unroll
        for (int q = 0; q < 4; ++q) *(LAS f32x4*)(lds + base + q * 1024u) = f32x4{acc[b][4 * q], acc[b][4 * q + 1], acc[b][4 * q + 2], acc[b][4 * q + 3]};
      }
    }
  }
  bar();
  if (w >= 4) {
    f32x16 r = kq == 0 ? acc[0] : (kq == 1 ? acc[1] : (kq == 2 ? acc[2] : acc[3]));
#pragma unroll
    for (int s = 1; s < 4; ++s) {
      const int src = (kq + s) & 3;
      const unsigned base = l3w(src) + (unsigned)((kq - src - 1 + 4) % 4) * 4096u + lane * 16u;
#pragma unroll
      for (int q = 0; q < 4; ++q) { const f32x4 v = lds_f4(base + q * 1024u);
#pragma unroll
        for (int e = 0; e < 4; ++e) r[4 * q + e] += v[e]; }
    }
    const int row = 64 * rb64 + 32 * (kq >> 1) + (lane & 31), col0 = 64 * cb + 32 * (kq & 1);
    unsigned o[8];
#pragma unroll
    for (int q = 0; q < 4; ++q) { o[2 * q] = pk(r[4 * q] + bf_lo(xr[q][0]), r[4 * q + 1] + bf_hi(xr[q][0])); o[2 * q + 1] = pk(r[4 * q + 2] + bf_lo(xr[q][1]), r[4 * q + 3] + bf_hi(xr[q][1])); }
    unsigned short* op = (unsigned short*)P.out + (size_t)row * DM + col0 + 8 * h;
#pragma unroll
    for (int q = 0; q < 4; q += 2) {
      const auto t0 = __builtin_amdgcn_permlane32_swap(o[2 * q], o[2 * q + 2], false, false), t1 = __builtin_amdgcn_permlane32_swap(o[2 * q + 1], o[2 * q + 3], false, false);
      *(GAS u32x4*)(op + 8 * q) = u32x4{t0[0], t1[0], t0[1], t1[1]};
    }
    if (keeper) {
#pragma unroll
      for (int q = 0; q < 4; ++q) { tie(fe[q]); if (!flags_ok(fe[q], true, tg)) flags_spin(rs_ws, WS_FQKV + (unsigned)(q * 64 + lane) * 64u, true, tg); }
      if (lane == 0) __hip_atomic_store((GAS u64*)(P.ws + WS_EPOCH), epoch, __ATOMIC_RELAXED, __HIP_MEMORY_SCOPE_AGENT);
    }
  }
}

__global__ __launch_bounds__(512, 2) void mk_kernel(const void* x, const void* wqkv, const void* wo, const void* wg, const void* wu, const void* wd, void* out, char* ws) {
  u64 pc0 = 0;
  asm volatile("mk_code_begin:\n\ts_getpc_b64 %0" : "=s"(pc0));
  {
    unsigned len;
    asm volatile("s_mov_b32 %0, mk_code_end-mk_code_begin" : "=s"(len));
    const unsigned wv = __builtin_amdgcn_readfirstlane(threadIdx.x >> 6);
    const unsigned per = ((len >> 5) + 1023u) & ~1023u;
    const unsigned o = (blockIdx.x >> 3) * per + wv * 1024u;
    dma16(L_DUMMY + wv * 1024u, (threadIdx.x & 63u) * 16u, mk_rsrc((const void*)(pc0 & ~255ull), len & ~15u), wv * 1024u < per ? o : 0x40000000u);
  }
  const Ptrs P{x, wqkv, wo, wg, wu, wd, out, ws};
  __builtin_amdgcn_s_setprio(1);
  const int b = blockIdx.x, xcd = b & 7, idx = b >> 3;
  const int w = __builtin_amdgcn_readfirstlane(threadIdx.x >> 6), lane = threadIdx.x & 63;
  Tag tg; u64 epoch;
  const int rb = 2 * (idx >> 2) + (xcd >> 2), g = (xcd & 3) * 4 + (idx & 3), rot1 = ((idx >> 2) + 2 * (idx & 3)) & 7;
  bool early;
  Pro pro;
  prologue(P, rb, g, rot1, w, lane, pro);
  if (w < 4) early = phase1<0>(P, pro, rb, g, rot1, w, lane, rb, g, tg, epoch); else early = phase1<1>(P, pro, rb, g, rot1, w - 4, lane, rb, g, tg, epoch);
  const int rot3 = ((idx & 7) * 2 + 5 * (idx >> 3)) % 21;
  phase23(P, early, rb, g, epoch, rb, g, xcd * 4 + (idx >> 3), idx & 7, rot3, w, lane, tg);
  asm volatile("mk_code_end:");
}

}

extern "C" void kernel_launch(void* const* d_in, const int* in_sizes, int n_in, void* d_out, int out_size, void* d_ws, size_t ws_size, hipStream_t stream) {
  const void *x = d_in[0], *wqkv = d_in[1], *wo = d_in[2], *wg = d_in[4], *wu = d_in[5], *wd = d_in[6];
  mk_kernel<<<256, 512, 0, stream>>>(x, wqkv, wo, wg, wu, wd, d_out, (char*)d_ws);
}
```
